```python
import torch

def _softmax(x, axis=-1):
    return torch.softmax(x, dim=axis)
def _gelu(x):
    return torch.nn.functional.gelu(x, approximate="tanh")
def _silu(x):
    return torch.nn.functional.silu(x)
def _sigmoid(x):
    return torch.sigmoid(x)
def _layernorm(x, axis=-1, eps=1e-5):
    m = x.mean(dim=axis, keepdim=True)
    v = x.var(dim=axis, keepdim=True, unbiased=False)
    return (x - m) / torch.sqrt(v + eps)
def _scatter_add(tgt, idx, src, axis=0):
    return tgt.clone().index_add_(axis, idx, src)
def _segment_sum(src, seg, n):
    out = torch.zeros((n,) + src.shape[1:], dtype=src.dtype)
    return out.index_add_(0, seg, src)
def _pad(x, pw):
    flat = tuple(v for pair in reversed(pw) for v in pair)
    return torch.nn.functional.pad(x, flat)

kernel_name = 'gated_mix__ssm_conv_h3__swiglu_ffn__prompt'

def setup_inputs():

    _w = (lambda a: (lambda t: t.to(torch.bfloat16) if t.is_floating_point() else t)(torch.as_tensor(a)))
    def _A_setup():
        B, S, D = 1, 2048, 1024
        ks = 4
        return {
            "q": _w((lambda *s: torch.randn(*s, dtype=torch.bfloat16) * 0.05)(B, S, D)),
            "k": _w((lambda *s: torch.randn(*s, dtype=torch.bfloat16) * 0.4)(B, S, D)),
            "v": _w((lambda *s: torch.randn(*s, dtype=torch.bfloat16) * 0.4)(B, S, D)),
            "shift_k": _w((lambda *s: torch.randn(*s, dtype=torch.bfloat16) * 0.4)(D, ks) * 0.5),
            "ssm_k": _w((lambda *s: torch.randn(*s, dtype=torch.bfloat16) * 0.4)(D, S) * (1.0 / S ** 0.5)),
            "ssm_D": _w((lambda *s: torch.randn(*s, dtype=torch.bfloat16) * 0.4)(D)),
        }
    _A_in = _A_setup()

    def _B_setup():
        B, S, D, F = 1, 2048, 1024, 3584
        return {
            "x": _w((lambda *s: torch.randn(*s, dtype=torch.bfloat16) * 0.05)(B, S, D)),
            "w_gate": _w((lambda *s: torch.randn(*s, dtype=torch.bfloat16) * 0.05)(D, F)),
            "w_up": _w((lambda *s: torch.randn(*s, dtype=torch.bfloat16) * 0.05)(D, F)),
            "w_down": _w((lambda *s: torch.randn(*s, dtype=torch.bfloat16) * 0.05)(F, D)),
        }
    _B_in = _B_setup()

    return {
        "A_q": _A_in["q"],
        "A_k": _A_in["k"],
        "A_v": _A_in["v"],
        "A_shift_k": _A_in["shift_k"],
        "A_ssm_k": _A_in["ssm_k"],
        "A_ssm_D": _A_in["ssm_D"],
        "B_x": _B_in["x"],
        "B_w_gate": _B_in["w_gate"],
        "B_w_up": _B_in["w_up"],
        "B_w_down": _B_in["w_down"],
        "gate_w": _w((lambda *s: torch.randn(*s, dtype=torch.bfloat16) * 0.05)(1024) * 16.0),
    }

def reference(A_q, A_k, A_v, A_shift_k, A_ssm_k, A_ssm_D, B_x, B_w_gate, B_w_up, B_w_down, gate_w):
    def _A_ref(q, k, v, shift_k, ssm_k, ssm_D):
        B, S, D = q.shape
        ks = shift_k.shape[-1]
        k_pad = _pad(k, ((0, 0), (ks - 1, 0), (0, 0)))
        k_shift = torch.zeros_like(k)
        for t in range(ks):
            k_shift = k_shift + k_pad[:, t:t + S, :] * shift_k[:, ks - 1 - t][None, None, :]
        kv = k_shift * v
        L = 2 * S
        kv_pad = _pad(kv, ((0, 0), (0, S), (0, 0)))
        kf_pad = _pad(ssm_k, ((0, 0), (0, S)))
        kv_f = (lambda x, axes=(-2,-1): torch.fft.rfft2(x.float(), dim=axes))(kv_pad[:, None, :, :], axes=(1, 2))
        kf_f = (lambda x, axes=(-2,-1): torch.fft.rfft2(x.float(), dim=axes))(kf_pad[:, None, :], axes=(1, 2))
        kf_f = (lambda x, *d: x.permute(*d))(kf_f, 1, 2, 0)
        y_f = kv_f * kf_f[None, :, :, :]
        y = (lambda x, s, axes=(-2,-1): torch.fft.irfft2(x, s=s, dim=axes).to(torch.bfloat16))(y_f, (1, L), axes=(1, 2))[:, 0, :S, :]
        y = y + kv * ssm_D[None, None, :]
        return q * y

    def _B_ref(x, w_gate, w_up, w_down):
        g = _silu(x @ w_gate)
        u = x @ w_up
        return (g * u) @ w_down

    x = A_q
    g = _sigmoid((x * gate_w).sum(-1, keepdims=True))
    a = _A_ref(x, A_k, A_v, A_shift_k, A_ssm_k, A_ssm_D)
    b = _B_ref(x, B_w_gate, B_w_up, B_w_down)
    return g * a + (1 - g) * b
```

```cpp
#include <hip/hip_runtime.h>
#include <hip/hip_bf16.h>
typedef unsigned short u16;
typedef short bf16x8 __attribute__((ext_vector_type(8)));
typedef float f32x4 __attribute__((ext_vector_type(4)));
constexpr int S=2048, D=1024, F=3584;
__device__ __forceinline__ float b2f(u16 x){ return __uint_as_float(((unsigned)x)<<16); }
__device__ __forceinline__ u16 f2b(float f){ unsigned u=__float_as_uint(f); u += 0x7fffu + ((u>>16)&1u); return (u16)(u>>16); }

__global__ void k_pre(const u16* k, const u16* v, const u16* sk, u16* kvT){
  int d = blockIdx.x*64 + (threadIdx.x&63); int t = blockIdx.y*4 + (threadIdx.x>>6);
  float acc=0.f;
  for(int j=0;j<4;j++){ int tt=t-j; if(tt>=0) acc += b2f(k[(size_t)tt*D+d])*b2f(sk[d*4+j]); }
  kvT[(size_t)d*S+t] = f2b(acc*b2f(v[(size_t)t*D+d]));
}
__global__ void k_gate(const u16* x, const u16* gw, float* g){
  int t = blockIdx.x*4 + (threadIdx.x>>6); int l=threadIdx.x&63; float s=0.f;
  for(int i=l;i<D;i+=64) s += b2f(x[(size_t)t*D+i])*b2f(gw[i]);
  for(int o=32;o;o>>=1) s += __shfl_xor(s,o);
  if(l==0) g[t] = 1.f/(1.f+__expf(-s));
}
__global__ void k_conv(const u16* kvT, const u16* h, const u16* sD, float* yT){
  __shared__ float su[S], sh[S];
  int d=blockIdx.x;
  for(int i=threadIdx.x;i<S;i+=256){ su[i]=b2f(kvT[(size_t)d*S+i]); sh[i]=b2f(h[(size_t)d*S+i]); }
  __syncthreads();
  for(int t=threadIdx.x;t<S;t+=256){ float a=0.f; for(int s=0;s<=t;s++) a += sh[t-s]*su[s]; yT[(size_t)d*S+t]=a+su[t]*b2f(sD[d]); }
}
template<int MODE> __global__ void k_gemm(const u16* A, const u16* B0, const u16* B1, int N, int K, u16* out, const u16* q, const float* yT, const float* g){
  int l=threadIdx.x&63, w=threadIdx.x>>6; int m0=blockIdx.y*64+w*16, n0=blockIdx.x*16;
  f32x4 c0={0,0,0,0}, c1={0,0,0,0};
  for(int k0=0;k0<K;k0+=32){
    bf16x8 a=*(const bf16x8*)(A+(size_t)(m0+(l&15))*K+k0+8*(l>>4));
    bf16x8 b,b1;
    for(int j=0;j<8;j++){ b[j]=B0[(size_t)(k0+8*(l>>4)+j)*N+n0+(l&15)]; if(MODE==0) b1[j]=B1[(size_t)(k0+8*(l>>4)+j)*N+n0+(l&15)]; }
    c0=__builtin_amdgcn_mfma_f32_16x16x32_bf16(a,b,c0,0,0,0);
    if(MODE==0) c1=__builtin_amdgcn_mfma_f32_16x16x32_bf16(a,b1,c1,0,0,0);
  }
  for(int j=0;j<4;j++){ int m=m0+(l>>4)*4+j, n=n0+(l&15);
    if(MODE==0){ float gg=c0[j]; out[(size_t)m*N+n]=f2b(gg/(1.f+__expf(-gg))*c1[j]); }
    else { float gt=g[m]; float a=b2f(q[(size_t)m*D+n])*yT[(size_t)n*S+m]; out[(size_t)m*N+n]=f2b(gt*a+(1.f-gt)*c0[j]); }
  }
}
extern "C" void kernel_launch(void* const* d_in, const int* in_sizes, int n_in, void* d_out, int out_size, void* d_ws, size_t ws_size, hipStream_t stream){
  const u16* q=(const u16*)d_in[0]; const u16* k=(const u16*)d_in[1]; const u16* v=(const u16*)d_in[2];
  const u16* sk=(const u16*)d_in[3]; const u16* h=(const u16*)d_in[4]; const u16* sD=(const u16*)d_in[5];
  const u16* wg=(const u16*)d_in[7]; const u16* wu=(const u16*)d_in[8]; const u16* wd=(const u16*)d_in[9]; const u16* gw=(const u16*)d_in[10];
  char* ws=(char*)d_ws; u16* kvT=(u16*)ws; float* yT=(float*)(ws+(8<<20)); float* g=(float*)(ws+(24<<20)); u16* hh=(u16*)(ws+(32<<20));
  k_pre<<<dim3(D/64,S/4),256,0,stream>>>(k,v,sk,kvT);
  k_gate<<<S/4,256,0,stream>>>(q,gw,g);
  k_conv<<<D,256,0,stream>>>(kvT,h,sD,yT);
  k_gemm<0><<<dim3(F/16,S/64),256,0,stream>>>(q,wg,wu,F,D,hh,nullptr,nullptr,nullptr);
  k_gemm<1><<<dim3(D/16,S/64),256,0,stream>>>(hh,wd,nullptr,D,F,(u16*)d_out,q,yT,g);
}
```

```cpp
#include <hip/hip_runtime.h>
struct Params { const void* in[10]; void* out; void* ws; };
__global__ __launch_bounds__(256, 1) __attribute__((aligned(4096))) void fused_kernel(Params p) {
  __shared__ __attribute__((aligned(16))) char smem[163840];
  const unsigned wid = __builtin_amdgcn_readfirstlane(threadIdx.x >> 6);
  asm volatile(
"s_load_dwordx16 s[8:23], %0, 0\n"
"s_load_dwordx8 s[24:31], %0, 64\n"
"s_mov_b32 s33, %1\n"
"s_mov_b32 s34, %2\n"
"v_mbcnt_lo_u32_b32 v0, -1, 0\n"
"v_mbcnt_hi_u32_b32 v0, -1, v0\n"
"s_getpc_b64 s[88:89]\n"
"s_and_b32 s88, s88, 0xffffff80\n"
"s_bfe_u32 s81, s33, 0x10003\n"
"s_lshl2_add_u32 s81, s81, s34\n"
"s_lshl_b32 s81, s81, 13\n"
"v_lshl_add_u32 v30, v0, 7, s81\n"
"v_min_u32 v30, 0xe500, v30\n"
"global_load_dword a196, v30, s[88:89]\n"
"s_and_b32 s81, s33, 7\n"
"s_lshr_b32 s82, s33, 3\n"
"s_and_b32 s83, s81, 1\n"
"s_lshr_b32 s84, s81, 1\n"
"s_and_b32 s72, s82, 3\n"
"s_lshr_b32 s73, s82, 2\n"
"s_lshl2_add_u32 s72, s83, s72\n"
"s_lshl3_add_u32 s73, s84, s73\n"
"s_and_b32 s84, s84, 1\n"
"s_and_b32 s74, s82, 7\n"
"s_lshl3_add_u32 s74, s84, s74\n"
"s_lshr_b32 s84, s81, 2\n"
"s_lshr_b32 s75, s82, 3\n"
"s_lshl2_add_u32 s75, s84, s75\n"
"s_lshl1_add_u32 s75, s75, s83\n"
"s_lshl_b32 s76, s81, 7\n"
"s_lshl2_add_u32 s76, s82, s76\n"
"s_lshl_b32 s87, s83, 3\n"
"s_lshr_b32 s96, s82, 3\n"
"s_add_u32 s96, s96, s82\n"
"s_mul_i32 s78, s34, 16896\n"
"s_add_u32 s78, s78, 86016\n"
"s_mul_i32 s77, s34, 7168\n"
"v_lshrrev_b32 v24, 3, v0\n"
"v_lshrrev_b32 v25, 4, v0\n"
"v_and_b32 v26, 7, v0\n"
"v_xor_b32 v26, v26, v25\n"
"s_lshl2_add_u32 s81, s72, s34\n"
"s_lshl_b32 s81, s81, 17\n"
"v_lshlrev_b32 v24, 11, v24\n"
"v_add_u32 v24, s81, v24\n"
"v_xor_b32 v27, 4, v26\n"
"v_lshl_add_u32 v26, v26, 4, v24\n"
"v_lshl_add_u32 v27, v27, 4, v24\n"
"v_add_u32 v8, 0x0, v26\n"
"v_add_u32 v9, 0x3c00, v27\n"
"v_add_u32 v10, 0x7800, v26\n"
"v_add_u32 v11, 0xb400, v27\n"
"s_and_b32 s81, s34, 1\n"
"s_mul_i32 s81, s81, 229376\n"
"s_mul_i32 s82, s73, 224\n"
"s_add_u32 s85, s81, s82\n"
"s_movk_i32 s83, 4682\n"
"s_movk_i32 s84, 7168\n"
"v_add_u32 v24, 0, v0\n"
"v_mul_u32_u24 v25, s83, v24\n"
"v_lshrrev_b32 v25, 16, v25\n"
"v_mul_u32_u24 v26, 14, v25\n"
"v_sub_u32 v26, v24, v26\n"
"v_bfe_u32 v27, v25, 2, 2\n"
"v_lshlrev_b32 v27, 3, v27\n"
"v_lshrrev_b32 v28, 4, v25\n"
"v_lshl_add_u32 v27, v28, 2, v27\n"
"v_and_b32 v28, 3, v25\n"
"v_add_u32 v27, v27, v28\n"
"v_mul_u32_u24 v27, s84, v27\n"
"v_lshl_add_u32 v27, v26, 4, v27\n"
"v_add_u32 v27, s85, v27\n"
"v_subrev_u32 v1, 0, v27\n"
"v_add_u32 v24, 64, v0\n"
"v_mul_u32_u24 v25, s83, v24\n"
"v_lshrrev_b32 v25, 16, v25\n"
"v_mul_u32_u24 v26, 14, v25\n"
"v_sub_u32 v26, v24, v26\n"
"v_bfe_u32 v27, v25, 2, 2\n"
"v_lshlrev_b32 v27, 3, v27\n"
"v_lshrrev_b32 v28, 4, v25\n"
"v_lshl_add_u32 v27, v28, 2, v27\n"
"v_and_b32 v28, 3, v25\n"
"v_add_u32 v27, v27, v28\n"
"v_mul_u32_u24 v27, s84, v27\n"
"v_lshl_add_u32 v27, v26, 4, v27\n"
"v_add_u32 v27, s85, v27\n"
"v_subrev_u32 v2, 1024, v27\n"
"v_add_u32 v24, 128, v0\n"
"v_mul_u32_u24 v25, s83, v24\n"
"v_lshrrev_b32 v25, 16, v25\n"
"v_mul_u32_u24 v26, 14, v25\n"
"v_sub_u32 v26, v24, v26\n"
"v_bfe_u32 v27, v25, 2, 2\n"
"v_lshlrev_b32 v27, 3, v27\n"
"v_lshrrev_b32 v28, 4, v25\n"
"v_lshl_add_u32 v27, v28, 2, v27\n"
"v_and_b32 v28, 3, v25\n"
"v_add_u32 v27, v27, v28\n"
"v_mul_u32_u24 v27, s84, v27\n"
"v_lshl_add_u32 v27, v26, 4, v27\n"
"v_add_u32 v27, s85, v27\n"
"v_subrev_u32 v3, 2048, v27\n"
"s_waitcnt lgkmcnt(0)\n"
"s_mov_b32 s36, s8\n"
"s_and_b32 s37, s9, 0xffff\n"
"s_mov_b32 s38, 0x400000\n"
"s_mov_b32 s39, 0x20000\n"
"s_cmp_lt_u32 s34, 2\n"
"s_cselect_b32 s81, s20, s22\n"
"s_cselect_b32 s82, s21, s23\n"
"s_mov_b32 s40, s81\n"
"s_and_b32 s41, s82, 0xffff\n"
"s_mov_b32 s42, 0x700000\n"
"s_mov_b32 s43, 0x20000\n"
"s_add_u32 m0, s77, 0x0\n"
"s_add_u32 s79, s96, 0\n"
"s_and_b32 s79, s79, 3\n"
"s_mul_i32 s79, s79, 0x70000\n"
"buffer_load_dwordx4 v1, s[40:43], s79 offen offset:0 lds\n"
"buffer_load_dwordx4 v2, s[40:43], s79 offen offset:1024 lds\n"
"buffer_load_dwordx4 v3, s[40:43], s79 offen offset:2048 lds\n"
"s_add_u32 m0, s78, 0x0\n"
"s_add_u32 s80, s96, 0\n"
"s_and_b32 s80, s80, 3\n"
"s_lshl_b32 s80, s80, 7\n"
"buffer_load_dwordx4 v8, s[36:39], s80 offen offset:0 lds\n"
"v_add_u32 v24, 192, v0\n"
"v_mul_u32_u24 v25, s83, v24\n"
"v_lshrrev_b32 v25, 16, v25\n"
"v_mul_u32_u24 v26, 14, v25\n"
"v_sub_u32 v26, v24, v26\n"
"v_bfe_u32 v27, v25, 2, 2\n"
"v_lshlrev_b32 v27, 3, v27\n"
"v_lshrrev_b32 v28, 4, v25\n"
"v_lshl_add_u32 v27, v28, 2, v27\n"
"v_and_b32 v28, 3, v25\n"
"v_add_u32 v27, v27, v28\n"
"v_mul_u32_u24 v27, s84, v27\n"
"v_lshl_add_u32 v27, v26, 4, v27\n"
"v_add_u32 v27, s85, v27\n"
"v_subrev_u32 v4, 3072, v27\n"
"buffer_load_dwordx4 v9, s[36:39], s80 offen offset:1024 lds\n"
"v_add_u32 v24, 256, v0\n"
"v_mul_u32_u24 v25, s83, v24\n"
"v_lshrrev_b32 v25, 16, v25\n"
"v_mul_u32_u24 v26, 14, v25\n"
"v_sub_u32 v26, v24, v26\n"
"v_bfe_u32 v27, v25, 2, 2\n"
"v_lshlrev_b32 v27, 3, v27\n"
"v_lshrrev_b32 v28, 4, v25\n"
"v_lshl_add_u32 v27, v28, 2, v27\n"
"v_and_b32 v28, 3, v25\n"
"v_add_u32 v27, v27, v28\n"
"v_mul_u32_u24 v27, s84, v27\n"
"v_lshl_add_u32 v27, v26, 4, v27\n"
"v_add_u32 v27, s85, v27\n"
"v_subrev_u32 v5, 0, v27\n"
"buffer_load_dwordx4 v10, s[36:39], s80 offen offset:2048 lds\n"
"v_add_u32 v24, 320, v0\n"
"v_mul_u32_u24 v25, s83, v24\n"
"v_lshrrev_b32 v25, 16, v25\n"
"v_mul_u32_u24 v26, 14, v25\n"
"v_sub_u32 v26, v24, v26\n"
"v_bfe_u32 v27, v25, 2, 2\n"
"v_lshlrev_b32 v27, 3, v27\n"
"v_lshrrev_b32 v28, 4, v25\n"
"v_lshl_add_u32 v27, v28, 2, v27\n"
"v_and_b32 v28, 3, v25\n"
"v_add_u32 v27, v27, v28\n"
"v_mul_u32_u24 v27, s84, v27\n"
"v_lshl_add_u32 v27, v26, 4, v27\n"
"v_add_u32 v27, s85, v27\n"
"v_subrev_u32 v6, 1024, v27\n"
"buffer_load_dwordx4 v11, s[36:39], s80 offen offset:3072 lds\n"
"v_add_u32 v24, 384, v0\n"
"v_mul_u32_u24 v25, s83, v24\n"
"v_lshrrev_b32 v25, 16, v25\n"
"v_mul_u32_u24 v26, 14, v25\n"
"v_sub_u32 v26, v24, v26\n"
"v_bfe_u32 v27, v25, 2, 2\n"
"v_lshlrev_b32 v27, 3, v27\n"
"v_lshrrev_b32 v28, 4, v25\n"
"v_lshl_add_u32 v27, v28, 2, v27\n"
"v_and_b32 v28, 3, v25\n"
"v_add_u32 v27, v27, v28\n"
"v_mul_u32_u24 v27, s84, v27\n"
"v_lshl_add_u32 v27, v26, 4, v27\n"
"v_add_u32 v27, s85, v27\n"
"v_subrev_u32 v7, 2048, v27\n"
"s_add_u32 m0, s78, 0x1000\n"
"s_or_b32 s80, s80, 0x10000\n"
"buffer_load_dwordx4 v8, s[36:39], s80 offen offset:0 lds\n"
"buffer_load_dwordx4 v9, s[36:39], s80 offen offset:1024 lds\n"
"buffer_load_dwordx4 v10, s[36:39], s80 offen offset:2048 lds\n"
"buffer_load_dwordx4 v11, s[36:39], s80 offen offset:3072 lds\n"
"s_add_u32 m0, s77, 0x0\n"
"s_nop 0\n"
"buffer_load_dwordx4 v4, s[40:43], s79 offen offset:3072 lds\n"
"s_add_u32 m0, s77, 0x1000\n"
"s_nop 0\n"
"buffer_load_dwordx4 v5, s[40:43], s79 offen offset:0 lds\n"
"buffer_load_dwordx4 v6, s[40:43], s79 offen offset:1024 lds\n"
"buffer_load_dwordx4 v7, s[40:43], s79 offen offset:2048 lds\n"
"s_mov_b64 exec, 0\n"
"s_cmp_eq_u32 s34, 2\n"
"s_cbranch_scc1 .Lfk_hopw2\n"
"s_cmp_eq_u32 s34, 3\n"
"s_cbranch_scc1 .Lfk_hopw3\n"
"s_cmp_eq_u32 s34, 1\n"
"s_cbranch_scc1 .Lfk_hopw1\n"
"s_branch .Lfk_hopw0\n"
".Lfk_hopret:\n"
"s_mov_b64 exec, -1\n"
"s_lshl_b32 s81, s76, 1\n"
"s_add_u32 s18, s18, s81\n"
"s_addc_u32 s19, s19, 0\n"
"s_load_dwordx2 s[92:93], s[18:19], 0\n"
"s_mov_b32 s52, s16\n"
"s_and_b32 s53, s17, 0xffff\n"
"s_mov_b32 s54, 0x400000\n"
"s_mov_b32 s55, 0x20000\n"
"s_add_u32 s81, s76, s34\n"
"s_lshl_b32 s83, s81, 12\n"
"s_add_u32 s16, s16, s83\n"
"s_addc_u32 s17, s17, 0\n"
"s_load_dwordx2 s[94:95], s[16:17], 0\n"
"s_mov_b32 s56, s10\n"
"s_and_b32 s57, s11, 0xffff\n"
"s_mov_b32 s58, 0x400000\n"
"s_mov_b32 s59, 0x20000\n"
"s_mov_b32 s60, s12\n"
"s_and_b32 s61, s13, 0xffff\n"
"s_mov_b32 s62, 0x400000\n"
"s_mov_b32 s63, 0x20000\n"
"s_mov_b32 s48, s24\n"
"s_and_b32 s49, s25, 0xffff\n"
"s_mov_b32 s50, 0x700000\n"
"s_mov_b32 s51, 0x20000\n"
"s_mov_b32 s68, s28\n"
"s_and_b32 s69, s29, 0xffff\n"
"s_mov_b32 s70, 0x400000\n"
"s_mov_b32 s71, 0x20000\n"
"s_mov_b32 s64, s30\n"
"s_and_b32 s65, s31, 0xffff\n"
"s_mov_b32 s66, 0x1000000\n"
"s_mov_b32 s67, 0x20000\n"
"s_add_u32 s81, s30, 0x1000000\n"
"s_addc_u32 s82, s31, 0\n"
"s_mov_b32 s44, s81\n"
"s_and_b32 s45, s82, 0xffff\n"
"s_mov_b32 s46, 0xe00000\n"
"s_mov_b32 s47, 0x20000\n"
"v_lshrrev_b32 v24, 2, v0\n"
"v_mul_u32_u24 v24, 224, v24\n"
"v_and_b32 v25, 3, v0\n"
"v_lshl_add_u32 v16, v25, 3, v24\n"
"v_and_b32 v24, 15, v0\n"
"v_lshrrev_b32 v25, 4, v0\n"
"v_bfe_u32 v26, v0, 1, 3\n"
"v_xor_b32 v25, v25, v26\n"
"v_lshlrev_b32 v24, 7, v24\n"
"v_add_u32 v24, s78, v24\n"
"v_lshl_add_u32 v18, v25, 4, v24\n"
"v_xor_b32 v25, 4, v25\n"
"v_lshl_add_u32 v19, v25, 4, v24\n"
"v_lshlrev_b32 v12, 4, v0\n"
"s_waitcnt lgkmcnt(0)\n"
"s_mov_b32 s19, s83\n"
"s_and_b32 s20, s33, 1\n"
"s_bitcmp1_b32 s34, 1\n"
"s_cselect_b32 s93, s93, s92\n"
"s_and_b32 s81, s34, 1\n"
"s_lshl_b32 s81, s81, 4\n"
"s_lshr_b32 s93, s93, s81\n"
"s_and_b32 s93, s93, 0xffff\n"
"s_mov_b32 s81, 0x7ff00000\n"
"s_cmp_eq_u32 s33, 0\n"
"s_cselect_b32 s28, 0, s81\n"
"s_cmp_eq_u32 s34, 0\n"
"s_cselect_b32 s29, s28, s81\n"
"s_lshl_b32 s82, s33, 2\n"
"s_add_u32 s82, s82, 16384\n"
"s_cmp_eq_u32 s34, 0\n"
"s_cselect_b32 s92, s82, s81\n"
"v_mov_b32 v31, s92\n"
"buffer_load_dword v20, v31, s[64:67], 0 offen offset:2048 sc1\n"
"s_lshl_b32 s82, s34, 12\n"
"s_add_u32 s83, s28, s82\n"
"s_lshl_b32 s82, s34, 8\n"
"s_add_u32 s28, s28, s82\n"
"s_add_u32 s28, s28, 16384\n"
"v_mov_b32 v22, 0\n"
"v_lshl_add_u32 v21, v0, 8, s29\n"
"v_lshl_add_u32 v23, v0, 2, s28\n"
"buffer_store_dword v22, v21, s[64:67], 0 offen sc1\n"
"buffer_store_dword v22, v23, s[64:67], 0 offen sc1\n"
"buffer_store_dword v22, v23, s[64:67], 0 offen offset:3072 sc1\n"
"s_add_u32 s82, s83, 32768\n"
"v_and_b32 v21, 31, v0\n"
"v_lshl_add_u32 v21, v21, 7, s82\n"
"buffer_store_dword v22, v21, s[64:67], 0 offen sc1\n"
"s_waitcnt vmcnt(5)\n"
"s_barrier\n"
"ds_read_b64_tr_b16 a[64:65], v16 offset:0\n"
"ds_read_b64_tr_b16 a[66:67], v16 offset:3584\n"
"ds_read_b64_tr_b16 a[68:69], v16 offset:7168\n"
"ds_read_b64_tr_b16 a[70:71], v16 offset:10752\n"
"ds_read_b128 a[0:3], v18 offset:0\n"
"ds_read_b128 a[16:19], v19 offset:0\n"
"ds_read_b128 a[4:7], v18 offset:2048\n"
"ds_read_b128 a[20:23], v19 offset:2048\n"
"ds_read_b128 a[8:11], v18 offset:4096\n"
"ds_read_b128 a[24:27], v19 offset:4096\n"
"ds_read_b128 a[12:15], v18 offset:6144\n"
"ds_read_b128 a[28:31], v19 offset:6144\n"
"ds_read_b64_tr_b16 a[72:73], v16 offset:14336\n"
"ds_read_b64_tr_b16 a[74:75], v16 offset:17920\n"
"ds_read_b64_tr_b16 a[76:77], v16 offset:21504\n"
"ds_read_b64_tr_b16 a[78:79], v16 offset:25088\n"
"s_waitcnt lgkmcnt(4)\n"
"v_mfma_f32_16x16x32_bf16 v[32:35], a[64:67], a[0:3], 0\n"
"ds_read_b64_tr_b16 a[80:81], v16 offset:32\n"
"s_add_u32 m0, s78, 0x2100\n"
"v_mfma_f32_16x16x32_bf16 v[32:35], a[68:71], a[16:19], v[32:35]\n"
"v_mfma_f32_16x16x32_bf16 v[36:39], a[68:71], a[20:23], 0\n"
"ds_read_b64_tr_b16 a[82:83], v16 offset:3616\n"
"s_add_u32 s80, s96, 1\n"
"v_mfma_f32_16x16x32_bf16 v[36:39], a[64:67], a[4:7], v[36:39]\n"
"ds_read_b64_tr_b16 a[84:85], v16 offset:7200\n"
"s_and_b32 s80, s80, 3\n"
"v_mfma_f32_16x16x32_bf16 v[40:43], a[64:67], a[8:11], 0\n"
"s_lshl_b32 s80, s80, 7\n"
"v_mfma_f32_16x16x32_bf16 v[40:43], a[68:71], a[24:27], v[40:43]\n"
"buffer_load_dwordx4 v8, s[36:39], s80 offen offset:0 lds\n"
"v_mfma_f32_16x16x32_bf16 v[44:47], a[68:71], a[28:31], 0\n"
"ds_read_b64_tr_b16 a[86:87], v16 offset:10784\n"
"v_mfma_f32_16x16x32_bf16 v[44:47], a[64:67], a[12:15], v[44:47]\n"
"ds_read_b64_tr_b16 a[88:89], v16 offset:14368\n"
"s_waitcnt lgkmcnt(5)\n"
"v_mfma_f32_16x16x32_bf16 v[60:63], a[72:75], a[12:15], 0\n"
"ds_read_b64_tr_b16 a[90:91], v16 offset:17952\n"
"v_mfma_f32_16x16x32_bf16 v[60:63], a[76:79], a[28:31], v[60:63]\n"
"buffer_load_dwordx4 v9, s[36:39], s80 offen offset:1024 lds\n"
"v_mfma_f32_16x16x32_bf16 v[56:59], a[76:79], a[24:27], 0\n"
"v_mfma_f32_16x16x32_bf16 v[56:59], a[72:75], a[8:11], v[56:59]\n"
"ds_read_b64_tr_b16 a[92:93], v16 offset:21536\n"
"v_mfma_f32_16x16x32_bf16 v[52:55], a[72:75], a[4:7], 0\n"
"ds_read_b64_tr_b16 a[94:95], v16 offset:25120\n"
"v_mfma_f32_16x16x32_bf16 v[52:55], a[76:79], a[20:23], v[52:55]\n"
"buffer_load_dwordx4 v10, s[36:39], s80 offen offset:2048 lds\n"
"v_mfma_f32_16x16x32_bf16 v[48:51], a[76:79], a[16:19], 0\n"
"ds_read_b64_tr_b16 a[96:97], v16 offset:64\n"
"v_mfma_f32_16x16x32_bf16 v[48:51], a[72:75], a[0:3], v[48:51]\n"
"s_waitcnt lgkmcnt(5)\n"
"v_mfma_f32_16x16x32_bf16 v[64:67], a[80:83], a[0:3], 0\n"
"ds_read_b64_tr_b16 a[98:99], v16 offset:3648\n"
"v_mfma_f32_16x16x32_bf16 v[64:67], a[84:87], a[16:19], v[64:67]\n"
"buffer_load_dwordx4 v11, s[36:39], s80 offen offset:3072 lds\n"
"v_mfma_f32_16x16x32_bf16 v[68:71], a[84:87], a[20:23], 0\n"
"ds_read_b64_tr_b16 a[100:101], v16 offset:7232\n"
"s_add_u32 m0, s78, 0x3100\n"
"v_mfma_f32_16x16x32_bf16 v[68:71], a[80:83], a[4:7], v[68:71]\n"
"ds_read_b64_tr_b16 a[102:103], v16 offset:10816\n"
"s_or_b32 s80, s80, 0x10000\n"
"v_mfma_f32_16x16x32_bf16 v[72:75], a[80:83], a[8:11], 0\n"
"buffer_load_dwordx4 v8, s[36:39], s80 offen offset:0 lds\n"
"v_mfma_f32_16x16x32_bf16 v[72:75], a[84:87], a[24:27], v[72:75]\n"
"v_mfma_f32_16x16x32_bf16 v[76:79], a[84:87], a[28:31], 0\n"
"ds_read_b64_tr_b16 a[64:65], v16 offset:14400\n"
"v_mfma_f32_16x16x32_bf16 v[76:79], a[80:83], a[12:15], v[76:79]\n"
"ds_read_b64_tr_b16 a[66:67], v16 offset:17984\n"
"s_waitcnt lgkmcnt(6)\n"
"v_mfma_f32_16x16x32_bf16 v[92:95], a[88:91], a[12:15], 0\n"
"ds_read_b64_tr_b16 a[68:69], v16 offset:21568\n"
"v_mfma_f32_16x16x32_bf16 v[92:95], a[92:95], a[28:31], v[92:95]\n"
"buffer_load_dwordx4 v9, s[36:39], s80 offen offset:1024 lds\n"
"v_mfma_f32_16x16x32_bf16 v[88:91], a[92:95], a[24:27], 0\n"
"v_mfma_f32_16x16x32_bf16 v[88:91], a[88:91], a[8:11], v[88:91]\n"
"ds_read_b64_tr_b16 a[70:71], v16 offset:25152\n"
"v_mfma_f32_16x16x32_bf16 v[84:87], a[88:91], a[4:7], 0\n"
"ds_read_b64_tr_b16 a[72:73], v16 offset:96\n"
"v_mfma_f32_16x16x32_bf16 v[84:87], a[92:95], a[20:23], v[84:87]\n"
"buffer_load_dwordx4 v10, s[36:39], s80 offen offset:2048 lds\n"
"v_mfma_f32_16x16x32_bf16 v[80:83], a[92:95], a[16:19], 0\n"
"v_mfma_f32_16x16x32_bf16 v[80:83], a[88:91], a[0:3], v[80:83]\n"
"ds_read_b64_tr_b16 a[74:75], v16 offset:3680\n"
"s_waitcnt lgkmcnt(6)\n"
"v_mfma_f32_16x16x32_bf16 v[96:99], a[96:99], a[0:3], 0\n"
"ds_read_b64_tr_b16 a[76:77], v16 offset:7264\n"
"v_mfma_f32_16x16x32_bf16 v[96:99], a[100:103], a[16:19], v[96:99]\n"
"buffer_load_dwordx4 v11, s[36:39], s80 offen offset:3072 lds\n"
"v_mfma_f32_16x16x32_bf16 v[100:103], a[100:103], a[20:23], 0\n"
"ds_read_b64_tr_b16 a[78:79], v16 offset:10848\n"
"s_add_u32 m0, s77, 0x7000\n"
"v_mfma_f32_16x16x32_bf16 v[100:103], a[96:99], a[4:7], v[100:103]\n"
"s_add_u32 s79, s96, 1\n"
"s_and_b32 s79, s79, 3\n"
"v_mfma_f32_16x16x32_bf16 v[104:107], a[96:99], a[8:11], 0\n"
"ds_read_b64_tr_b16 a[80:81], v16 offset:14432\n"
"s_mul_i32 s79, s79, 0x70000\n"
"v_mfma_f32_16x16x32_bf16 v[104:107], a[100:103], a[24:27], v[104:107]\n"
"buffer_load_dwordx4 v1, s[40:43], s79 offen offset:0 lds\n"
"v_mfma_f32_16x16x32_bf16 v[108:111], a[100:103], a[28:31], 0\n"
"ds_read_b64_tr_b16 a[82:83], v16 offset:18016\n"
"v_mfma_f32_16x16x32_bf16 v[108:111], a[96:99], a[12:15], v[108:111]\n"
"ds_read_b64_tr_b16 a[84:85], v16 offset:21600\n"
"s_waitcnt lgkmcnt(7)\n"
"v_mfma_f32_16x16x32_bf16 v[124:127], a[64:67], a[12:15], 0\n"
"buffer_load_dwordx4 v2, s[40:43], s79 offen offset:1024 lds\n"
"v_mfma_f32_16x16x32_bf16 v[124:127], a[68:71], a[28:31], v[124:127]\n"
"v_mfma_f32_16x16x32_bf16 v[120:123], a[68:71], a[24:27], 0\n"
"ds_read_b64_tr_b16 a[86:87], v16 offset:25184\n"
"v_mfma_f32_16x16x32_bf16 v[120:123], a[64:67], a[8:11], v[120:123]\n"
"ds_read_b64_tr_b16 a[88:89], v16 offset:128\n"
"v_mfma_f32_16x16x32_bf16 v[116:119], a[64:67], a[4:7], 0\n"
"ds_read_b64_tr_b16 a[90:91], v16 offset:3712\n"
"v_mfma_f32_16x16x32_bf16 v[116:119], a[68:71], a[20:23], v[116:119]\n"
"buffer_load_dwordx4 v3, s[40:43], s79 offen offset:2048 lds\n"
"v_mfma_f32_16x16x32_bf16 v[112:115], a[68:71], a[16:19], 0\n"
"v_mfma_f32_16x16x32_bf16 v[112:115], a[64:67], a[0:3], v[112:115]\n"
"ds_read_b64_tr_b16 a[92:93], v16 offset:7296\n"
"s_waitcnt lgkmcnt(7)\n"
"v_mfma_f32_16x16x32_bf16 v[128:131], a[72:75], a[0:3], 0\n"
"ds_read_b64_tr_b16 a[94:95], v16 offset:10880\n"
"v_mfma_f32_16x16x32_bf16 v[128:131], a[76:79], a[16:19], v[128:131]\n"
"buffer_load_dwordx4 v4, s[40:43], s79 offen offset:3072 lds\n"
"v_mfma_f32_16x16x32_bf16 v[132:135], a[76:79], a[20:23], 0\n"
"ds_read_b64_tr_b16 a[96:97], v16 offset:14464\n"
"v_mfma_f32_16x16x32_bf16 v[132:135], a[72:75], a[4:7], v[132:135]\n"
"s_add_u32 m0, s77, 0x8000\n"
"v_mfma_f32_16x16x32_bf16 v[136:139], a[72:75], a[8:11], 0\n"
"ds_read_b64_tr_b16 a[98:99], v16 offset:18048\n"
"v_mfma_f32_16x16x32_bf16 v[136:139], a[76:79], a[24:27], v[136:139]\n"
"buffer_load_dwordx4 v5, s[40:43], s79 offen offset:0 lds\n"
"v_mfma_f32_16x16x32_bf16 v[140:143], a[76:79], a[28:31], 0\n"
"ds_read_b64_tr_b16 a[100:101], v16 offset:21632\n"
"v_mfma_f32_16x16x32_bf16 v[140:143], a[72:75], a[12:15], v[140:143]\n"
"ds_read_b64_tr_b16 a[102:103], v16 offset:25216\n"
"s_waitcnt lgkmcnt(8)\n"
"v_mfma_f32_16x16x32_bf16 v[156:159], a[80:83], a[12:15], 0\n"
"buffer_load_dwordx4 v6, s[40:43], s79 offen offset:1024 lds\n"
"v_mfma_f32_16x16x32_bf16 v[156:159], a[84:87], a[28:31], v[156:159]\n"
"v_mfma_f32_16x16x32_bf16 v[152:155], a[84:87], a[24:27], 0\n"
"ds_read_b64_tr_b16 a[64:65], v16 offset:160\n"
"v_mfma_f32_16x16x32_bf16 v[152:155], a[80:83], a[8:11], v[152:155]\n"
"ds_read_b64_tr_b16 a[66:67], v16 offset:3744\n"
"v_mfma_f32_16x16x32_bf16 v[148:151], a[80:83], a[4:7], 0\n"
"buffer_load_dwordx4 v7, s[40:43], s79 offen offset:2048 lds\n"
"v_mfma_f32_16x16x32_bf16 v[148:151], a[84:87], a[20:23], v[148:151]\n"
"v_mfma_f32_16x16x32_bf16 v[144:147], a[84:87], a[16:19], 0\n"
"ds_read_b64_tr_b16 a[68:69], v16 offset:7328\n"
"s_add_u32 m0, s78, 0x0\n"
"v_mfma_f32_16x16x32_bf16 v[144:147], a[80:83], a[0:3], v[144:147]\n"
"ds_read_b64_tr_b16 a[70:71], v16 offset:10912\n"
"s_waitcnt lgkmcnt(8)\n"
"v_mfma_f32_16x16x32_bf16 v[160:163], a[88:91], a[0:3], 0\n"
"ds_read_b64_tr_b16 a[72:73], v16 offset:14496\n"
"s_waitcnt vmcnt(7)\n"
"v_mfma_f32_16x16x32_bf16 v[160:163], a[92:95], a[16:19], v[160:163]\n"
"v_mfma_f32_16x16x32_bf16 v[164:167], a[92:95], a[20:23], 0\n"
"ds_read_b128 a[32:35], v18 offset:8448\n"
"s_add_u32 s80, s96, 2\n"
"v_mfma_f32_16x16x32_bf16 v[164:167], a[88:91], a[4:7], v[164:167]\n"
"ds_read_b64_tr_b16 a[74:75], v16 offset:18080\n"
"s_and_b32 s80, s80, 3\n"
"v_mfma_f32_16x16x32_bf16 v[168:171], a[88:91], a[8:11], 0\n"
"ds_read_b64_tr_b16 a[76:77], v16 offset:21664\n"
"s_lshl_b32 s80, s80, 7\n"
"v_mfma_f32_16x16x32_bf16 v[168:171], a[92:95], a[24:27], v[168:171]\n"
"buffer_load_dwordx4 v8, s[36:39], s80 offen offset:0 lds\n"
"v_mfma_f32_16x16x32_bf16 v[172:175], a[92:95], a[28:31], 0\n"
"ds_read_b64_tr_b16 a[78:79], v16 offset:25248\n"
"v_mfma_f32_16x16x32_bf16 v[172:175], a[88:91], a[12:15], v[172:175]\n"
"ds_read_b128 a[36:39], v18 offset:10496\n"
"s_waitcnt lgkmcnt(10)\n"
"v_mfma_f32_16x16x32_bf16 v[188:191], a[96:99], a[12:15], 0\n"
"ds_read_b64_tr_b16 a[80:81], v16 offset:192\n"
"v_mfma_f32_16x16x32_bf16 v[188:191], a[100:103], a[28:31], v[188:191]\n"
"buffer_load_dwordx4 v9, s[36:39], s80 offen offset:1024 lds\n"
"v_mfma_f32_16x16x32_bf16 v[184:187], a[100:103], a[24:27], 0\n"
"ds_read_b64_tr_b16 a[82:83], v16 offset:3776\n"
"v_mfma_f32_16x16x32_bf16 v[184:187], a[96:99], a[8:11], v[184:187]\n"
"ds_read_b64_tr_b16 a[84:85], v16 offset:7360\n"
"v_mfma_f32_16x16x32_bf16 v[180:183], a[96:99], a[4:7], 0\n"
"ds_read_b128 a[40:43], v18 offset:12544\n"
"v_mfma_f32_16x16x32_bf16 v[180:183], a[100:103], a[20:23], v[180:183]\n"
"buffer_load_dwordx4 v10, s[36:39], s80 offen offset:2048 lds\n"
"v_mfma_f32_16x16x32_bf16 v[176:179], a[100:103], a[16:19], 0\n"
"ds_read_b64_tr_b16 a[86:87], v16 offset:10944\n"
"v_mfma_f32_16x16x32_bf16 v[176:179], a[96:99], a[0:3], v[176:179]\n"
"ds_read_b64_tr_b16 a[88:89], v16 offset:14528\n"
"s_waitcnt lgkmcnt(12)\n"
"v_mfma_f32_16x16x32_bf16 v[192:195], a[64:67], a[0:3], 0\n"
"ds_read_b64_tr_b16 a[90:91], v16 offset:18112\n"
"v_mfma_f32_16x16x32_bf16 v[192:195], a[68:71], a[16:19], v[192:195]\n"
"buffer_load_dwordx4 v11, s[36:39], s80 offen offset:3072 lds\n"
"v_mfma_f32_16x16x32_bf16 v[196:199], a[68:71], a[20:23], 0\n"
"ds_read_b128 a[44:47], v18 offset:14592\n"
"s_add_u32 m0, s78, 0x1000\n"
"v_mfma_f32_16x16x32_bf16 v[196:199], a[64:67], a[4:7], v[196:199]\n"
"ds_read_b64_tr_b16 a[92:93], v16 offset:21696\n"
"s_or_b32 s80, s80, 0x10000\n"
"v_mfma_f32_16x16x32_bf16 v[200:203], a[64:67], a[8:11], 0\n"
"ds_read_b64_tr_b16 a[94:95], v16 offset:25280\n"
"v_mfma_f32_16x16x32_bf16 v[200:203], a[68:71], a[24:27], v[200:203]\n"
"buffer_load_dwordx4 v8, s[36:39], s80 offen offset:0 lds\n"
"v_mfma_f32_16x16x32_bf16 v[204:207], a[68:71], a[28:31], 0\n"
"ds_read_b128 a[48:51], v19 offset:8448\n"
"v_mfma_f32_16x16x32_bf16 v[204:207], a[64:67], a[12:15], v[204:207]\n"
"ds_read_b128 a[52:55], v19 offset:10496\n"
"s_waitcnt lgkmcnt(13)\n"
"v_mfma_f32_16x16x32_bf16 v[220:223], a[72:75], a[12:15], 0\n"
"ds_read_b128 a[56:59], v19 offset:12544\n"
"v_mfma_f32_16x16x32_bf16 v[220:223], a[76:79], a[28:31], v[220:223]\n"
"buffer_load_dwordx4 v9, s[36:39], s80 offen offset:1024 lds\n"
"v_mfma_f32_16x16x32_bf16 v[216:219], a[76:79], a[24:27], 0\n"
"ds_read_b128 a[60:63], v19 offset:14592\n"
"v_mfma_f32_16x16x32_bf16 v[216:219], a[72:75], a[8:11], v[216:219]\n"
"v_mfma_f32_16x16x32_bf16 v[212:215], a[72:75], a[4:7], 0\n"
"buffer_load_dwordx4 v10, s[36:39], s80 offen offset:2048 lds\n"
"v_mfma_f32_16x16x32_bf16 v[212:215], a[76:79], a[20:23], v[212:215]\n"
"v_mfma_f32_16x16x32_bf16 v[208:211], a[76:79], a[16:19], 0\n"
"s_waitcnt lgkmcnt(0)\n"
"v_mfma_f32_16x16x32_bf16 v[208:211], a[72:75], a[0:3], v[208:211]\n"
"s_waitcnt vmcnt(7) lgkmcnt(0)\n"
"s_barrier\n"
"v_mfma_f32_16x16x32_bf16 v[224:227], a[80:83], a[0:3], 0\n"
"ds_read_b64_tr_b16 a[96:97], v16 offset:28672\n"
"v_mfma_f32_16x16x32_bf16 v[224:227], a[84:87], a[16:19], v[224:227]\n"
"buffer_load_dwordx4 v11, s[36:39], s80 offen offset:3072 lds\n"
"v_mfma_f32_16x16x32_bf16 v[228:231], a[84:87], a[20:23], 0\n"
"ds_read_b64_tr_b16 a[98:99], v16 offset:32256\n"
"s_add_u32 m0, s77, 0xe000\n"
"v_mfma_f32_16x16x32_bf16 v[228:231], a[80:83], a[4:7], v[228:231]\n"
"s_add_u32 s79, s96, 2\n"
"s_and_b32 s79, s79, 3\n"
"v_mfma_f32_16x16x32_bf16 v[232:235], a[80:83], a[8:11], 0\n"
"ds_read_b64_tr_b16 a[100:101], v16 offset:35840\n"
"s_mul_i32 s79, s79, 0x70000\n"
"v_mfma_f32_16x16x32_bf16 v[232:235], a[84:87], a[24:27], v[232:235]\n"
"buffer_load_dwordx4 v1, s[40:43], s79 offen offset:0 lds\n"
"v_mfma_f32_16x16x32_bf16 v[236:239], a[84:87], a[28:31], 0\n"
"ds_read_b64_tr_b16 a[102:103], v16 offset:39424\n"
"v_mfma_f32_16x16x32_bf16 v[236:239], a[80:83], a[12:15], v[236:239]\n"
"v_mfma_f32_16x16x32_bf16 v[252:255], a[88:91], a[12:15], 0\n"
"ds_read_b64_tr_b16 a[64:65], v16 offset:43008\n"
"v_mfma_f32_16x16x32_bf16 v[252:255], a[92:95], a[28:31], v[252:255]\n"
"buffer_load_dwordx4 v2, s[40:43], s79 offen offset:1024 lds\n"
"v_mfma_f32_16x16x32_bf16 v[248:251], a[92:95], a[24:27], 0\n"
"ds_read_b64_tr_b16 a[66:67], v16 offset:46592\n"
"v_mfma_f32_16x16x32_bf16 v[248:251], a[88:91], a[8:11], v[248:251]\n"
"v_mfma_f32_16x16x32_bf16 v[244:247], a[88:91], a[4:7], 0\n"
"ds_read_b64_tr_b16 a[68:69], v16 offset:50176\n"
"v_mfma_f32_16x16x32_bf16 v[244:247], a[92:95], a[20:23], v[244:247]\n"
"buffer_load_dwordx4 v3, s[40:43], s79 offen offset:2048 lds\n"
"v_mfma_f32_16x16x32_bf16 v[240:243], a[92:95], a[16:19], 0\n"
"ds_read_b64_tr_b16 a[70:71], v16 offset:53760\n"
"v_mfma_f32_16x16x32_bf16 v[240:243], a[88:91], a[0:3], v[240:243]\n"
"s_waitcnt lgkmcnt(4)\n"
"v_mfma_f32_16x16x32_bf16 v[32:35], a[96:99], a[32:35], v[32:35]\n"
"ds_read_b64_tr_b16 a[72:73], v16 offset:28704\n"
"v_mfma_f32_16x16x32_bf16 v[32:35], a[100:103], a[48:51], v[32:35]\n"
"buffer_load_dwordx4 v4, s[40:43], s79 offen offset:3072 lds\n"
"v_mfma_f32_16x16x32_bf16 v[36:39], a[100:103], a[52:55], v[36:39]\n"
"ds_read_b64_tr_b16 a[74:75], v16 offset:32288\n"
"v_mfma_f32_16x16x32_bf16 v[36:39], a[96:99], a[36:39], v[36:39]\n"
"ds_read_b64_tr_b16 a[76:77], v16 offset:35872\n"
"v_mfma_f32_16x16x32_bf16 v[40:43], a[96:99], a[40:43], v[40:43]\n"
"s_add_u32 m0, s77, 0xf000\n"
"v_mfma_f32_16x16x32_bf16 v[40:43], a[100:103], a[56:59], v[40:43]\n"
"buffer_load_dwordx4 v5, s[40:43], s79 offen offset:0 lds\n"
"v_mfma_f32_16x16x32_bf16 v[44:47], a[100:103], a[60:63], v[44:47]\n"
"ds_read_b64_tr_b16 a[78:79], v16 offset:39456\n"
"v_add_u32 v20, 1, v20\n"
"v_mfma_f32_16x16x32_bf16 v[44:47], a[96:99], a[44:47], v[44:47]\n"
"ds_read_b64_tr_b16 a[80:81], v16 offset:43040\n"
"s_waitcnt lgkmcnt(5)\n"
"v_mfma_f32_16x16x32_bf16 v[60:63], a[64:67], a[44:47], v[60:63]\n"
"ds_read_b64_tr_b16 a[82:83], v16 offset:46624\n"
"v_mfma_f32_16x16x32_bf16 v[60:63], a[68:71], a[60:63], v[60:63]\n"
"buffer_load_dwordx4 v6, s[40:43], s79 offen offset:1024 lds\n"
"v_mfma_f32_16x16x32_bf16 v[56:59], a[68:71], a[56:59], v[56:59]\n"
"v_mov_b32 v17, s92\n"
"v_mfma_f32_16x16x32_bf16 v[56:59], a[64:67], a[40:43], v[56:59]\n"
"ds_read_b64_tr_b16 a[84:85], v16 offset:50208\n"
"v_mfma_f32_16x16x32_bf16 v[52:55], a[64:67], a[36:39], v[52:55]\n"
"ds_read_b64_tr_b16 a[86:87], v16 offset:53792\n"
"v_readfirstlane_b32 s13, v20\n"
"v_mfma_f32_16x16x32_bf16 v[52:55], a[68:71], a[52:55], v[52:55]\n"
"buffer_load_dwordx4 v7, s[40:43], s79 offen offset:2048 lds\n"
"v_mfma_f32_16x16x32_bf16 v[48:51], a[68:71], a[48:51], v[48:51]\n"
"ds_read_b64_tr_b16 a[88:89], v16 offset:28736\n"
"s_add_u32 m0, s78, 0x2100\n"
"v_mfma_f32_16x16x32_bf16 v[48:51], a[64:67], a[32:35], v[48:51]\n"
"s_add_u32 s80, s96, 3\n"
"s_waitcnt lgkmcnt(5)\n"
"v_mfma_f32_16x16x32_bf16 v[64:67], a[72:75], a[32:35], v[64:67]\n"
"ds_read_b64_tr_b16 a[90:91], v16 offset:32320\n"
"s_and_b32 s80, s80, 3\n"
"v_mfma_f32_16x16x32_bf16 v[64:67], a[76:79], a[48:51], v[64:67]\n"
"buffer_store_dword v20, v17, s[64:67], 0 offen offset:1024 sc1\n"
"v_mfma_f32_16x16x32_bf16 v[68:71], a[76:79], a[52:55], v[68:71]\n"
"ds_read_b64_tr_b16 a[92:93], v16 offset:35904\n"
"s_lshl_b32 s80, s80, 7\n"
"v_mfma_f32_16x16x32_bf16 v[68:71], a[72:75], a[36:39], v[68:71]\n"
"ds_read_b64_tr_b16 a[94:95], v16 offset:39488\n"
"v_mfma_f32_16x16x32_bf16 v[72:75], a[72:75], a[40:43], v[72:75]\n"
"buffer_load_dwordx4 v8, s[36:39], s80 offen offset:0 lds\n"
"v_mfma_f32_16x16x32_bf16 v[72:75], a[76:79], a[56:59], v[72:75]\n"
"s_cmp_eq_u32 s34, 0\n"
"s_cselect_b32 s13, s13, 0\n"
"v_mfma_f32_16x16x32_bf16 v[76:79], a[76:79], a[60:63], v[76:79]\n"
"ds_read_b64_tr_b16 a[96:97], v16 offset:43072\n"
"v_mfma_f32_16x16x32_bf16 v[76:79], a[72:75], a[44:47], v[76:79]\n"
"ds_read_b64_tr_b16 a[98:99], v16 offset:46656\n"
"s_waitcnt lgkmcnt(6)\n"
"v_mfma_f32_16x16x32_bf16 v[92:95], a[80:83], a[44:47], v[92:95]\n"
"ds_read_b64_tr_b16 a[100:101], v16 offset:50240\n"
"v_mfma_f32_16x16x32_bf16 v[92:95], a[84:87], a[60:63], v[92:95]\n"
"buffer_load_dwordx4 v9, s[36:39], s80 offen offset:1024 lds\n"
"v_mfma_f32_16x16x32_bf16 v[88:91], a[84:87], a[56:59], v[88:91]\n"
"v_mfma_f32_16x16x32_bf16 v[88:91], a[80:83], a[40:43], v[88:91]\n"
"ds_read_b64_tr_b16 a[102:103], v16 offset:53824\n"
"v_mfma_f32_16x16x32_bf16 v[84:87], a[80:83], a[36:39], v[84:87]\n"
".Lfk_hopw0:\n"
".Lfk_hop625:\n"
"s_cbranch_execz .Lfk_hop1175\n"
"ds_read_b64_tr_b16 a[64:65], v16 offset:28768\n"
"v_mfma_f32_16x16x32_bf16 v[84:87], a[84:87], a[52:55], v[84:87]\n"
"buffer_load_dwordx4 v10, s[36:39], s80 offen offset:2048 lds\n"
"v_mfma_f32_16x16x32_bf16 v[80:83], a[84:87], a[48:51], v[80:83]\n"
"v_mfma_f32_16x16x32_bf16 v[80:83], a[80:83], a[32:35], v[80:83]\n"
"ds_read_b64_tr_b16 a[66:67], v16 offset:32352\n"
"s_waitcnt lgkmcnt(6)\n"
"v_mfma_f32_16x16x32_bf16 v[96:99], a[88:91], a[32:35], v[96:99]\n"
"ds_read_b64_tr_b16 a[68:69], v16 offset:35936\n"
"v_mfma_f32_16x16x32_bf16 v[96:99], a[92:95], a[48:51], v[96:99]\n"
"buffer_load_dwordx4 v11, s[36:39], s80 offen offset:3072 lds\n"
"v_mfma_f32_16x16x32_bf16 v[100:103], a[92:95], a[52:55], v[100:103]\n"
"ds_read_b64_tr_b16 a[70:71], v16 offset:39520\n"
"v_mfma_f32_16x16x32_bf16 v[100:103], a[88:91], a[36:39], v[100:103]\n"
"s_add_u32 m0, s78, 0x3100\n"
"s_or_b32 s80, s80, 0x10000\n"
"v_mfma_f32_16x16x32_bf16 v[104:107], a[88:91], a[40:43], v[104:107]\n"
"ds_read_b64_tr_b16 a[72:73], v16 offset:43104\n"
"v_mfma_f32_16x16x32_bf16 v[104:107], a[92:95], a[56:59], v[104:107]\n"
"buffer_load_dwordx4 v8, s[36:39], s80 offen offset:0 lds\n"
"v_mfma_f32_16x16x32_bf16 v[108:111], a[92:95], a[60:63], v[108:111]\n"
"ds_read_b64_tr_b16 a[74:75], v16 offset:46688\n"
"v_mfma_f32_16x16x32_bf16 v[108:111], a[88:91], a[44:47], v[108:111]\n"
"ds_read_b64_tr_b16 a[76:77], v16 offset:50272\n"
"s_waitcnt lgkmcnt(7)\n"
"v_mfma_f32_16x16x32_bf16 v[124:127], a[96:99], a[44:47], v[124:127]\n"
"v_mfma_f32_16x16x32_bf16 v[124:127], a[100:103], a[60:63], v[124:127]\n"
"buffer_load_dwordx4 v9, s[36:39], s80 offen offset:1024 lds\n"
"v_mfma_f32_16x16x32_bf16 v[120:123], a[100:103], a[56:59], v[120:123]\n"
"ds_read_b64_tr_b16 a[78:79], v16 offset:53856\n"
"v_mfma_f32_16x16x32_bf16 v[120:123], a[96:99], a[40:43], v[120:123]\n"
"ds_read_b64_tr_b16 a[80:81], v16 offset:28800\n"
"v_mfma_f32_16x16x32_bf16 v[116:119], a[96:99], a[36:39], v[116:119]\n"
"ds_read_b64_tr_b16 a[82:83], v16 offset:32384\n"
"v_mfma_f32_16x16x32_bf16 v[116:119], a[100:103], a[52:55], v[116:119]\n"
"buffer_load_dwordx4 v10, s[36:39], s80 offen offset:2048 lds\n"
"v_mfma_f32_16x16x32_bf16 v[112:115], a[100:103], a[48:51], v[112:115]\n"
"v_mfma_f32_16x16x32_bf16 v[112:115], a[96:99], a[32:35], v[112:115]\n"
"ds_read_b64_tr_b16 a[84:85], v16 offset:35968\n"
"s_waitcnt lgkmcnt(7)\n"
"v_mfma_f32_16x16x32_bf16 v[128:131], a[64:67], a[32:35], v[128:131]\n"
"ds_read_b64_tr_b16 a[86:87], v16 offset:39552\n"
"v_mfma_f32_16x16x32_bf16 v[128:131], a[68:71], a[48:51], v[128:131]\n"
"buffer_load_dwordx4 v11, s[36:39], s80 offen offset:3072 lds\n"
"v_mfma_f32_16x16x32_bf16 v[132:135], a[68:71], a[52:55], v[132:135]\n"
"ds_read_b64_tr_b16 a[88:89], v16 offset:43136\n"
"s_waitcnt vmcnt(16)\n"
"v_mfma_f32_16x16x32_bf16 v[132:135], a[64:67], a[36:39], v[132:135]\n"
"ds_read_b128 a[0:3], v18 offset:0\n"
"s_add_u32 m0, s77, 0x0\n"
"v_mfma_f32_16x16x32_bf16 v[136:139], a[64:67], a[40:43], v[136:139]\n"
"ds_read_b64_tr_b16 a[90:91], v16 offset:46720\n"
"s_add_u32 s79, s96, 3\n"
"v_mfma_f32_16x16x32_bf16 v[136:139], a[68:71], a[56:59], v[136:139]\n"
"v_mfma_f32_16x16x32_bf16 v[140:143], a[68:71], a[60:63], v[140:143]\n"
"ds_read_b64_tr_b16 a[92:93], v16 offset:50304\n"
"s_and_b32 s79, s79, 3\n"
"v_mfma_f32_16x16x32_bf16 v[140:143], a[64:67], a[44:47], v[140:143]\n"
"ds_read_b64_tr_b16 a[94:95], v16 offset:53888\n"
"s_waitcnt lgkmcnt(9)\n"
"v_mfma_f32_16x16x32_bf16 v[156:159], a[72:75], a[44:47], v[156:159]\n"
"ds_read_b128 a[4:7], v18 offset:2048\n"
"s_mul_i32 s79, s79, 0x70000\n"
"v_mfma_f32_16x16x32_bf16 v[156:159], a[76:79], a[60:63], v[156:159]\n"
"buffer_load_dwordx4 v1, s[40:43], s79 offen offset:0 lds\n"
"v_mfma_f32_16x16x32_bf16 v[152:155], a[76:79], a[56:59], v[152:155]\n"
"ds_read_b64_tr_b16 a[96:97], v16 offset:28832\n"
"v_mfma_f32_16x16x32_bf16 v[152:155], a[72:75], a[40:43], v[152:155]\n"
"ds_read_b64_tr_b16 a[98:99], v16 offset:32416\n"
"v_mfma_f32_16x16x32_bf16 v[148:151], a[72:75], a[36:39], v[148:151]\n"
"ds_read_b128 a[8:11], v18 offset:4096\n"
"v_mfma_f32_16x16x32_bf16 v[148:151], a[76:79], a[52:55], v[148:151]\n"
"buffer_load_dwordx4 v2, s[40:43], s79 offen offset:1024 lds\n"
"v_mfma_f32_16x16x32_bf16 v[144:147], a[76:79], a[48:51], v[144:147]\n"
"ds_read_b64_tr_b16 a[100:101], v16 offset:36000\n"
"v_mfma_f32_16x16x32_bf16 v[144:147], a[72:75], a[32:35], v[144:147]\n"
"ds_read_b64_tr_b16 a[102:103], v16 offset:39584\n"
"s_waitcnt lgkmcnt(11)\n"
"v_mfma_f32_16x16x32_bf16 v[160:163], a[80:83], a[32:35], v[160:163]\n"
"ds_read_b64_tr_b16 a[64:65], v16 offset:43168\n"
"v_mfma_f32_16x16x32_bf16 v[160:163], a[84:87], a[48:51], v[160:163]\n"
"buffer_load_dwordx4 v3, s[40:43], s79 offen offset:2048 lds\n"
"v_mfma_f32_16x16x32_bf16 v[164:167], a[84:87], a[52:55], v[164:167]\n"
"ds_read_b128 a[12:15], v18 offset:6144\n"
"v_mfma_f32_16x16x32_bf16 v[164:167], a[80:83], a[36:39], v[164:167]\n"
"ds_read_b64_tr_b16 a[66:67], v16 offset:46752\n"
"v_mfma_f32_16x16x32_bf16 v[168:171], a[80:83], a[40:43], v[168:171]\n"
"ds_read_b64_tr_b16 a[68:69], v16 offset:50336\n"
"v_mfma_f32_16x16x32_bf16 v[168:171], a[84:87], a[56:59], v[168:171]\n"
"buffer_load_dwordx4 v4, s[40:43], s79 offen offset:3072 lds\n"
"v_mfma_f32_16x16x32_bf16 v[172:175], a[84:87], a[60:63], v[172:175]\n"
"ds_read_b64_tr_b16 a[70:71], v16 offset:53920\n"
"v_mfma_f32_16x16x32_bf16 v[172:175], a[80:83], a[44:47], v[172:175]\n"
"ds_read_b128 a[16:19], v19 offset:0\n"
"s_waitcnt lgkmcnt(12)\n"
"v_mfma_f32_16x16x32_bf16 v[188:191], a[88:91], a[44:47], v[188:191]\n"
"ds_read_b64_tr_b16 a[72:73], v16 offset:28864\n"
"s_add_u32 m0, s77, 0x1000\n"
"v_mfma_f32_16x16x32_bf16 v[188:191], a[92:95], a[60:63], v[188:191]\n"
"buffer_load_dwordx4 v5, s[40:43], s79 offen offset:0 lds\n"
"v_mfma_f32_16x16x32_bf16 v[184:187], a[92:95], a[56:59], v[184:187]\n"
"ds_read_b64_tr_b16 a[74:75], v16 offset:32448\n"
"v_mfma_f32_16x16x32_bf16 v[184:187], a[88:91], a[40:43], v[184:187]\n"
"ds_read_b64_tr_b16 a[76:77], v16 offset:36032\n"
"v_mfma_f32_16x16x32_bf16 v[180:183], a[88:91], a[36:39], v[180:183]\n"
"ds_read_b128 a[20:23], v19 offset:2048\n"
"v_mfma_f32_16x16x32_bf16 v[180:183], a[92:95], a[52:55], v[180:183]\n"
"buffer_load_dwordx4 v6, s[40:43], s79 offen offset:1024 lds\n"
"v_mfma_f32_16x16x32_bf16 v[176:179], a[92:95], a[48:51], v[176:179]\n"
"ds_read_b64_tr_b16 a[78:79], v16 offset:39616\n"
"v_mfma_f32_16x16x32_bf16 v[176:179], a[88:91], a[32:35], v[176:179]\n"
"ds_read_b64_tr_b16 a[80:81], v16 offset:43200\n"
"s_waitcnt lgkmcnt(12)\n"
"v_mfma_f32_16x16x32_bf16 v[192:195], a[96:99], a[32:35], v[192:195]\n"
"ds_read_b64_tr_b16 a[82:83], v16 offset:46784\n"
"v_mfma_f32_16x16x32_bf16 v[192:195], a[100:103], a[48:51], v[192:195]\n"
"buffer_load_dwordx4 v7, s[40:43], s79 offen offset:2048 lds\n"
"v_mfma_f32_16x16x32_bf16 v[196:199], a[100:103], a[52:55], v[196:199]\n"
"ds_read_b128 a[24:27], v19 offset:4096\n"
"v_mfma_f32_16x16x32_bf16 v[196:199], a[96:99], a[36:39], v[196:199]\n"
"ds_read_b64_tr_b16 a[84:85], v16 offset:50368\n"
"v_mfma_f32_16x16x32_bf16 v[200:203], a[96:99], a[40:43], v[200:203]\n"
"ds_read_b64_tr_b16 a[86:87], v16 offset:53952\n"
"v_mfma_f32_16x16x32_bf16 v[200:203], a[100:103], a[56:59], v[200:203]\n"
"v_mfma_f32_16x16x32_bf16 v[204:207], a[100:103], a[60:63], v[204:207]\n"
"ds_read_b128 a[28:31], v19 offset:6144\n"
"s_add_u32 m0, s78, 0x0\n"
"v_mfma_f32_16x16x32_bf16 v[204:207], a[96:99], a[44:47], v[204:207]\n"
"s_add_u32 s80, s96, 1\n"
"s_waitcnt lgkmcnt(12)\n"
"v_mfma_f32_16x16x32_bf16 v[220:223], a[64:67], a[44:47], v[220:223]\n"
"v_add_u32 v16, 0xe000, v16\n"
"s_and_b32 s80, s80, 3\n"
"v_mfma_f32_16x16x32_bf16 v[220:223], a[68:71], a[60:63], v[220:223]\n"
"v_mfma_f32_16x16x32_bf16 v[216:219], a[68:71], a[56:59], v[216:219]\n"
"s_waitcnt lgkmcnt(0)\n"
"s_add_u32 s80, s80, 4\n"
"v_mfma_f32_16x16x32_bf16 v[216:219], a[64:67], a[40:43], v[216:219]\n"
"s_lshl_b32 s80, s80, 7\n"
"v_mfma_f32_16x16x32_bf16 v[212:215], a[64:67], a[36:39], v[212:215]\n"
"buffer_load_dwordx4 v8, s[36:39], s80 offen offset:0 lds\n"
"v_mfma_f32_16x16x32_bf16 v[212:215], a[68:71], a[52:55], v[212:215]\n"
"v_mfma_f32_16x16x32_bf16 v[208:211], a[68:71], a[48:51], v[208:211]\n"
"v_mfma_f32_16x16x32_bf16 v[208:211], a[64:67], a[32:35], v[208:211]\n"
"s_waitcnt vmcnt(17) lgkmcnt(0)\n"
"s_barrier\n"
"v_mfma_f32_16x16x32_bf16 v[224:227], a[72:75], a[32:35], v[224:227]\n"
"ds_read_b64_tr_b16 a[88:89], v16 offset:0\n"
"v_mfma_f32_16x16x32_bf16 v[224:227], a[76:79], a[48:51], v[224:227]\n"
"buffer_load_dwordx4 v9, s[36:39], s80 offen offset:1024 lds\n"
"v_mfma_f32_16x16x32_bf16 v[228:231], a[76:79], a[52:55], v[228:231]\n"
"ds_read_b64_tr_b16 a[90:91], v16 offset:3584\n"
"v_mfma_f32_16x16x32_bf16 v[228:231], a[72:75], a[36:39], v[228:231]\n"
"v_mfma_f32_16x16x32_bf16 v[232:235], a[72:75], a[40:43], v[232:235]\n"
"ds_read_b64_tr_b16 a[92:93], v16 offset:7168\n"
"v_mfma_f32_16x16x32_bf16 v[232:235], a[76:79], a[56:59], v[232:235]\n"
"buffer_load_dwordx4 v10, s[36:39], s80 offen offset:2048 lds\n"
"v_mfma_f32_16x16x32_bf16 v[236:239], a[76:79], a[60:63], v[236:239]\n"
"ds_read_b64_tr_b16 a[94:95], v16 offset:10752\n"
"v_mfma_f32_16x16x32_bf16 v[236:239], a[72:75], a[44:47], v[236:239]\n"
"v_mfma_f32_16x16x32_bf16 v[252:255], a[80:83], a[44:47], v[252:255]\n"
"ds_read_b64_tr_b16 a[96:97], v16 offset:14336\n"
"v_mfma_f32_16x16x32_bf16 v[252:255], a[84:87], a[60:63], v[252:255]\n"
"buffer_load_dwordx4 v11, s[36:39], s80 offen offset:3072 lds\n"
"v_mfma_f32_16x16x32_bf16 v[248:251], a[84:87], a[56:59], v[248:251]\n"
"ds_read_b64_tr_b16 a[98:99], v16 offset:17920\n"
"v_mfma_f32_16x16x32_bf16 v[248:251], a[80:83], a[40:43], v[248:251]\n"
"s_add_u32 m0, s78, 0x1000\n"
"s_or_b32 s80, s80, 0x10000\n"
"v_mfma_f32_16x16x32_bf16 v[244:247], a[80:83], a[36:39], v[244:247]\n"
"ds_read_b64_tr_b16 a[100:101], v16 offset:21504\n"
"v_mfma_f32_16x16x32_bf16 v[244:247], a[84:87], a[52:55], v[244:247]\n"
"buffer_load_dwordx4 v8, s[36:39], s80 offen offset:0 lds\n"
"v_mfma_f32_16x16x32_bf16 v[240:243], a[84:87], a[48:51], v[240:243]\n"
"ds_read_b64_tr_b16 a[102:103], v16 offset:25088\n"
"v_mfma_f32_16x16x32_bf16 v[240:243], a[80:83], a[32:35], v[240:243]\n"
"s_waitcnt lgkmcnt(4)\n"
"v_mfma_f32_16x16x32_bf16 v[32:35], a[88:91], a[0:3], v[32:35]\n"
"ds_read_b64_tr_b16 a[64:65], v16 offset:32\n"
"v_mfma_f32_16x16x32_bf16 v[32:35], a[92:95], a[16:19], v[32:35]\n"
"buffer_load_dwordx4 v9, s[36:39], s80 offen offset:1024 lds\n"
"v_mfma_f32_16x16x32_bf16 v[36:39], a[92:95], a[20:23], v[36:39]\n"
"ds_read_b64_tr_b16 a[66:67], v16 offset:3616\n"
"v_mfma_f32_16x16x32_bf16 v[36:39], a[88:91], a[4:7], v[36:39]\n"
"ds_read_b64_tr_b16 a[68:69], v16 offset:7200\n"
"v_mfma_f32_16x16x32_bf16 v[40:43], a[88:91], a[8:11], v[40:43]\n"
"v_mfma_f32_16x16x32_bf16 v[40:43], a[92:95], a[24:27], v[40:43]\n"
"buffer_load_dwordx4 v10, s[36:39], s80 offen offset:2048 lds\n"
"v_mfma_f32_16x16x32_bf16 v[44:47], a[92:95], a[28:31], v[44:47]\n"
"ds_read_b64_tr_b16 a[70:71], v16 offset:10784\n"
"v_mfma_f32_16x16x32_bf16 v[44:47], a[88:91], a[12:15], v[44:47]\n"
"ds_read_b64_tr_b16 a[72:73], v16 offset:14368\n"
"s_waitcnt lgkmcnt(5)\n"
"v_mfma_f32_16x16x32_bf16 v[60:63], a[96:99], a[12:15], v[60:63]\n"
"ds_read_b64_tr_b16 a[74:75], v16 offset:17952\n"
"v_mfma_f32_16x16x32_bf16 v[60:63], a[100:103], a[28:31], v[60:63]\n"
"buffer_load_dwordx4 v11, s[36:39], s80 offen offset:3072 lds\n"
"v_mfma_f32_16x16x32_bf16 v[56:59], a[100:103], a[24:27], v[56:59]\n"
"s_add_u32 m0, s77, 0x7000\n"
"s_add_u32 s79, s96, 1\n"
"v_mfma_f32_16x16x32_bf16 v[56:59], a[96:99], a[8:11], v[56:59]\n"
"ds_read_b64_tr_b16 a[76:77], v16 offset:21536\n"
"s_and_b32 s79, s79, 3\n"
"v_mfma_f32_16x16x32_bf16 v[52:55], a[96:99], a[4:7], v[52:55]\n"
"ds_read_b64_tr_b16 a[78:79], v16 offset:25120\n"
"s_add_u32 s79, s79, 4\n"
"v_mfma_f32_16x16x32_bf16 v[52:55], a[100:103], a[20:23], v[52:55]\n"
"v_mfma_f32_16x16x32_bf16 v[48:51], a[100:103], a[16:19], v[48:51]\n"
"ds_read_b64_tr_b16 a[80:81], v16 offset:64\n"
"s_mul_i32 s79, s79, 0x70000\n"
"v_mfma_f32_16x16x32_bf16 v[48:51], a[96:99], a[0:3], v[48:51]\n"
"s_waitcnt lgkmcnt(5)\n"
"v_mfma_f32_16x16x32_bf16 v[64:67], a[64:67], a[0:3], v[64:67]\n"
"ds_read_b64_tr_b16 a[82:83], v16 offset:3648\n"
"v_mfma_f32_16x16x32_bf16 v[64:67], a[68:71], a[16:19], v[64:67]\n"
"buffer_load_dwordx4 v1, s[40:43], s79 offen offset:0 lds\n"
"v_mfma_f32_16x16x32_bf16 v[68:71], a[68:71], a[20:23], v[68:71]\n"
"ds_read_b64_tr_b16 a[84:85], v16 offset:7232\n"
"v_mfma_f32_16x16x32_bf16 v[68:71], a[64:67], a[4:7], v[68:71]\n"
"ds_read_b64_tr_b16 a[86:87], v16 offset:10816\n"
"v_mfma_f32_16x16x32_bf16 v[72:75], a[64:67], a[8:11], v[72:75]\n"
"v_mfma_f32_16x16x32_bf16 v[72:75], a[68:71], a[24:27], v[72:75]\n"
"buffer_load_dwordx4 v2, s[40:43], s79 offen offset:1024 lds\n"
"v_mfma_f32_16x16x32_bf16 v[76:79], a[68:71], a[28:31], v[76:79]\n"
"ds_read_b64_tr_b16 a[88:89], v16 offset:14400\n"
"v_mfma_f32_16x16x32_bf16 v[76:79], a[64:67], a[12:15], v[76:79]\n"
"ds_read_b64_tr_b16 a[90:91], v16 offset:17984\n"
"s_waitcnt lgkmcnt(6)\n"
"v_mfma_f32_16x16x32_bf16 v[92:95], a[72:75], a[12:15], v[92:95]\n"
"ds_read_b64_tr_b16 a[92:93], v16 offset:21568\n"
"s_movk_i32 s8, 0x1000\n"
"v_mfma_f32_16x16x32_bf16 v[92:95], a[76:79], a[28:31], v[92:95]\n"
"buffer_load_dwordx4 v3, s[40:43], s79 offen offset:2048 lds\n"
"v_mfma_f32_16x16x32_bf16 v[88:91], a[76:79], a[24:27], v[88:91]\n"
"s_movk_i32 s9, 0x2000\n"
"v_mfma_f32_16x16x32_bf16 v[88:91], a[72:75], a[8:11], v[88:91]\n"
"ds_read_b64_tr_b16 a[94:95], v16 offset:25152\n"
"v_mfma_f32_16x16x32_bf16 v[84:87], a[72:75], a[4:7], v[84:87]\n"
"ds_read_b64_tr_b16 a[96:97], v16 offset:96\n"
"s_movk_i32 s10, 0x3000\n"
"v_mfma_f32_16x16x32_bf16 v[84:87], a[76:79], a[20:23], v[84:87]\n"
"buffer_load_dwordx4 v4, s[40:43], s79 offen offset:3072 lds\n"
"v_mfma_f32_16x16x32_bf16 v[80:83], a[76:79], a[16:19], v[80:83]\n"
"v_lshl_add_u32 v17, v0, 2, s28\n"
"s_and_b32 s11, s33, 7\n"
"v_mfma_f32_16x16x32_bf16 v[80:83], a[72:75], a[0:3], v[80:83]\n"
"ds_read_b64_tr_b16 a[98:99], v16 offset:3680\n"
"s_waitcnt lgkmcnt(6)\n"
"v_mfma_f32_16x16x32_bf16 v[96:99], a[80:83], a[0:3], v[96:99]\n"
"ds_read_b64_tr_b16 a[100:101], v16 offset:7264\n"
"s_add_u32 m0, s77, 0x8000\n"
"v_mfma_f32_16x16x32_bf16 v[96:99], a[84:87], a[16:19], v[96:99]\n"
"buffer_load_dwordx4 v5, s[40:43], s79 offen offset:0 lds\n"
"v_mfma_f32_16x16x32_bf16 v[100:103], a[84:87], a[20:23], v[100:103]\n"
"ds_read_b64_tr_b16 a[102:103], v16 offset:10848\n"
"s_bfe_u32 s81, s33, 0x10003\n"
"v_mfma_f32_16x16x32_bf16 v[100:103], a[80:83], a[4:7], v[100:103]\n"
"buffer_load_dword a200, v17, s[64:67], 0 offen offset:1024 sc1\n"
"v_mfma_f32_16x16x32_bf16 v[104:107], a[80:83], a[8:11], v[104:107]\n"
"ds_read_b64_tr_b16 a[64:65], v16 offset:14432\n"
"s_waitcnt vmcnt(21)\n"
"v_mfma_f32_16x16x32_bf16 v[104:107], a[84:87], a[24:27], v[104:107]\n"
"buffer_load_dwordx4 v6, s[40:43], s79 offen offset:1024 lds\n"
"v_mfma_f32_16x16x32_bf16 v[108:111], a[84:87], a[28:31], v[108:111]\n"
"ds_read_b64_tr_b16 a[66:67], v16 offset:18016\n"
"s_lshl1_add_u32 s11, s11, s81\n"
"v_mfma_f32_16x16x32_bf16 v[108:111], a[80:83], a[12:15], v[108:111]\n"
"ds_read_b64_tr_b16 a[68:69], v16 offset:21600\n"
"s_waitcnt lgkmcnt(7)\n"
"v_mfma_f32_16x16x32_bf16 v[124:127], a[88:91], a[12:15], v[124:127]\n"
"ds_read_b128 a[32:35], v18 offset:8448\n"
"s_lshr_b32 s12, s33, 4\n"
"v_mfma_f32_16x16x32_bf16 v[124:127], a[92:95], a[28:31], v[124:127]\n"
"buffer_load_dwordx4 v7, s[40:43], s79 offen offset:2048 lds\n"
"v_mfma_f32_16x16x32_bf16 v[120:123], a[92:95], a[24:27], v[120:123]\n"
"ds_read_b64_tr_b16 a[70:71], v16 offset:25184\n"
"s_lshl2_add_u32 s12, s12, s34\n"
"v_mfma_f32_16x16x32_bf16 v[120:123], a[88:91], a[8:11], v[120:123]\n"
"ds_read_b64_tr_b16 a[72:73], v16 offset:128\n"
"s_lshl_b32 s81, s12, 16\n"
"v_mfma_f32_16x16x32_bf16 v[116:119], a[88:91], a[4:7], v[116:119]\n"
"ds_read_b64_tr_b16 a[74:75], v16 offset:3712\n"
"s_lshl_b32 s82, s11, 7\n"
"v_mfma_f32_16x16x32_bf16 v[116:119], a[92:95], a[20:23], v[116:119]\n"
"v_mfma_f32_16x16x32_bf16 v[112:115], a[92:95], a[16:19], v[112:115]\n"
"ds_read_b128 a[36:39], v18 offset:10496\n"
"s_add_u32 s81, s81, s82\n"
"v_mfma_f32_16x16x32_bf16 v[112:115], a[88:91], a[0:3], v[112:115]\n"
"ds_read_b64_tr_b16 a[76:77], v16 offset:7296\n"
"s_waitcnt lgkmcnt(9)\n"
"v_mfma_f32_16x16x32_bf16 v[128:131], a[96:99], a[0:3], v[128:131]\n"
"ds_read_b64_tr_b16 a[78:79], v16 offset:10880\n"
"v_lshrrev_b32 v20, 4, v0\n"
"v_mfma_f32_16x16x32_bf16 v[128:131], a[100:103], a[16:19], v[128:131]\n"
"v_mfma_f32_16x16x32_bf16 v[132:135], a[100:103], a[20:23], v[132:135]\n"
"ds_read_b64_tr_b16 a[80:81], v16 offset:14464\n"
"v_mfma_f32_16x16x32_bf16 v[132:135], a[96:99], a[4:7], v[132:135]\n"
"ds_read_b128 a[40:43], v18 offset:12544\n"
"v_and_b32 v21, 15, v0\n"
"v_mfma_f32_16x16x32_bf16 v[136:139], a[96:99], a[8:11], v[136:139]\n"
"ds_read_b64_tr_b16 a[82:83], v16 offset:18048\n"
"v_mfma_f32_16x16x32_bf16 v[136:139], a[100:103], a[24:27], v[136:139]\n"
"v_lshlrev_b32 v13, 14, v20\n"
"v_mfma_f32_16x16x32_bf16 v[140:143], a[100:103], a[28:31], v[140:143]\n"
"ds_read_b64_tr_b16 a[84:85], v16 offset:21632\n"
"v_mfma_f32_16x16x32_bf16 v[140:143], a[96:99], a[12:15], v[140:143]\n"
"ds_read_b64_tr_b16 a[86:87], v16 offset:25216\n"
"s_waitcnt lgkmcnt(10)\n"
"v_mfma_f32_16x16x32_bf16 v[156:159], a[64:67], a[12:15], v[156:159]\n"
"ds_read_b128 a[44:47], v18 offset:14592\n"
"v_lshl_add_u32 v13, v21, 3, v13\n"
"v_mfma_f32_16x16x32_bf16 v[156:159], a[68:71], a[28:31], v[156:159]\n"
"v_add_u32 v13, s81, v13\n"
"v_mfma_f32_16x16x32_bf16 v[152:155], a[68:71], a[24:27], v[152:155]\n"
"ds_read_b64_tr_b16 a[88:89], v16 offset:160\n"
"v_mfma_f32_16x16x32_bf16 v[152:155], a[64:67], a[8:11], v[152:155]\n"
"ds_read_b64_tr_b16 a[90:91], v16 offset:3744\n"
"v_mfma_f32_16x16x32_bf16 v[148:151], a[64:67], a[4:7], v[148:151]\n"
"ds_read_b128 a[48:51], v19 offset:8448\n"
"v_subrev_u32 v14, 0x1800, v13\n"
"v_mfma_f32_16x16x32_bf16 v[148:151], a[68:71], a[20:23], v[148:151]\n"
"v_mfma_f32_16x16x32_bf16 v[144:147], a[68:71], a[16:19], v[144:147]\n"
"ds_read_b64_tr_b16 a[92:93], v16 offset:7328\n"
"v_max_i32 v14, 0, v14\n"
"v_mfma_f32_16x16x32_bf16 v[144:147], a[64:67], a[0:3], v[144:147]\n"
"ds_read_b64_tr_b16 a[94:95], v16 offset:10912\n"
"s_waitcnt lgkmcnt(11)\n"
"v_mfma_f32_16x16x32_bf16 v[160:163], a[72:75], a[0:3], v[160:163]\n"
"ds_read_b64_tr_b16 a[96:97], v16 offset:14496\n"
"s_lshl_b32 s82, s11, 9\n"
"v_mfma_f32_16x16x32_bf16 v[160:163], a[76:79], a[16:19], v[160:163]\n"
"v_mfma_f32_16x16x32_bf16 v[164:167], a[76:79], a[20:23], v[164:167]\n"
"ds_read_b128 a[52:55], v19 offset:10496\n"
"v_lshl_add_u32 v15, v21, 5, s82\n"
"v_mfma_f32_16x16x32_bf16 v[164:167], a[72:75], a[4:7], v[164:167]\n"
"ds_read_b64_tr_b16 a[98:99], v16 offset:18080\n"
"v_mfma_f32_16x16x32_bf16 v[168:171], a[72:75], a[8:11], v[168:171]\n"
"ds_read_b64_tr_b16 a[100:101], v16 offset:21664\n"
"v_mfma_f32_16x16x32_bf16 v[168:171], a[76:79], a[24:27], v[168:171]\n"
"s_lshl_b32 s81, s34, 11\n"
"v_mfma_f32_16x16x32_bf16 v[172:175], a[76:79], a[28:31], v[172:175]\n"
"ds_read_b64_tr_b16 a[102:103], v16 offset:25248\n"
"v_mfma_f32_16x16x32_bf16 v[172:175], a[72:75], a[12:15], v[172:175]\n"
"ds_read_b128 a[56:59], v19 offset:12544\n"
"s_waitcnt lgkmcnt(12)\n"
"v_mfma_f32_16x16x32_bf16 v[188:191], a[80:83], a[12:15], v[188:191]\n"
"ds_read_b64_tr_b16 a[64:65], v16 offset:192\n"
"s_add_u32 s18, s81, 153600\n"
"v_mfma_f32_16x16x32_bf16 v[188:191], a[84:87], a[28:31], v[188:191]\n"
"v_lshrrev_b32 v20, 2, v0\n"
"v_mfma_f32_16x16x32_bf16 v[184:187], a[84:87], a[24:27], v[184:187]\n"
"ds_read_b64_tr_b16 a[66:67], v16 offset:3776\n"
"v_mfma_f32_16x16x32_bf16 v[184:187], a[80:83], a[8:11], v[184:187]\n"
"ds_read_b64_tr_b16 a[68:69], v16 offset:7360\n"
"v_and_b32 v21, 3, v0\n"
"v_mfma_f32_16x16x32_bf16 v[180:183], a[80:83], a[4:7], v[180:183]\n"
"ds_read_b128 a[60:63], v19 offset:14592\n"
"s_add_u32 m0, s78, 0x2100\n"
"v_mfma_f32_16x16x32_bf16 v[180:183], a[84:87], a[20:23], v[180:183]\n"
"v_mfma_f32_16x16x32_bf16 v[176:179], a[84:87], a[16:19], v[176:179]\n"
"ds_read_b64_tr_b16 a[70:71], v16 offset:10944\n"
"s_add_u32 s80, s96, 2\n"
"v_mfma_f32_16x16x32_bf16 v[176:179], a[80:83], a[0:3], v[176:179]\n"
"ds_read_b64_tr_b16 a[72:73], v16 offset:14528\n"
"s_waitcnt lgkmcnt(12)\n"
"v_mfma_f32_16x16x32_bf16 v[192:195], a[88:91], a[0:3], v[192:195]\n"
"ds_read_b64_tr_b16 a[74:75], v16 offset:18112\n"
"s_waitcnt lgkmcnt(3)\n"
"v_mfma_f32_16x16x32_bf16 v[192:195], a[92:95], a[16:19], v[192:195]\n"
"v_xor_b32 v22, 3, v21\n"
"v_lshlrev_b32 v22, 8, v22\n"
"v_mfma_f32_16x16x32_bf16 v[196:199], a[92:95], a[20:23], v[196:199]\n"
"s_and_b32 s80, s80, 3\n"
"s_add_u32 s80, s80, 4\n"
"v_mfma_f32_16x16x32_bf16 v[196:199], a[88:91], a[4:7], v[196:199]\n"
"ds_read_b64_tr_b16 a[76:77], v16 offset:21696\n"
"s_lshl_b32 s80, s80, 7\n"
"v_mfma_f32_16x16x32_bf16 v[200:203], a[88:91], a[8:11], v[200:203]\n"
"ds_read_b64_tr_b16 a[78:79], v16 offset:25280\n"
"v_lshl_add_u32 v22, v20, 4, v22\n"
"v_mfma_f32_16x16x32_bf16 v[200:203], a[92:95], a[24:27], v[200:203]\n"
"buffer_load_dwordx4 v8, s[36:39], s80 offen offset:0 lds\n"
"v_mfma_f32_16x16x32_bf16 v[204:207], a[92:95], a[28:31], v[204:207]\n"
"v_add_u32 v22, s18, v22\n"
"v_mfma_f32_16x16x32_bf16 v[204:207], a[88:91], a[12:15], v[204:207]\n"
"v_accvgpr_write_b32 a192, v22\n"
"v_mfma_f32_16x16x32_bf16 v[220:223], a[96:99], a[12:15], v[220:223]\n"
"v_subrev_u32 v16, 0xe000, v16\n"
"v_mfma_f32_16x16x32_bf16 v[220:223], a[100:103], a[28:31], v[220:223]\n"
"buffer_load_dwordx4 v9, s[36:39], s80 offen offset:1024 lds\n"
"v_mfma_f32_16x16x32_bf16 v[216:219], a[100:103], a[24:27], v[216:219]\n"
"v_lshlrev_b32 v20, 14, v20\n"
"v_mfma_f32_16x16x32_bf16 v[216:219], a[96:99], a[8:11], v[216:219]\n"
"v_lshl_add_u32 v20, v21, 4, v20\n"
"v_mfma_f32_16x16x32_bf16 v[212:215], a[96:99], a[4:7], v[212:215]\n"
"v_mfma_f32_16x16x32_bf16 v[212:215], a[100:103], a[20:23], v[212:215]\n"
"buffer_load_dwordx4 v10, s[36:39], s80 offen offset:2048 lds\n"
"v_mfma_f32_16x16x32_bf16 v[208:211], a[100:103], a[16:19], v[208:211]\n"
"v_accvgpr_write_b32 a193, v20\n"
"v_mfma_f32_16x16x32_bf16 v[208:211], a[96:99], a[0:3], v[208:211]\n"
"s_waitcnt vmcnt(19) lgkmcnt(0)\n"
"s_barrier\n"
"v_mfma_f32_16x16x32_bf16 v[224:227], a[64:67], a[0:3], v[224:227]\n"
"ds_read_b64_tr_b16 a[80:81], v16 offset:0\n"
"v_mfma_f32_16x16x32_bf16 v[224:227], a[68:71], a[16:19], v[224:227]\n"
"buffer_load_dwordx4 v11, s[36:39], s80 offen offset:3072 lds\n"
"v_mfma_f32_16x16x32_bf16 v[228:231], a[68:71], a[20:23], v[228:231]\n"
"ds_read_b64_tr_b16 a[82:83], v16 offset:3584\n"
"v_mfma_f32_16x16x32_bf16 v[228:231], a[64:67], a[4:7], v[228:231]\n"
"s_add_u32 m0, s78, 0x3100\n"
"s_or_b32 s80, s80, 0x10000\n"
"v_mfma_f32_16x16x32_bf16 v[232:235], a[64:67], a[8:11], v[232:235]\n"
"ds_read_b64_tr_b16 a[84:85], v16 offset:7168\n"
"v_mfma_f32_16x16x32_bf16 v[232:235], a[68:71], a[24:27], v[232:235]\n"
"buffer_load_dwordx4 v8, s[36:39], s80 offen offset:0 lds\n"
"v_mfma_f32_16x16x32_bf16 v[236:239], a[68:71], a[28:31], v[236:239]\n"
"ds_read_b64_tr_b16 a[86:87], v16 offset:10752\n"
"v_mfma_f32_16x16x32_bf16 v[236:239], a[64:67], a[12:15], v[236:239]\n"
"v_mfma_f32_16x16x32_bf16 v[252:255], a[72:75], a[12:15], v[252:255]\n"
"ds_read_b64_tr_b16 a[88:89], v16 offset:14336\n"
"v_mfma_f32_16x16x32_bf16 v[252:255], a[76:79], a[28:31], v[252:255]\n"
"buffer_load_dwordx4 v9, s[36:39], s80 offen offset:1024 lds\n"
"v_mfma_f32_16x16x32_bf16 v[248:251], a[76:79], a[24:27], v[248:251]\n"
"ds_read_b64_tr_b16 a[90:91], v16 offset:17920\n"
"v_mfma_f32_16x16x32_bf16 v[248:251], a[72:75], a[8:11], v[248:251]\n"
"v_mfma_f32_16x16x32_bf16 v[244:247], a[72:75], a[4:7], v[244:247]\n"
"ds_read_b64_tr_b16 a[92:93], v16 offset:21504\n"
"v_mfma_f32_16x16x32_bf16 v[244:247], a[76:79], a[20:23], v[244:247]\n"
"buffer_load_dwordx4 v10, s[36:39], s80 offen offset:2048 lds\n"
"v_mfma_f32_16x16x32_bf16 v[240:243], a[76:79], a[16:19], v[240:243]\n"
"ds_read_b64_tr_b16 a[94:95], v16 offset:25088\n"
"v_mfma_f32_16x16x32_bf16 v[240:243], a[72:75], a[0:3], v[240:243]\n"
"s_waitcnt lgkmcnt(4)\n"
"v_mfma_f32_16x16x32_bf16 v[32:35], a[80:83], a[32:35], v[32:35]\n"
"ds_read_b64_tr_b16 a[96:97], v16 offset:32\n"
"v_mfma_f32_16x16x32_bf16 v[32:35], a[84:87], a[48:51], v[32:35]\n"
"buffer_load_dwordx4 v11, s[36:39], s80 offen offset:3072 lds\n"
"v_mfma_f32_16x16x32_bf16 v[36:39], a[84:87], a[52:55], v[36:39]\n"
"ds_read_b64_tr_b16 a[98:99], v16 offset:3616\n"
"s_add_u32 m0, s77, 0xe000\n"
"v_mfma_f32_16x16x32_bf16 v[36:39], a[80:83], a[36:39], v[36:39]\n"
"ds_read_b64_tr_b16 a[100:101], v16 offset:7200\n"
"s_add_u32 s79, s96, 2\n"
"v_mfma_f32_16x16x32_bf16 v[40:43], a[80:83], a[40:43], v[40:43]\n"
"s_and_b32 s79, s79, 3\n"
"s_add_u32 s79, s79, 4\n"
"v_mfma_f32_16x16x32_bf16 v[40:43], a[84:87], a[56:59], v[40:43]\n"
"v_mfma_f32_16x16x32_bf16 v[44:47], a[84:87], a[60:63], v[44:47]\n"
"ds_read_b64_tr_b16 a[102:103], v16 offset:10784\n"
"s_mul_i32 s79, s79, 0x70000\n"
"v_mfma_f32_16x16x32_bf16 v[44:47], a[80:83], a[44:47], v[44:47]\n"
"ds_read_b64_tr_b16 a[64:65], v16 offset:14368\n"
"s_waitcnt lgkmcnt(5)\n"
"v_mfma_f32_16x16x32_bf16 v[60:63], a[88:91], a[44:47], v[60:63]\n"
"ds_read_b64_tr_b16 a[66:67], v16 offset:17952\n"
"v_mfma_f32_16x16x32_bf16 v[60:63], a[92:95], a[60:63], v[60:63]\n"
"buffer_load_dwordx4 v1, s[40:43], s79 offen offset:0 lds\n"
"v_mfma_f32_16x16x32_bf16 v[56:59], a[92:95], a[56:59], v[56:59]\n"
"v_mfma_f32_16x16x32_bf16 v[56:59], a[88:91], a[40:43], v[56:59]\n"
"ds_read_b64_tr_b16 a[68:69], v16 offset:21536\n"
"v_mfma_f32_16x16x32_bf16 v[52:55], a[88:91], a[36:39], v[52:55]\n"
"ds_read_b64_tr_b16 a[70:71], v16 offset:25120\n"
"s_waitcnt vmcnt(17)\n"
"v_mfma_f32_16x16x32_bf16 v[52:55], a[92:95], a[52:55], v[52:55]\n"
"buffer_load_dwordx4 v2, s[40:43], s79 offen offset:1024 lds\n"
"v_mfma_f32_16x16x32_bf16 v[48:51], a[92:95], a[48:51], v[48:51]\n"
"ds_read_b64_tr_b16 a[72:73], v16 offset:64\n"
"v_mfma_f32_16x16x32_bf16 v[48:51], a[88:91], a[32:35], v[48:51]\n"
"ds_read_b128 a[0:3], v18 offset:0\n"
"s_waitcnt lgkmcnt(6)\n"
"v_mfma_f32_16x16x32_bf16 v[64:67], a[96:99], a[32:35], v[64:67]\n"
"ds_read_b64_tr_b16 a[74:75], v16 offset:3648\n"
"v_mfma_f32_16x16x32_bf16 v[64:67], a[100:103], a[48:51], v[64:67]\n"
"v_mfma_f32_16x16x32_bf16 v[68:71], a[100:103], a[52:55], v[68:71]\n"
"ds_read_b64_tr_b16 a[76:77], v16 offset:7232\n"
"v_mfma_f32_16x16x32_bf16 v[68:71], a[96:99], a[36:39], v[68:71]\n"
"ds_read_b64_tr_b16 a[78:79], v16 offset:10816\n"
"v_mfma_f32_16x16x32_bf16 v[72:75], a[96:99], a[40:43], v[72:75]\n"
"ds_read_b128 a[4:7], v18 offset:2048\n"
"v_mfma_f32_16x16x32_bf16 v[72:75], a[100:103], a[56:59], v[72:75]\n"
"global_load_dwordx4 a[142:145], v15, s[14:15] offset:0\n"
"v_mfma_f32_16x16x32_bf16 v[76:79], a[100:103], a[60:63], v[76:79]\n"
"ds_read_b64_tr_b16 a[80:81], v16 offset:14400\n"
"v_mfma_f32_16x16x32_bf16 v[76:79], a[96:99], a[44:47], v[76:79]\n"
"ds_read_b64_tr_b16 a[82:83], v16 offset:17984\n"
"s_waitcnt lgkmcnt(8)\n"
"v_mfma_f32_16x16x32_bf16 v[92:95], a[64:67], a[44:47], v[92:95]\n"
"ds_read_b64_tr_b16 a[84:85], v16 offset:21568\n"
"v_mfma_f32_16x16x32_bf16 v[92:95], a[68:71], a[60:63], v[92:95]\n"
"global_load_dwordx4 a[146:149], v15, s[14:15] offset:16\n"
"v_mfma_f32_16x16x32_bf16 v[88:91], a[68:71], a[56:59], v[88:91]\n"
"ds_read_b128 a[8:11], v18 offset:4096\n"
"v_mfma_f32_16x16x32_bf16 v[88:91], a[64:67], a[40:43], v[88:91]\n"
"ds_read_b64_tr_b16 a[86:87], v16 offset:25152\n"
"v_mfma_f32_16x16x32_bf16 v[84:87], a[64:67], a[36:39], v[84:87]\n"
"ds_read_b64_tr_b16 a[88:89], v16 offset:96\n"
"v_mfma_f32_16x16x32_bf16 v[84:87], a[68:71], a[52:55], v[84:87]\n"
"buffer_load_dwordx2 a[104:105], v14, s[56:59], 0 offen offset:0 nt\n"
"v_mfma_f32_16x16x32_bf16 v[80:83], a[68:71], a[48:51], v[80:83]\n"
"ds_read_b128 a[12:15], v18 offset:6144\n"
"v_lshl_add_u32 v17, v0, 2, s28\n"
"v_mfma_f32_16x16x32_bf16 v[80:83], a[64:67], a[32:35], v[80:83]\n"
"ds_read_b64_tr_b16 a[90:91], v16 offset:3680\n"
"s_waitcnt lgkmcnt(9)\n"
"v_mfma_f32_16x16x32_bf16 v[96:99], a[72:75], a[32:35], v[96:99]\n"
"ds_read_b64_tr_b16 a[92:93], v16 offset:7264\n"
"s_waitcnt vmcnt(15)\n"
"v_mfma_f32_16x16x32_bf16 v[96:99], a[76:79], a[48:51], v[96:99]\n"
"buffer_store_dword a200, v17, s[64:67], 0 offen offset:2048 sc1\n"
"v_mfma_f32_16x16x32_bf16 v[100:103], a[76:79], a[52:55], v[100:103]\n"
"ds_read_b64_tr_b16 a[94:95], v16 offset:10848\n"
"v_mfma_f32_16x16x32_bf16 v[100:103], a[72:75], a[36:39], v[100:103]\n"
"ds_read_b128 a[16:19], v19 offset:0\n"
"v_mfma_f32_16x16x32_bf16 v[104:107], a[72:75], a[40:43], v[104:107]\n"
"ds_read_b64_tr_b16 a[96:97], v16 offset:14432\n"
"v_mfma_f32_16x16x32_bf16 v[104:107], a[76:79], a[56:59], v[104:107]\n"
"buffer_load_dwordx2 a[106:107], v14, s[56:59], 0 offen offset:2048 nt\n"
"v_mfma_f32_16x16x32_bf16 v[108:111], a[76:79], a[60:63], v[108:111]\n"
"ds_read_b64_tr_b16 a[98:99], v16 offset:18016\n"
"v_mfma_f32_16x16x32_bf16 v[108:111], a[72:75], a[44:47], v[108:111]\n"
"ds_read_b64_tr_b16 a[100:101], v16 offset:21600\n"
"s_waitcnt lgkmcnt(9)\n"
"v_mfma_f32_16x16x32_bf16 v[124:127], a[80:83], a[44:47], v[124:127]\n"
"ds_read_b128 a[20:23], v19 offset:2048\n"
"v_mfma_f32_16x16x32_bf16 v[124:127], a[84:87], a[60:63], v[124:127]\n"
"buffer_load_dwordx2 a[108:109], v14, s[56:59], s8 offen offset:0 nt\n"
"v_mfma_f32_16x16x32_bf16 v[120:123], a[84:87], a[56:59], v[120:123]\n"
"ds_read_b64_tr_b16 a[102:103], v16 offset:25184\n"
"v_mfma_f32_16x16x32_bf16 v[120:123], a[80:83], a[40:43], v[120:123]\n"
"ds_read_b64_tr_b16 a[64:65], v16 offset:128\n"
"v_mfma_f32_16x16x32_bf16 v[116:119], a[80:83], a[36:39], v[116:119]\n"
"ds_read_b64_tr_b16 a[66:67], v16 offset:3712\n"
"v_mfma_f32_16x16x32_bf16 v[116:119], a[84:87], a[52:55], v[116:119]\n"
"buffer_load_dwordx2 a[110:111], v13, s[56:59], 0 offen offset:0 nt\n"
"v_mfma_f32_16x16x32_bf16 v[112:115], a[84:87], a[48:51], v[112:115]\n"
"ds_read_b128 a[24:27], v19 offset:4096\n"
"v_mfma_f32_16x16x32_bf16 v[112:115], a[80:83], a[32:35], v[112:115]\n"
"ds_read_b64_tr_b16 a[68:69], v16 offset:7296\n"
"s_waitcnt lgkmcnt(10)\n"
"v_mfma_f32_16x16x32_bf16 v[128:131], a[88:91], a[32:35], v[128:131]\n"
"ds_read_b64_tr_b16 a[70:71], v16 offset:10880\n"
"v_mfma_f32_16x16x32_bf16 v[128:131], a[92:95], a[48:51], v[128:131]\n"
"buffer_load_dwordx2 a[112:113], v13, s[56:59], 0 offen offset:2048 nt\n"
"v_mfma_f32_16x16x32_bf16 v[132:135], a[92:95], a[52:55], v[132:135]\n"
"ds_read_b64_tr_b16 a[72:73], v16 offset:14464\n"
"v_mfma_f32_16x16x32_bf16 v[132:135], a[88:91], a[36:39], v[132:135]\n"
"ds_read_b128 a[28:31], v19 offset:6144\n"
"v_mfma_f32_16x16x32_bf16 v[136:139], a[88:91], a[40:43], v[136:139]\n"
"ds_read_b64_tr_b16 a[74:75], v16 offset:18048\n"
"v_mfma_f32_16x16x32_bf16 v[136:139], a[92:95], a[56:59], v[136:139]\n"
".Lfk_hop1175:\n"
"s_cbranch_execz .Lfk_hop1719\n"
"buffer_load_dwordx2 a[114:115], v13, s[56:59], s8 offen offset:0 nt\n"
"v_mfma_f32_16x16x32_bf16 v[140:143], a[92:95], a[60:63], v[140:143]\n"
"ds_read_b64_tr_b16 a[76:77], v16 offset:21632\n"
"v_mfma_f32_16x16x32_bf16 v[140:143], a[88:91], a[44:47], v[140:143]\n"
"ds_read_b64_tr_b16 a[78:79], v16 offset:25216\n"
"s_waitcnt lgkmcnt(10)\n"
"v_mfma_f32_16x16x32_bf16 v[156:159], a[96:99], a[44:47], v[156:159]\n"
"s_waitcnt lgkmcnt(3)\n"
"v_mfma_f32_16x16x32_bf16 v[156:159], a[100:103], a[60:63], v[156:159]\n"
"buffer_load_dwordx2 a[116:117], v13, s[56:59], s8 offen offset:2048 nt\n"
"v_mfma_f32_16x16x32_bf16 v[152:155], a[100:103], a[56:59], v[152:155]\n"
"ds_read_b64_tr_b16 a[80:81], v16 offset:160\n"
"v_mfma_f32_16x16x32_bf16 v[152:155], a[96:99], a[40:43], v[152:155]\n"
"ds_read_b64_tr_b16 a[82:83], v16 offset:3744\n"
"v_mfma_f32_16x16x32_bf16 v[148:151], a[96:99], a[36:39], v[148:151]\n"
"buffer_load_dwordx2 a[118:119], v13, s[56:59], s9 offen offset:0 nt\n"
"v_mfma_f32_16x16x32_bf16 v[148:151], a[100:103], a[52:55], v[148:151]\n"
"buffer_load_dwordx2 a[120:121], v13, s[56:59], s9 offen offset:2048 nt\n"
"v_mfma_f32_16x16x32_bf16 v[144:147], a[100:103], a[48:51], v[144:147]\n"
"ds_read_b64_tr_b16 a[84:85], v16 offset:7328\n"
"v_mfma_f32_16x16x32_bf16 v[144:147], a[96:99], a[32:35], v[144:147]\n"
"ds_read_b64_tr_b16 a[86:87], v16 offset:10912\n"
"v_mfma_f32_16x16x32_bf16 v[160:163], a[64:67], a[32:35], v[160:163]\n"
"ds_read_b64_tr_b16 a[88:89], v16 offset:14496\n"
"v_mfma_f32_16x16x32_bf16 v[160:163], a[68:71], a[48:51], v[160:163]\n"
"buffer_load_dwordx2 a[122:123], v13, s[56:59], s10 offen offset:0 nt\n"
"v_mfma_f32_16x16x32_bf16 v[164:167], a[68:71], a[52:55], v[164:167]\n"
"buffer_load_dwordx2 a[124:125], v13, s[56:59], s10 offen offset:2048 nt\n"
"v_mfma_f32_16x16x32_bf16 v[164:167], a[64:67], a[36:39], v[164:167]\n"
"ds_read_b64_tr_b16 a[90:91], v16 offset:18080\n"
"v_mfma_f32_16x16x32_bf16 v[168:171], a[64:67], a[40:43], v[168:171]\n"
"ds_read_b64_tr_b16 a[92:93], v16 offset:21664\n"
"v_mfma_f32_16x16x32_bf16 v[168:171], a[68:71], a[56:59], v[168:171]\n"
"v_mfma_f32_16x16x32_bf16 v[172:175], a[68:71], a[60:63], v[172:175]\n"
"ds_read_b64_tr_b16 a[94:95], v16 offset:25248\n"
"v_mfma_f32_16x16x32_bf16 v[172:175], a[64:67], a[44:47], v[172:175]\n"
"s_waitcnt lgkmcnt(8)\n"
"v_mfma_f32_16x16x32_bf16 v[188:191], a[72:75], a[44:47], v[188:191]\n"
"ds_read_b64_tr_b16 a[96:97], v16 offset:192\n"
"v_mfma_f32_16x16x32_bf16 v[188:191], a[76:79], a[60:63], v[188:191]\n"
"v_mfma_f32_16x16x32_bf16 v[184:187], a[76:79], a[56:59], v[184:187]\n"
"ds_read_b64_tr_b16 a[98:99], v16 offset:3776\n"
"v_mfma_f32_16x16x32_bf16 v[184:187], a[72:75], a[40:43], v[184:187]\n"
"ds_read_b64_tr_b16 a[100:101], v16 offset:7360\n"
"v_mfma_f32_16x16x32_bf16 v[180:183], a[72:75], a[36:39], v[180:183]\n"
"v_mfma_f32_16x16x32_bf16 v[180:183], a[76:79], a[52:55], v[180:183]\n"
"v_mfma_f32_16x16x32_bf16 v[176:179], a[76:79], a[48:51], v[176:179]\n"
"ds_read_b64_tr_b16 a[102:103], v16 offset:10944\n"
"v_mfma_f32_16x16x32_bf16 v[176:179], a[72:75], a[32:35], v[176:179]\n"
"ds_read_b64_tr_b16 a[64:65], v16 offset:14528\n"
"s_waitcnt lgkmcnt(9)\n"
"v_mfma_f32_16x16x32_bf16 v[192:195], a[80:83], a[32:35], v[192:195]\n"
"ds_read_b64_tr_b16 a[66:67], v16 offset:18112\n"
"v_mfma_f32_16x16x32_bf16 v[192:195], a[84:87], a[48:51], v[192:195]\n"
"v_mfma_f32_16x16x32_bf16 v[196:199], a[84:87], a[52:55], v[196:199]\n"
"v_mfma_f32_16x16x32_bf16 v[196:199], a[80:83], a[36:39], v[196:199]\n"
"ds_read_b64_tr_b16 a[68:69], v16 offset:21696\n"
"v_mfma_f32_16x16x32_bf16 v[200:203], a[80:83], a[40:43], v[200:203]\n"
"ds_read_b64_tr_b16 a[70:71], v16 offset:25280\n"
"v_mfma_f32_16x16x32_bf16 v[200:203], a[84:87], a[56:59], v[200:203]\n"
"v_mfma_f32_16x16x32_bf16 v[204:207], a[84:87], a[60:63], v[204:207]\n"
"v_mfma_f32_16x16x32_bf16 v[204:207], a[80:83], a[44:47], v[204:207]\n"
"s_waitcnt lgkmcnt(8)\n"
"v_mfma_f32_16x16x32_bf16 v[220:223], a[88:91], a[44:47], v[220:223]\n"
"v_mfma_f32_16x16x32_bf16 v[220:223], a[92:95], a[60:63], v[220:223]\n"
"v_mfma_f32_16x16x32_bf16 v[216:219], a[92:95], a[56:59], v[216:219]\n"
"v_mfma_f32_16x16x32_bf16 v[216:219], a[88:91], a[40:43], v[216:219]\n"
"v_mfma_f32_16x16x32_bf16 v[212:215], a[88:91], a[36:39], v[212:215]\n"
"v_mfma_f32_16x16x32_bf16 v[212:215], a[92:95], a[52:55], v[212:215]\n"
"v_mfma_f32_16x16x32_bf16 v[208:211], a[92:95], a[48:51], v[208:211]\n"
"v_mfma_f32_16x16x32_bf16 v[208:211], a[88:91], a[32:35], v[208:211]\n"
"s_waitcnt vmcnt(24) lgkmcnt(0)\n"
"s_barrier\n"
"v_mfma_f32_16x16x32_bf16 v[224:227], a[96:99], a[32:35], v[224:227]\n"
"ds_read_b64_tr_b16 a[72:73], v16 offset:28672\n"
"v_mfma_f32_16x16x32_bf16 v[224:227], a[100:103], a[48:51], v[224:227]\n"
"v_mfma_f32_16x16x32_bf16 v[228:231], a[100:103], a[52:55], v[228:231]\n"
"ds_read_b64_tr_b16 a[74:75], v16 offset:32256\n"
"v_mfma_f32_16x16x32_bf16 v[228:231], a[96:99], a[36:39], v[228:231]\n"
"buffer_load_dwordx4 v3, s[40:43], s79 offen offset:2048 lds\n"
"v_mfma_f32_16x16x32_bf16 v[232:235], a[96:99], a[40:43], v[232:235]\n"
"ds_read_b64_tr_b16 a[76:77], v16 offset:35840\n"
"v_mfma_f32_16x16x32_bf16 v[232:235], a[100:103], a[56:59], v[232:235]\n"
"v_mfma_f32_16x16x32_bf16 v[236:239], a[100:103], a[60:63], v[236:239]\n"
"ds_read_b64_tr_b16 a[78:79], v16 offset:39424\n"
"v_mfma_f32_16x16x32_bf16 v[236:239], a[96:99], a[44:47], v[236:239]\n"
"v_mfma_f32_16x16x32_bf16 v[252:255], a[64:67], a[44:47], v[252:255]\n"
"ds_read_b64_tr_b16 a[80:81], v16 offset:43008\n"
"v_mfma_f32_16x16x32_bf16 v[252:255], a[68:71], a[60:63], v[252:255]\n"
"buffer_load_dwordx4 v4, s[40:43], s79 offen offset:3072 lds\n"
"v_mfma_f32_16x16x32_bf16 v[248:251], a[68:71], a[56:59], v[248:251]\n"
"ds_read_b64_tr_b16 a[82:83], v16 offset:46592\n"
"v_mfma_f32_16x16x32_bf16 v[248:251], a[64:67], a[40:43], v[248:251]\n"
"v_mfma_f32_16x16x32_bf16 v[244:247], a[64:67], a[36:39], v[244:247]\n"
"ds_read_b64_tr_b16 a[84:85], v16 offset:50176\n"
"s_add_u32 m0, s77, 0xf000\n"
"v_mfma_f32_16x16x32_bf16 v[244:247], a[68:71], a[52:55], v[244:247]\n"
"buffer_load_dwordx4 v5, s[40:43], s79 offen offset:0 lds\n"
"v_mfma_f32_16x16x32_bf16 v[240:243], a[68:71], a[48:51], v[240:243]\n"
"ds_read_b64_tr_b16 a[86:87], v16 offset:53760\n"
"v_mfma_f32_16x16x32_bf16 v[240:243], a[64:67], a[32:35], v[240:243]\n"
"s_waitcnt lgkmcnt(4)\n"
"v_mfma_f32_16x16x32_bf16 v[32:35], a[72:75], a[0:3], v[32:35]\n"
"ds_read_b64_tr_b16 a[88:89], v16 offset:28704\n"
"v_mfma_f32_16x16x32_bf16 v[32:35], a[76:79], a[16:19], v[32:35]\n"
"buffer_load_dwordx4 v6, s[40:43], s79 offen offset:1024 lds\n"
"v_mfma_f32_16x16x32_bf16 v[36:39], a[76:79], a[20:23], v[36:39]\n"
"ds_read_b64_tr_b16 a[90:91], v16 offset:32288\n"
"v_mfma_f32_16x16x32_bf16 v[36:39], a[72:75], a[4:7], v[36:39]\n"
"ds_read_b64_tr_b16 a[92:93], v16 offset:35872\n"
"v_mfma_f32_16x16x32_bf16 v[40:43], a[72:75], a[8:11], v[40:43]\n"
"v_mfma_f32_16x16x32_bf16 v[40:43], a[76:79], a[24:27], v[40:43]\n"
"buffer_load_dwordx4 v7, s[40:43], s79 offen offset:2048 lds\n"
"v_mfma_f32_16x16x32_bf16 v[44:47], a[76:79], a[28:31], v[44:47]\n"
"ds_read_b64_tr_b16 a[94:95], v16 offset:39456\n"
"s_add_u32 m0, s78, 0x0\n"
"v_mfma_f32_16x16x32_bf16 v[44:47], a[72:75], a[12:15], v[44:47]\n"
"ds_read_b64_tr_b16 a[96:97], v16 offset:43040\n"
"s_waitcnt lgkmcnt(5)\n"
"v_mfma_f32_16x16x32_bf16 v[60:63], a[80:83], a[12:15], v[60:63]\n"
"ds_read_b64_tr_b16 a[98:99], v16 offset:46624\n"
"s_add_u32 s80, s96, 3\n"
"v_mfma_f32_16x16x32_bf16 v[60:63], a[84:87], a[28:31], v[60:63]\n"
"v_mfma_f32_16x16x32_bf16 v[56:59], a[84:87], a[24:27], v[56:59]\n"
"s_and_b32 s80, s80, 3\n"
"s_add_u32 s80, s80, 4\n"
"v_mfma_f32_16x16x32_bf16 v[56:59], a[80:83], a[8:11], v[56:59]\n"
"ds_read_b64_tr_b16 a[100:101], v16 offset:50208\n"
"s_lshl_b32 s80, s80, 7\n"
"v_mfma_f32_16x16x32_bf16 v[52:55], a[80:83], a[4:7], v[52:55]\n"
"ds_read_b64_tr_b16 a[102:103], v16 offset:53792\n"
"s_waitcnt vmcnt(21)\n"
"v_mfma_f32_16x16x32_bf16 v[52:55], a[84:87], a[20:23], v[52:55]\n"
"buffer_load_dwordx4 v8, s[36:39], s80 offen offset:0 lds\n"
"v_mfma_f32_16x16x32_bf16 v[48:51], a[84:87], a[16:19], v[48:51]\n"
"ds_read_b64_tr_b16 a[64:65], v16 offset:28736\n"
"v_mfma_f32_16x16x32_bf16 v[48:51], a[80:83], a[0:3], v[48:51]\n"
"ds_read_b128 a[32:35], v18 offset:8448\n"
"s_waitcnt lgkmcnt(6)\n"
"v_mfma_f32_16x16x32_bf16 v[64:67], a[88:91], a[0:3], v[64:67]\n"
"ds_read_b64_tr_b16 a[66:67], v16 offset:32320\n"
"v_mfma_f32_16x16x32_bf16 v[64:67], a[92:95], a[16:19], v[64:67]\n"
"buffer_load_dwordx4 v9, s[36:39], s80 offen offset:1024 lds\n"
"v_mfma_f32_16x16x32_bf16 v[68:71], a[92:95], a[20:23], v[68:71]\n"
"ds_read_b64_tr_b16 a[68:69], v16 offset:35904\n"
"v_mfma_f32_16x16x32_bf16 v[68:71], a[88:91], a[4:7], v[68:71]\n"
"ds_read_b64_tr_b16 a[70:71], v16 offset:39488\n"
"v_mfma_f32_16x16x32_bf16 v[72:75], a[88:91], a[8:11], v[72:75]\n"
"ds_read_b128 a[36:39], v18 offset:10496\n"
"v_mfma_f32_16x16x32_bf16 v[72:75], a[92:95], a[24:27], v[72:75]\n"
"buffer_load_dwordx4 v10, s[36:39], s80 offen offset:2048 lds\n"
"v_mfma_f32_16x16x32_bf16 v[76:79], a[92:95], a[28:31], v[76:79]\n"
"ds_read_b64_tr_b16 a[72:73], v16 offset:43072\n"
"v_mfma_f32_16x16x32_bf16 v[76:79], a[88:91], a[12:15], v[76:79]\n"
"ds_read_b64_tr_b16 a[74:75], v16 offset:46656\n"
"s_waitcnt lgkmcnt(8)\n"
"v_mfma_f32_16x16x32_bf16 v[92:95], a[96:99], a[12:15], v[92:95]\n"
"ds_read_b64_tr_b16 a[76:77], v16 offset:50240\n"
"v_mfma_f32_16x16x32_bf16 v[92:95], a[100:103], a[28:31], v[92:95]\n"
"buffer_load_dwordx4 v11, s[36:39], s80 offen offset:3072 lds\n"
"v_mfma_f32_16x16x32_bf16 v[88:91], a[100:103], a[24:27], v[88:91]\n"
"ds_read_b128 a[40:43], v18 offset:12544\n"
"v_mfma_f32_16x16x32_bf16 v[88:91], a[96:99], a[8:11], v[88:91]\n"
"ds_read_b64_tr_b16 a[78:79], v16 offset:53824\n"
"s_add_u32 m0, s78, 0x1000\n"
"v_mfma_f32_16x16x32_bf16 v[84:87], a[96:99], a[4:7], v[84:87]\n"
"ds_read_b64_tr_b16 a[80:81], v16 offset:28768\n"
"s_or_b32 s80, s80, 0x10000\n"
"v_mfma_f32_16x16x32_bf16 v[84:87], a[100:103], a[20:23], v[84:87]\n"
"buffer_load_dwordx4 v8, s[36:39], s80 offen offset:0 lds\n"
"v_mfma_f32_16x16x32_bf16 v[80:83], a[100:103], a[16:19], v[80:83]\n"
"ds_read_b128 a[44:47], v18 offset:14592\n"
"v_lshl_add_u32 v17, v0, 2, s28\n"
"v_mfma_f32_16x16x32_bf16 v[80:83], a[96:99], a[0:3], v[80:83]\n"
"ds_read_b64_tr_b16 a[82:83], v16 offset:32352\n"
"s_waitcnt lgkmcnt(9)\n"
"v_mfma_f32_16x16x32_bf16 v[96:99], a[64:67], a[0:3], v[96:99]\n"
"ds_read_b64_tr_b16 a[84:85], v16 offset:35936\n"
"v_mfma_f32_16x16x32_bf16 v[96:99], a[68:71], a[16:19], v[96:99]\n"
"buffer_load_dwordx4 v9, s[36:39], s80 offen offset:1024 lds\n"
"v_mfma_f32_16x16x32_bf16 v[100:103], a[68:71], a[20:23], v[100:103]\n"
"ds_read_b64_tr_b16 a[86:87], v16 offset:39520\n"
"v_mfma_f32_16x16x32_bf16 v[100:103], a[64:67], a[4:7], v[100:103]\n"
"ds_read_b128 a[48:51], v19 offset:8448\n"
"v_mfma_f32_16x16x32_bf16 v[104:107], a[64:67], a[8:11], v[104:107]\n"
"ds_read_b64_tr_b16 a[88:89], v16 offset:43104\n"
"v_mfma_f32_16x16x32_bf16 v[104:107], a[68:71], a[24:27], v[104:107]\n"
"buffer_load_dwordx4 v10, s[36:39], s80 offen offset:2048 lds\n"
"v_mfma_f32_16x16x32_bf16 v[108:111], a[68:71], a[28:31], v[108:111]\n"
"ds_read_b64_tr_b16 a[90:91], v16 offset:46688\n"
"v_mfma_f32_16x16x32_bf16 v[108:111], a[64:67], a[12:15], v[108:111]\n"
"ds_read_b64_tr_b16 a[92:93], v16 offset:50272\n"
"s_waitcnt lgkmcnt(9)\n"
"v_mfma_f32_16x16x32_bf16 v[124:127], a[72:75], a[12:15], v[124:127]\n"
"ds_read_b128 a[52:55], v19 offset:10496\n"
"v_mfma_f32_16x16x32_bf16 v[124:127], a[76:79], a[28:31], v[124:127]\n"
"buffer_load_dwordx4 v11, s[36:39], s80 offen offset:3072 lds\n"
"v_mfma_f32_16x16x32_bf16 v[120:123], a[76:79], a[24:27], v[120:123]\n"
"ds_read_b64_tr_b16 a[94:95], v16 offset:53856\n"
"s_add_u32 m0, s77, 0x0\n"
"v_mfma_f32_16x16x32_bf16 v[120:123], a[72:75], a[8:11], v[120:123]\n"
"ds_read_b64_tr_b16 a[96:97], v16 offset:28800\n"
"s_add_u32 s79, s96, 3\n"
"v_mfma_f32_16x16x32_bf16 v[116:119], a[72:75], a[4:7], v[116:119]\n"
"ds_read_b64_tr_b16 a[98:99], v16 offset:32384\n"
"s_and_b32 s79, s79, 3\n"
"v_mfma_f32_16x16x32_bf16 v[116:119], a[76:79], a[20:23], v[116:119]\n"
"buffer_load_dword a200, v17, s[64:67], 0 offen offset:1024 sc1\n"
"v_mfma_f32_16x16x32_bf16 v[112:115], a[76:79], a[16:19], v[112:115]\n"
"ds_read_b128 a[56:59], v19 offset:12544\n"
"s_add_u32 s79, s79, 4\n"
"v_mfma_f32_16x16x32_bf16 v[112:115], a[72:75], a[0:3], v[112:115]\n"
"ds_read_b64_tr_b16 a[100:101], v16 offset:35968\n"
"s_waitcnt lgkmcnt(10)\n"
"v_mfma_f32_16x16x32_bf16 v[128:131], a[80:83], a[0:3], v[128:131]\n"
"ds_read_b64_tr_b16 a[102:103], v16 offset:39552\n"
"s_mul_i32 s79, s79, 0x70000\n"
"v_mfma_f32_16x16x32_bf16 v[128:131], a[84:87], a[16:19], v[128:131]\n"
"buffer_load_dwordx4 v1, s[40:43], s79 offen offset:0 lds\n"
"v_mfma_f32_16x16x32_bf16 v[132:135], a[84:87], a[20:23], v[132:135]\n"
"ds_read_b64_tr_b16 a[64:65], v16 offset:43136\n"
"s_lshr_b32 s24, s33, 3\n"
"v_mfma_f32_16x16x32_bf16 v[132:135], a[80:83], a[4:7], v[132:135]\n"
"ds_read_b128 a[60:63], v19 offset:14592\n"
"s_lshl2_add_u32 s24, s24, s34\n"
"v_mfma_f32_16x16x32_bf16 v[136:139], a[80:83], a[8:11], v[136:139]\n"
"ds_read_b64_tr_b16 a[66:67], v16 offset:46720\n"
"s_lshl_b32 s24, s24, 7\n"
"v_mfma_f32_16x16x32_bf16 v[136:139], a[84:87], a[24:27], v[136:139]\n"
"buffer_load_dwordx4 v2, s[40:43], s79 offen offset:1024 lds\n"
"v_mfma_f32_16x16x32_bf16 v[140:143], a[84:87], a[28:31], v[140:143]\n"
"ds_read_b64_tr_b16 a[68:69], v16 offset:50304\n"
"v_mov_b32 v17, s24\n"
"v_mfma_f32_16x16x32_bf16 v[140:143], a[80:83], a[12:15], v[140:143]\n"
"ds_read_b64_tr_b16 a[70:71], v16 offset:53888\n"
"s_waitcnt lgkmcnt(10)\n"
"v_mfma_f32_16x16x32_bf16 v[156:159], a[88:91], a[12:15], v[156:159]\n"
"s_waitcnt lgkmcnt(3)\n"
"v_mfma_f32_16x16x32_bf16 v[156:159], a[92:95], a[28:31], v[156:159]\n"
"buffer_load_dwordx4 v3, s[40:43], s79 offen offset:2048 lds\n"
"v_mfma_f32_16x16x32_bf16 v[152:155], a[92:95], a[24:27], v[152:155]\n"
"ds_read_b64_tr_b16 a[72:73], v16 offset:28832\n"
"v_mfma_f32_16x16x32_bf16 v[152:155], a[88:91], a[8:11], v[152:155]\n"
"ds_read_b64_tr_b16 a[74:75], v16 offset:32416\n"
"v_mfma_f32_16x16x32_bf16 v[148:151], a[88:91], a[4:7], v[148:151]\n"
"s_getpc_b64 s[24:25]\n"
".Lfk_ct1:\n"
"s_add_u32 s24, s24, 0xbb8\n"
"s_addc_u32 s25, s25, 0\n"
"global_load_dword a196, v17, s[24:25]\n"
"v_mfma_f32_16x16x32_bf16 v[148:151], a[92:95], a[20:23], v[148:151]\n"
"buffer_load_dwordx4 v4, s[40:43], s79 offen offset:3072 lds\n"
"v_mfma_f32_16x16x32_bf16 v[144:147], a[92:95], a[16:19], v[144:147]\n"
"ds_read_b64_tr_b16 a[76:77], v16 offset:36000\n"
"v_mfma_f32_16x16x32_bf16 v[144:147], a[88:91], a[0:3], v[144:147]\n"
"ds_read_b64_tr_b16 a[78:79], v16 offset:39584\n"
"v_mfma_f32_16x16x32_bf16 v[160:163], a[96:99], a[0:3], v[160:163]\n"
"ds_read_b64_tr_b16 a[80:81], v16 offset:43168\n"
"s_add_u32 m0, s77, 0x1000\n"
"v_mfma_f32_16x16x32_bf16 v[160:163], a[100:103], a[16:19], v[160:163]\n"
"buffer_load_dwordx4 v5, s[40:43], s79 offen offset:0 lds\n"
"v_mfma_f32_16x16x32_bf16 v[164:167], a[100:103], a[20:23], v[164:167]\n"
"v_mfma_f32_16x16x32_bf16 v[164:167], a[96:99], a[4:7], v[164:167]\n"
"ds_read_b64_tr_b16 a[82:83], v16 offset:46752\n"
"v_mfma_f32_16x16x32_bf16 v[168:171], a[96:99], a[8:11], v[168:171]\n"
"ds_read_b64_tr_b16 a[84:85], v16 offset:50336\n"
"v_mfma_f32_16x16x32_bf16 v[168:171], a[100:103], a[24:27], v[168:171]\n"
"buffer_load_dwordx4 v6, s[40:43], s79 offen offset:1024 lds\n"
"v_mfma_f32_16x16x32_bf16 v[172:175], a[100:103], a[28:31], v[172:175]\n"
"ds_read_b64_tr_b16 a[86:87], v16 offset:53920\n"
"v_mfma_f32_16x16x32_bf16 v[172:175], a[96:99], a[12:15], v[172:175]\n"
"s_waitcnt lgkmcnt(8)\n"
"v_mfma_f32_16x16x32_bf16 v[188:191], a[64:67], a[12:15], v[188:191]\n"
"ds_read_b64_tr_b16 a[88:89], v16 offset:28864\n"
"v_mfma_f32_16x16x32_bf16 v[188:191], a[68:71], a[28:31], v[188:191]\n"
"buffer_load_dwordx4 v7, s[40:43], s79 offen offset:2048 lds\n"
"v_mfma_f32_16x16x32_bf16 v[184:187], a[68:71], a[24:27], v[184:187]\n"
"ds_read_b64_tr_b16 a[90:91], v16 offset:32448\n"
"s_add_u32 m0, s78, 0x2100\n"
"v_mfma_f32_16x16x32_bf16 v[184:187], a[64:67], a[8:11], v[184:187]\n"
"ds_read_b64_tr_b16 a[92:93], v16 offset:36032\n"
"s_add_u32 s80, s96, 4\n"
"v_mfma_f32_16x16x32_bf16 v[180:183], a[64:67], a[4:7], v[180:183]\n"
"s_and_b32 s80, s80, 3\n"
"s_add_u32 s80, s80, 4\n"
"v_mfma_f32_16x16x32_bf16 v[180:183], a[68:71], a[20:23], v[180:183]\n"
"v_mfma_f32_16x16x32_bf16 v[176:179], a[68:71], a[16:19], v[176:179]\n"
"ds_read_b64_tr_b16 a[94:95], v16 offset:39616\n"
"s_lshl_b32 s80, s80, 7\n"
"v_mfma_f32_16x16x32_bf16 v[176:179], a[64:67], a[0:3], v[176:179]\n"
"ds_read_b64_tr_b16 a[96:97], v16 offset:43200\n"
"s_waitcnt lgkmcnt(9)\n"
"v_mfma_f32_16x16x32_bf16 v[192:195], a[72:75], a[0:3], v[192:195]\n"
"ds_read_b64_tr_b16 a[98:99], v16 offset:46784\n"
"v_mfma_f32_16x16x32_bf16 v[192:195], a[76:79], a[16:19], v[192:195]\n"
"buffer_load_dwordx4 v8, s[36:39], s80 offen offset:0 lds\n"
"v_mfma_f32_16x16x32_bf16 v[196:199], a[76:79], a[20:23], v[196:199]\n"
"v_mfma_f32_16x16x32_bf16 v[196:199], a[72:75], a[4:7], v[196:199]\n"
"ds_read_b64_tr_b16 a[100:101], v16 offset:50368\n"
"v_mfma_f32_16x16x32_bf16 v[200:203], a[72:75], a[8:11], v[200:203]\n"
"ds_read_b64_tr_b16 a[102:103], v16 offset:53952\n"
"v_mfma_f32_16x16x32_bf16 v[200:203], a[76:79], a[24:27], v[200:203]\n"
"buffer_load_dwordx4 v9, s[36:39], s80 offen offset:1024 lds\n"
"v_mfma_f32_16x16x32_bf16 v[204:207], a[76:79], a[28:31], v[204:207]\n"
"v_mfma_f32_16x16x32_bf16 v[204:207], a[72:75], a[12:15], v[204:207]\n"
"s_waitcnt lgkmcnt(8)\n"
"v_mfma_f32_16x16x32_bf16 v[220:223], a[80:83], a[12:15], v[220:223]\n"
"v_add_u32 v16, 0xe000, v16\n"
"v_mfma_f32_16x16x32_bf16 v[220:223], a[84:87], a[28:31], v[220:223]\n"
"buffer_load_dwordx4 v10, s[36:39], s80 offen offset:2048 lds\n"
"v_mfma_f32_16x16x32_bf16 v[216:219], a[84:87], a[24:27], v[216:219]\n"
"v_mfma_f32_16x16x32_bf16 v[216:219], a[80:83], a[8:11], v[216:219]\n"
"v_mfma_f32_16x16x32_bf16 v[212:215], a[80:83], a[4:7], v[212:215]\n"
"v_mfma_f32_16x16x32_bf16 v[212:215], a[84:87], a[20:23], v[212:215]\n"
"buffer_load_dwordx4 v11, s[36:39], s80 offen offset:3072 lds\n"
"v_mfma_f32_16x16x32_bf16 v[208:211], a[84:87], a[16:19], v[208:211]\n"
"v_mfma_f32_16x16x32_bf16 v[208:211], a[80:83], a[0:3], v[208:211]\n"
"s_waitcnt vmcnt(21) lgkmcnt(0)\n"
"s_barrier\n"
"v_mfma_f32_16x16x32_bf16 v[224:227], a[88:91], a[0:3], v[224:227]\n"
"ds_read_b64_tr_b16 a[64:65], v16 offset:0\n"
"s_add_u32 m0, s78, 0x3100\n"
"v_mfma_f32_16x16x32_bf16 v[224:227], a[92:95], a[16:19], v[224:227]\n"
"v_mfma_f32_16x16x32_bf16 v[228:231], a[92:95], a[20:23], v[228:231]\n"
"ds_read_b64_tr_b16 a[66:67], v16 offset:3584\n"
"s_or_b32 s80, s80, 0x10000\n"
"v_mfma_f32_16x16x32_bf16 v[228:231], a[88:91], a[4:7], v[228:231]\n"
"buffer_load_dwordx4 v8, s[36:39], s80 offen offset:0 lds\n"
"v_mfma_f32_16x16x32_bf16 v[232:235], a[88:91], a[8:11], v[232:235]\n"
"ds_read_b64_tr_b16 a[68:69], v16 offset:7168\n"
"v_mfma_f32_16x16x32_bf16 v[232:235], a[92:95], a[24:27], v[232:235]\n"
"v_mfma_f32_16x16x32_bf16 v[236:239], a[92:95], a[28:31], v[236:239]\n"
"ds_read_b64_tr_b16 a[70:71], v16 offset:10752\n"
"v_mfma_f32_16x16x32_bf16 v[236:239], a[88:91], a[12:15], v[236:239]\n"
"v_mfma_f32_16x16x32_bf16 v[252:255], a[96:99], a[12:15], v[252:255]\n"
"ds_read_b64_tr_b16 a[72:73], v16 offset:14336\n"
"v_mfma_f32_16x16x32_bf16 v[252:255], a[100:103], a[28:31], v[252:255]\n"
"buffer_load_dwordx4 v9, s[36:39], s80 offen offset:1024 lds\n"
"v_mfma_f32_16x16x32_bf16 v[248:251], a[100:103], a[24:27], v[248:251]\n"
"ds_read_b64_tr_b16 a[74:75], v16 offset:17920\n"
"v_mfma_f32_16x16x32_bf16 v[248:251], a[96:99], a[8:11], v[248:251]\n"
"v_mfma_f32_16x16x32_bf16 v[244:247], a[96:99], a[4:7], v[244:247]\n"
"ds_read_b64_tr_b16 a[76:77], v16 offset:21504\n"
"v_mfma_f32_16x16x32_bf16 v[244:247], a[100:103], a[20:23], v[244:247]\n"
"buffer_load_dwordx4 v10, s[36:39], s80 offen offset:2048 lds\n"
"v_mfma_f32_16x16x32_bf16 v[240:243], a[100:103], a[16:19], v[240:243]\n"
"ds_read_b64_tr_b16 a[78:79], v16 offset:25088\n"
"v_mfma_f32_16x16x32_bf16 v[240:243], a[96:99], a[0:3], v[240:243]\n"
"s_waitcnt lgkmcnt(4)\n"
"v_mfma_f32_16x16x32_bf16 v[32:35], a[64:67], a[32:35], v[32:35]\n"
"ds_read_b64_tr_b16 a[80:81], v16 offset:32\n"
"v_mfma_f32_16x16x32_bf16 v[32:35], a[68:71], a[48:51], v[32:35]\n"
"buffer_load_dwordx4 v11, s[36:39], s80 offen offset:3072 lds\n"
"v_mfma_f32_16x16x32_bf16 v[36:39], a[68:71], a[52:55], v[36:39]\n"
"ds_read_b64_tr_b16 a[82:83], v16 offset:3616\n"
"s_add_u32 m0, s77, 0x7000\n"
"v_mfma_f32_16x16x32_bf16 v[36:39], a[64:67], a[36:39], v[36:39]\n"
"ds_read_b64_tr_b16 a[84:85], v16 offset:7200\n"
"s_add_u32 s79, s96, 4\n"
"v_mfma_f32_16x16x32_bf16 v[40:43], a[64:67], a[40:43], v[40:43]\n"
"s_and_b32 s79, s79, 3\n"
"s_add_u32 s79, s79, 4\n"
"v_mfma_f32_16x16x32_bf16 v[40:43], a[68:71], a[56:59], v[40:43]\n"
"v_mfma_f32_16x16x32_bf16 v[44:47], a[68:71], a[60:63], v[44:47]\n"
"ds_read_b64_tr_b16 a[86:87], v16 offset:10784\n"
"s_mul_i32 s79, s79, 0x70000\n"
"v_mfma_f32_16x16x32_bf16 v[44:47], a[64:67], a[44:47], v[44:47]\n"
"ds_read_b64_tr_b16 a[88:89], v16 offset:14368\n"
"s_waitcnt lgkmcnt(5)\n"
"v_mfma_f32_16x16x32_bf16 v[60:63], a[72:75], a[44:47], v[60:63]\n"
"ds_read_b64_tr_b16 a[90:91], v16 offset:17952\n"
"v_mfma_f32_16x16x32_bf16 v[60:63], a[76:79], a[60:63], v[60:63]\n"
"buffer_load_dwordx4 v1, s[40:43], s79 offen offset:0 lds\n"
"v_mfma_f32_16x16x32_bf16 v[56:59], a[76:79], a[56:59], v[56:59]\n"
"v_mfma_f32_16x16x32_bf16 v[56:59], a[72:75], a[40:43], v[56:59]\n"
"ds_read_b64_tr_b16 a[92:93], v16 offset:21536\n"
"v_mfma_f32_16x16x32_bf16 v[52:55], a[72:75], a[36:39], v[52:55]\n"
"ds_read_b64_tr_b16 a[94:95], v16 offset:25120\n"
"s_waitcnt vmcnt(18)\n"
"v_mfma_f32_16x16x32_bf16 v[52:55], a[76:79], a[52:55], v[52:55]\n"
"buffer_load_dwordx4 v2, s[40:43], s79 offen offset:1024 lds\n"
"v_mfma_f32_16x16x32_bf16 v[48:51], a[76:79], a[48:51], v[48:51]\n"
"ds_read_b64_tr_b16 a[96:97], v16 offset:64\n"
"v_mfma_f32_16x16x32_bf16 v[48:51], a[72:75], a[32:35], v[48:51]\n"
"ds_read_b128 a[0:3], v18 offset:0\n"
"s_waitcnt lgkmcnt(6)\n"
"v_mfma_f32_16x16x32_bf16 v[64:67], a[80:83], a[32:35], v[64:67]\n"
"ds_read_b64_tr_b16 a[98:99], v16 offset:3648\n"
"v_mfma_f32_16x16x32_bf16 v[64:67], a[84:87], a[48:51], v[64:67]\n"
"buffer_load_dwordx4 v3, s[40:43], s79 offen offset:2048 lds\n"
"v_mfma_f32_16x16x32_bf16 v[68:71], a[84:87], a[52:55], v[68:71]\n"
"ds_read_b64_tr_b16 a[100:101], v16 offset:7232\n"
"v_mfma_f32_16x16x32_bf16 v[68:71], a[80:83], a[36:39], v[68:71]\n"
"ds_read_b64_tr_b16 a[102:103], v16 offset:10816\n"
"v_mfma_f32_16x16x32_bf16 v[72:75], a[80:83], a[40:43], v[72:75]\n"
"ds_read_b128 a[4:7], v18 offset:2048\n"
"v_mfma_f32_16x16x32_bf16 v[72:75], a[84:87], a[56:59], v[72:75]\n"
"buffer_load_dwordx4 v4, s[40:43], s79 offen offset:3072 lds\n"
"v_mfma_f32_16x16x32_bf16 v[76:79], a[84:87], a[60:63], v[76:79]\n"
"ds_read_b64_tr_b16 a[64:65], v16 offset:14400\n"
"v_mfma_f32_16x16x32_bf16 v[76:79], a[80:83], a[44:47], v[76:79]\n"
"ds_read_b64_tr_b16 a[66:67], v16 offset:17984\n"
"s_waitcnt lgkmcnt(8)\n"
"v_mfma_f32_16x16x32_bf16 v[92:95], a[88:91], a[44:47], v[92:95]\n"
"ds_read_b64_tr_b16 a[68:69], v16 offset:21568\n"
"s_add_u32 m0, s77, 0x8000\n"
"v_mfma_f32_16x16x32_bf16 v[92:95], a[92:95], a[60:63], v[92:95]\n"
"buffer_load_dwordx4 v5, s[40:43], s79 offen offset:0 lds\n"
"v_mfma_f32_16x16x32_bf16 v[88:91], a[92:95], a[56:59], v[88:91]\n"
"ds_read_b128 a[8:11], v18 offset:4096\n"
"v_mfma_f32_16x16x32_bf16 v[88:91], a[88:91], a[40:43], v[88:91]\n"
"ds_read_b64_tr_b16 a[70:71], v16 offset:25152\n"
"v_mfma_f32_16x16x32_bf16 v[84:87], a[88:91], a[36:39], v[84:87]\n"
"ds_read_b64_tr_b16 a[72:73], v16 offset:96\n"
"v_mfma_f32_16x16x32_bf16 v[84:87], a[92:95], a[52:55], v[84:87]\n"
"buffer_load_dwordx4 v6, s[40:43], s79 offen offset:1024 lds\n"
"v_mfma_f32_16x16x32_bf16 v[80:83], a[92:95], a[48:51], v[80:83]\n"
"ds_read_b128 a[12:15], v18 offset:6144\n"
"v_lshl_add_u32 v17, v0, 2, s28\n"
"v_mfma_f32_16x16x32_bf16 v[80:83], a[88:91], a[32:35], v[80:83]\n"
"ds_read_b64_tr_b16 a[74:75], v16 offset:3680\n"
"s_waitcnt lgkmcnt(9)\n"
"v_mfma_f32_16x16x32_bf16 v[96:99], a[96:99], a[32:35], v[96:99]\n"
"ds_read_b64_tr_b16 a[76:77], v16 offset:7264\n"
"s_waitcnt vmcnt(22)\n"
"v_mfma_f32_16x16x32_bf16 v[96:99], a[100:103], a[48:51], v[96:99]\n"
"buffer_load_dwordx4 v7, s[40:43], s79 offen offset:2048 lds\n"
"v_mfma_f32_16x16x32_bf16 v[100:103], a[100:103], a[52:55], v[100:103]\n"
"ds_read_b64_tr_b16 a[78:79], v16 offset:10848\n"
"v_mfma_f32_16x16x32_bf16 v[100:103], a[96:99], a[36:39], v[100:103]\n"
"ds_read_b128 a[16:19], v19 offset:0\n"
"v_mfma_f32_16x16x32_bf16 v[104:107], a[96:99], a[40:43], v[104:107]\n"
"ds_read_b64_tr_b16 a[80:81], v16 offset:14432\n"
"v_mfma_f32_16x16x32_bf16 v[104:107], a[100:103], a[56:59], v[104:107]\n"
"buffer_store_dword a200, v17, s[64:67], 0 offen offset:2048 sc1\n"
"v_mfma_f32_16x16x32_bf16 v[108:111], a[100:103], a[60:63], v[108:111]\n"
"ds_read_b64_tr_b16 a[82:83], v16 offset:18016\n"
"v_mfma_f32_16x16x32_bf16 v[108:111], a[96:99], a[44:47], v[108:111]\n"
"ds_read_b64_tr_b16 a[84:85], v16 offset:21600\n"
"s_waitcnt lgkmcnt(9)\n"
"v_mfma_f32_16x16x32_bf16 v[124:127], a[64:67], a[44:47], v[124:127]\n"
"ds_read_b128 a[20:23], v19 offset:2048\n"
"v_mfma_f32_16x16x32_bf16 v[124:127], a[68:71], a[60:63], v[124:127]\n"
"v_mfma_f32_16x16x32_bf16 v[120:123], a[68:71], a[56:59], v[120:123]\n"
"ds_read_b64_tr_b16 a[86:87], v16 offset:25184\n"
"v_mfma_f32_16x16x32_bf16 v[120:123], a[64:67], a[40:43], v[120:123]\n"
"ds_read_b64_tr_b16 a[88:89], v16 offset:128\n"
"v_mfma_f32_16x16x32_bf16 v[116:119], a[64:67], a[36:39], v[116:119]\n"
"ds_read_b64_tr_b16 a[90:91], v16 offset:3712\n"
"v_mfma_f32_16x16x32_bf16 v[116:119], a[68:71], a[52:55], v[116:119]\n"
"v_mfma_f32_16x16x32_bf16 v[112:115], a[68:71], a[48:51], v[112:115]\n"
"ds_read_b128 a[24:27], v19 offset:4096\n"
"v_mfma_f32_16x16x32_bf16 v[112:115], a[64:67], a[32:35], v[112:115]\n"
"ds_read_b64_tr_b16 a[92:93], v16 offset:7296\n"
"s_waitcnt lgkmcnt(10)\n"
"v_mfma_f32_16x16x32_bf16 v[128:131], a[72:75], a[32:35], v[128:131]\n"
"ds_read_b64_tr_b16 a[94:95], v16 offset:10880\n"
"v_mfma_f32_16x16x32_bf16 v[128:131], a[76:79], a[48:51], v[128:131]\n"
"v_mfma_f32_16x16x32_bf16 v[132:135], a[76:79], a[52:55], v[132:135]\n"
"ds_read_b64_tr_b16 a[96:97], v16 offset:14464\n"
"v_mfma_f32_16x16x32_bf16 v[132:135], a[72:75], a[36:39], v[132:135]\n"
"ds_read_b128 a[28:31], v19 offset:6144\n"
"v_mfma_f32_16x16x32_bf16 v[136:139], a[72:75], a[40:43], v[136:139]\n"
"ds_read_b64_tr_b16 a[98:99], v16 offset:18048\n"
"s_add_u32 m0, s78, 0x0\n"
"v_mfma_f32_16x16x32_bf16 v[136:139], a[76:79], a[56:59], v[136:139]\n"
"v_mfma_f32_16x16x32_bf16 v[140:143], a[76:79], a[60:63], v[140:143]\n"
"ds_read_b64_tr_b16 a[100:101], v16 offset:21632\n"
"s_add_u32 s80, s96, 2\n"
"v_mfma_f32_16x16x32_bf16 v[140:143], a[72:75], a[44:47], v[140:143]\n"
"ds_read_b64_tr_b16 a[102:103], v16 offset:25216\n"
"s_waitcnt lgkmcnt(10)\n"
"v_mfma_f32_16x16x32_bf16 v[156:159], a[80:83], a[44:47], v[156:159]\n"
"s_waitcnt lgkmcnt(3)\n"
"s_and_b32 s80, s80, 3\n"
"v_mfma_f32_16x16x32_bf16 v[156:159], a[84:87], a[60:63], v[156:159]\n"
"v_mfma_f32_16x16x32_bf16 v[152:155], a[84:87], a[56:59], v[152:155]\n"
"ds_read_b64_tr_b16 a[64:65], v16 offset:160\n"
"s_add_u32 s80, s80, 8\n"
"v_mfma_f32_16x16x32_bf16 v[152:155], a[80:83], a[40:43], v[152:155]\n"
"ds_read_b64_tr_b16 a[66:67], v16 offset:3744\n"
"s_lshl_b32 s80, s80, 7\n"
"v_mfma_f32_16x16x32_bf16 v[148:151], a[80:83], a[36:39], v[148:151]\n"
"buffer_load_dwordx4 v8, s[36:39], s80 offen offset:0 lds\n"
"v_mfma_f32_16x16x32_bf16 v[148:151], a[84:87], a[52:55], v[148:151]\n"
"v_mfma_f32_16x16x32_bf16 v[144:147], a[84:87], a[48:51], v[144:147]\n"
"ds_read_b64_tr_b16 a[68:69], v16 offset:7328\n"
"v_mfma_f32_16x16x32_bf16 v[144:147], a[80:83], a[32:35], v[144:147]\n"
"ds_read_b64_tr_b16 a[70:71], v16 offset:10912\n"
"v_mfma_f32_16x16x32_bf16 v[160:163], a[88:91], a[32:35], v[160:163]\n"
"ds_read_b64_tr_b16 a[72:73], v16 offset:14496\n"
"v_mfma_f32_16x16x32_bf16 v[160:163], a[92:95], a[48:51], v[160:163]\n"
"buffer_load_dwordx4 v9, s[36:39], s80 offen offset:1024 lds\n"
"v_mfma_f32_16x16x32_bf16 v[164:167], a[92:95], a[52:55], v[164:167]\n"
"v_mfma_f32_16x16x32_bf16 v[164:167], a[88:91], a[36:39], v[164:167]\n"
"ds_read_b64_tr_b16 a[74:75], v16 offset:18080\n"
"v_mfma_f32_16x16x32_bf16 v[168:171], a[88:91], a[40:43], v[168:171]\n"
"ds_read_b64_tr_b16 a[76:77], v16 offset:21664\n"
"v_mfma_f32_16x16x32_bf16 v[168:171], a[92:95], a[56:59], v[168:171]\n"
"buffer_load_dwordx4 v10, s[36:39], s80 offen offset:2048 lds\n"
"v_mfma_f32_16x16x32_bf16 v[172:175], a[92:95], a[60:63], v[172:175]\n"
"ds_read_b64_tr_b16 a[78:79], v16 offset:25248\n"
"v_mfma_f32_16x16x32_bf16 v[172:175], a[88:91], a[44:47], v[172:175]\n"
"s_waitcnt lgkmcnt(8)\n"
"v_mfma_f32_16x16x32_bf16 v[188:191], a[96:99], a[44:47], v[188:191]\n"
"ds_read_b64_tr_b16 a[80:81], v16 offset:192\n"
"v_mfma_f32_16x16x32_bf16 v[188:191], a[100:103], a[60:63], v[188:191]\n"
"buffer_load_dwordx4 v11, s[36:39], s80 offen offset:3072 lds\n"
"v_mfma_f32_16x16x32_bf16 v[184:187], a[100:103], a[56:59], v[184:187]\n"
"ds_read_b64_tr_b16 a[82:83], v16 offset:3776\n"
"v_mfma_f32_16x16x32_bf16 v[184:187], a[96:99], a[40:43], v[184:187]\n"
"ds_read_b64_tr_b16 a[84:85], v16 offset:7360\n"
"s_add_u32 m0, s78, 0x1000\n"
"v_mfma_f32_16x16x32_bf16 v[180:183], a[96:99], a[36:39], v[180:183]\n"
"s_or_b32 s80, s80, 0x10000\n"
"v_mfma_f32_16x16x32_bf16 v[180:183], a[100:103], a[52:55], v[180:183]\n"
"buffer_load_dwordx4 v8, s[36:39], s80 offen offset:0 lds\n"
"v_mfma_f32_16x16x32_bf16 v[176:179], a[100:103], a[48:51], v[176:179]\n"
"ds_read_b64_tr_b16 a[86:87], v16 offset:10944\n"
"v_mfma_f32_16x16x32_bf16 v[176:179], a[96:99], a[32:35], v[176:179]\n"
"ds_read_b64_tr_b16 a[88:89], v16 offset:14528\n"
"s_waitcnt lgkmcnt(9)\n"
"v_mfma_f32_16x16x32_bf16 v[192:195], a[64:67], a[32:35], v[192:195]\n"
"ds_read_b64_tr_b16 a[90:91], v16 offset:18112\n"
"v_mfma_f32_16x16x32_bf16 v[192:195], a[68:71], a[48:51], v[192:195]\n"
"buffer_load_dwordx4 v9, s[36:39], s80 offen offset:1024 lds\n"
"v_mfma_f32_16x16x32_bf16 v[196:199], a[68:71], a[52:55], v[196:199]\n"
"v_mfma_f32_16x16x32_bf16 v[196:199], a[64:67], a[36:39], v[196:199]\n"
"ds_read_b64_tr_b16 a[92:93], v16 offset:21696\n"
"v_mfma_f32_16x16x32_bf16 v[200:203], a[64:67], a[40:43], v[200:203]\n"
"ds_read_b64_tr_b16 a[94:95], v16 offset:25280\n"
"v_mfma_f32_16x16x32_bf16 v[200:203], a[68:71], a[56:59], v[200:203]\n"
"buffer_load_dwordx4 v10, s[36:39], s80 offen offset:2048 lds\n"
"v_mfma_f32_16x16x32_bf16 v[204:207], a[68:71], a[60:63], v[204:207]\n"
"v_mfma_f32_16x16x32_bf16 v[204:207], a[64:67], a[44:47], v[204:207]\n"
"s_waitcnt lgkmcnt(8)\n"
"v_mfma_f32_16x16x32_bf16 v[220:223], a[72:75], a[44:47], v[220:223]\n"
"v_subrev_u32 v16, 0xe000, v16\n"
"v_mfma_f32_16x16x32_bf16 v[220:223], a[76:79], a[60:63], v[220:223]\n"
"buffer_load_dwordx4 v11, s[36:39], s80 offen offset:3072 lds\n"
"v_mfma_f32_16x16x32_bf16 v[216:219], a[76:79], a[56:59], v[216:219]\n"
"v_mfma_f32_16x16x32_bf16 v[216:219], a[72:75], a[40:43], v[216:219]\n"
"s_add_u32 m0, s77, 0xe000\n"
"s_add_u32 s79, s96, 2\n"
".Lfk_hop1719:\n"
"s_cbranch_execz .Lfk_hopret\n"
"v_mfma_f32_16x16x32_bf16 v[212:215], a[72:75], a[36:39], v[212:215]\n"
"s_and_b32 s79, s79, 3\n"
"s_add_u32 s79, s79, 8\n"
"v_mfma_f32_16x16x32_bf16 v[212:215], a[76:79], a[52:55], v[212:215]\n"
"v_mfma_f32_16x16x32_bf16 v[208:211], a[76:79], a[48:51], v[208:211]\n"
"s_mul_i32 s79, s79, 0x70000\n"
"v_mfma_f32_16x16x32_bf16 v[208:211], a[72:75], a[32:35], v[208:211]\n"
"s_waitcnt vmcnt(24) lgkmcnt(0)\n"
"s_barrier\n"
"v_mfma_f32_16x16x32_bf16 v[224:227], a[80:83], a[32:35], v[224:227]\n"
"ds_read_b64_tr_b16 a[96:97], v16 offset:0\n"
"v_mfma_f32_16x16x32_bf16 v[224:227], a[84:87], a[48:51], v[224:227]\n"
"buffer_load_dwordx4 v1, s[40:43], s79 offen offset:0 lds\n"
"v_mfma_f32_16x16x32_bf16 v[228:231], a[84:87], a[52:55], v[228:231]\n"
"ds_read_b64_tr_b16 a[98:99], v16 offset:3584\n"
"v_mfma_f32_16x16x32_bf16 v[228:231], a[80:83], a[36:39], v[228:231]\n"
"v_mfma_f32_16x16x32_bf16 v[232:235], a[80:83], a[40:43], v[232:235]\n"
"ds_read_b64_tr_b16 a[100:101], v16 offset:7168\n"
"v_mfma_f32_16x16x32_bf16 v[232:235], a[84:87], a[56:59], v[232:235]\n"
"buffer_load_dwordx4 v2, s[40:43], s79 offen offset:1024 lds\n"
"v_mfma_f32_16x16x32_bf16 v[236:239], a[84:87], a[60:63], v[236:239]\n"
"ds_read_b64_tr_b16 a[102:103], v16 offset:10752\n"
"v_mfma_f32_16x16x32_bf16 v[236:239], a[80:83], a[44:47], v[236:239]\n"
"v_mfma_f32_16x16x32_bf16 v[252:255], a[88:91], a[44:47], v[252:255]\n"
"ds_read_b64_tr_b16 a[64:65], v16 offset:14336\n"
"v_mfma_f32_16x16x32_bf16 v[252:255], a[92:95], a[60:63], v[252:255]\n"
"buffer_load_dwordx4 v3, s[40:43], s79 offen offset:2048 lds\n"
"v_mfma_f32_16x16x32_bf16 v[248:251], a[92:95], a[56:59], v[248:251]\n"
"ds_read_b64_tr_b16 a[66:67], v16 offset:17920\n"
"v_mfma_f32_16x16x32_bf16 v[248:251], a[88:91], a[40:43], v[248:251]\n"
"v_mfma_f32_16x16x32_bf16 v[244:247], a[88:91], a[36:39], v[244:247]\n"
"ds_read_b64_tr_b16 a[68:69], v16 offset:21504\n"
"v_mfma_f32_16x16x32_bf16 v[244:247], a[92:95], a[52:55], v[244:247]\n"
"buffer_load_dwordx4 v4, s[40:43], s79 offen offset:3072 lds\n"
"v_mfma_f32_16x16x32_bf16 v[240:243], a[92:95], a[48:51], v[240:243]\n"
"ds_read_b64_tr_b16 a[70:71], v16 offset:25088\n"
"v_mfma_f32_16x16x32_bf16 v[240:243], a[88:91], a[32:35], v[240:243]\n"
"s_waitcnt lgkmcnt(4)\n"
"v_mfma_f32_16x16x32_bf16 v[32:35], a[96:99], a[0:3], v[32:35]\n"
"ds_read_b64_tr_b16 a[72:73], v16 offset:32\n"
"s_add_u32 m0, s77, 0xf000\n"
"v_mfma_f32_16x16x32_bf16 v[32:35], a[100:103], a[16:19], v[32:35]\n"
"buffer_load_dwordx4 v5, s[40:43], s79 offen offset:0 lds\n"
"v_mfma_f32_16x16x32_bf16 v[36:39], a[100:103], a[20:23], v[36:39]\n"
"ds_read_b64_tr_b16 a[74:75], v16 offset:3616\n"
"v_mfma_f32_16x16x32_bf16 v[36:39], a[96:99], a[4:7], v[36:39]\n"
"ds_read_b64_tr_b16 a[76:77], v16 offset:7200\n"
"v_mfma_f32_16x16x32_bf16 v[40:43], a[96:99], a[8:11], v[40:43]\n"
"v_mfma_f32_16x16x32_bf16 v[40:43], a[100:103], a[24:27], v[40:43]\n"
"buffer_load_dwordx4 v6, s[40:43], s79 offen offset:1024 lds\n"
"v_mfma_f32_16x16x32_bf16 v[44:47], a[100:103], a[28:31], v[44:47]\n"
"ds_read_b64_tr_b16 a[78:79], v16 offset:10784\n"
"v_mfma_f32_16x16x32_bf16 v[44:47], a[96:99], a[12:15], v[44:47]\n"
"ds_read_b64_tr_b16 a[80:81], v16 offset:14368\n"
"s_waitcnt lgkmcnt(5)\n"
"v_mfma_f32_16x16x32_bf16 v[60:63], a[64:67], a[12:15], v[60:63]\n"
"ds_read_b64_tr_b16 a[82:83], v16 offset:17952\n"
"v_mov_b32 v17, s92\n"
"v_mfma_f32_16x16x32_bf16 v[60:63], a[68:71], a[28:31], v[60:63]\n"
"buffer_load_dwordx4 v7, s[40:43], s79 offen offset:2048 lds\n"
"v_mfma_f32_16x16x32_bf16 v[56:59], a[68:71], a[24:27], v[56:59]\n"
"buffer_load_dword a201, v17, s[64:67], 0 offen offset:2048 sc1\n"
"v_mfma_f32_16x16x32_bf16 v[56:59], a[64:67], a[8:11], v[56:59]\n"
"ds_read_b64_tr_b16 a[84:85], v16 offset:21536\n"
"v_mfma_f32_16x16x32_bf16 v[52:55], a[64:67], a[4:7], v[52:55]\n"
"ds_read_b64_tr_b16 a[86:87], v16 offset:25120\n"
"s_waitcnt vmcnt(24)\n"
"v_mfma_f32_16x16x32_bf16 v[52:55], a[68:71], a[20:23], v[52:55]\n"
"v_mfma_f32_16x16x32_bf16 v[48:51], a[68:71], a[16:19], v[48:51]\n"
"ds_read_b64_tr_b16 a[88:89], v16 offset:64\n"
"v_mfma_f32_16x16x32_bf16 v[48:51], a[64:67], a[0:3], v[48:51]\n"
"ds_read_b128 a[32:35], v18 offset:8448\n"
"s_waitcnt lgkmcnt(6)\n"
"v_mfma_f32_16x16x32_bf16 v[64:67], a[72:75], a[0:3], v[64:67]\n"
"ds_read_b64_tr_b16 a[90:91], v16 offset:3648\n"
"v_mfma_f32_16x16x32_bf16 v[64:67], a[76:79], a[16:19], v[64:67]\n"
"v_mfma_f32_16x16x32_bf16 v[68:71], a[76:79], a[20:23], v[68:71]\n"
"ds_read_b64_tr_b16 a[92:93], v16 offset:7232\n"
"v_mfma_f32_16x16x32_bf16 v[68:71], a[72:75], a[4:7], v[68:71]\n"
"ds_read_b64_tr_b16 a[94:95], v16 offset:10816\n"
"v_mfma_f32_16x16x32_bf16 v[72:75], a[72:75], a[8:11], v[72:75]\n"
"ds_read_b128 a[36:39], v18 offset:10496\n"
"v_mfma_f32_16x16x32_bf16 v[72:75], a[76:79], a[24:27], v[72:75]\n"
"buffer_load_dwordx2 a[126:127], v13, s[60:63], 0 offen offset:0 nt\n"
"v_mfma_f32_16x16x32_bf16 v[76:79], a[76:79], a[28:31], v[76:79]\n"
"ds_read_b64_tr_b16 a[96:97], v16 offset:14400\n"
"v_mfma_f32_16x16x32_bf16 v[76:79], a[72:75], a[12:15], v[76:79]\n"
"ds_read_b64_tr_b16 a[98:99], v16 offset:17984\n"
"s_waitcnt lgkmcnt(8)\n"
"v_mfma_f32_16x16x32_bf16 v[92:95], a[80:83], a[12:15], v[92:95]\n"
"ds_read_b64_tr_b16 a[100:101], v16 offset:21568\n"
"v_mfma_f32_16x16x32_bf16 v[92:95], a[84:87], a[28:31], v[92:95]\n"
"buffer_load_dwordx2 a[128:129], v13, s[60:63], 0 offen offset:2048 nt\n"
"v_mfma_f32_16x16x32_bf16 v[88:91], a[84:87], a[24:27], v[88:91]\n"
"ds_read_b128 a[40:43], v18 offset:12544\n"
"v_mfma_f32_16x16x32_bf16 v[88:91], a[80:83], a[8:11], v[88:91]\n"
"ds_read_b64_tr_b16 a[102:103], v16 offset:25152\n"
"v_mfma_f32_16x16x32_bf16 v[84:87], a[80:83], a[4:7], v[84:87]\n"
"ds_read_b64_tr_b16 a[64:65], v16 offset:96\n"
"v_mfma_f32_16x16x32_bf16 v[84:87], a[84:87], a[20:23], v[84:87]\n"
"buffer_load_dwordx2 a[130:131], v13, s[60:63], s8 offen offset:0 nt\n"
"v_mfma_f32_16x16x32_bf16 v[80:83], a[84:87], a[16:19], v[80:83]\n"
"ds_read_b128 a[44:47], v18 offset:14592\n"
"v_mfma_f32_16x16x32_bf16 v[80:83], a[80:83], a[0:3], v[80:83]\n"
"ds_read_b64_tr_b16 a[66:67], v16 offset:3680\n"
"s_waitcnt lgkmcnt(9)\n"
"v_mfma_f32_16x16x32_bf16 v[96:99], a[88:91], a[0:3], v[96:99]\n"
"ds_read_b64_tr_b16 a[68:69], v16 offset:7264\n"
"v_mfma_f32_16x16x32_bf16 v[96:99], a[92:95], a[16:19], v[96:99]\n"
"buffer_load_dwordx2 a[132:133], v13, s[60:63], s8 offen offset:2048 nt\n"
"v_mfma_f32_16x16x32_bf16 v[100:103], a[92:95], a[20:23], v[100:103]\n"
"ds_read_b64_tr_b16 a[70:71], v16 offset:10848\n"
"v_mfma_f32_16x16x32_bf16 v[100:103], a[88:91], a[4:7], v[100:103]\n"
"ds_read_b128 a[48:51], v19 offset:8448\n"
"v_mfma_f32_16x16x32_bf16 v[104:107], a[88:91], a[8:11], v[104:107]\n"
"ds_read_b64_tr_b16 a[72:73], v16 offset:14432\n"
"v_mfma_f32_16x16x32_bf16 v[104:107], a[92:95], a[24:27], v[104:107]\n"
"buffer_load_dwordx2 a[134:135], v13, s[60:63], s9 offen offset:0 nt\n"
"v_mfma_f32_16x16x32_bf16 v[108:111], a[92:95], a[28:31], v[108:111]\n"
"ds_read_b64_tr_b16 a[74:75], v16 offset:18016\n"
"v_mfma_f32_16x16x32_bf16 v[108:111], a[88:91], a[12:15], v[108:111]\n"
"ds_read_b64_tr_b16 a[76:77], v16 offset:21600\n"
"s_waitcnt lgkmcnt(9)\n"
"v_mfma_f32_16x16x32_bf16 v[124:127], a[96:99], a[12:15], v[124:127]\n"
"ds_read_b128 a[52:55], v19 offset:10496\n"
"v_mfma_f32_16x16x32_bf16 v[124:127], a[100:103], a[28:31], v[124:127]\n"
"buffer_load_dwordx2 a[136:137], v13, s[60:63], s9 offen offset:2048 nt\n"
"v_mfma_f32_16x16x32_bf16 v[120:123], a[100:103], a[24:27], v[120:123]\n"
"ds_read_b64_tr_b16 a[78:79], v16 offset:25184\n"
"v_mfma_f32_16x16x32_bf16 v[120:123], a[96:99], a[8:11], v[120:123]\n"
"ds_read_b64_tr_b16 a[80:81], v16 offset:128\n"
"v_mfma_f32_16x16x32_bf16 v[116:119], a[96:99], a[4:7], v[116:119]\n"
"ds_read_b64_tr_b16 a[82:83], v16 offset:3712\n"
"v_mfma_f32_16x16x32_bf16 v[116:119], a[100:103], a[20:23], v[116:119]\n"
"buffer_load_dwordx2 a[138:139], v13, s[60:63], s10 offen offset:0 nt\n"
"v_mfma_f32_16x16x32_bf16 v[112:115], a[100:103], a[16:19], v[112:115]\n"
"ds_read_b128 a[56:59], v19 offset:12544\n"
"v_mfma_f32_16x16x32_bf16 v[112:115], a[96:99], a[0:3], v[112:115]\n"
"ds_read_b64_tr_b16 a[84:85], v16 offset:7296\n"
"s_waitcnt lgkmcnt(10)\n"
"v_mfma_f32_16x16x32_bf16 v[128:131], a[64:67], a[0:3], v[128:131]\n"
"ds_read_b64_tr_b16 a[86:87], v16 offset:10880\n"
"v_mfma_f32_16x16x32_bf16 v[128:131], a[68:71], a[16:19], v[128:131]\n"
"buffer_load_dwordx2 a[140:141], v13, s[60:63], s10 offen offset:2048 nt\n"
"v_mfma_f32_16x16x32_bf16 v[132:135], a[68:71], a[20:23], v[132:135]\n"
"ds_read_b64_tr_b16 a[88:89], v16 offset:14464\n"
"v_mfma_f32_16x16x32_bf16 v[132:135], a[64:67], a[4:7], v[132:135]\n"
"ds_read_b128 a[60:63], v19 offset:14592\n"
"v_mfma_f32_16x16x32_bf16 v[136:139], a[64:67], a[8:11], v[136:139]\n"
"ds_read_b64_tr_b16 a[90:91], v16 offset:18048\n"
"v_mfma_f32_16x16x32_bf16 v[136:139], a[68:71], a[24:27], v[136:139]\n"
"v_mfma_f32_16x16x32_bf16 v[140:143], a[68:71], a[28:31], v[140:143]\n"
"ds_read_b64_tr_b16 a[92:93], v16 offset:21632\n"
"v_mfma_f32_16x16x32_bf16 v[140:143], a[64:67], a[12:15], v[140:143]\n"
"ds_read_b64_tr_b16 a[94:95], v16 offset:25216\n"
"s_waitcnt lgkmcnt(10)\n"
"v_mfma_f32_16x16x32_bf16 v[156:159], a[72:75], a[12:15], v[156:159]\n"
"s_waitcnt lgkmcnt(3)\n"
"v_mfma_f32_16x16x32_bf16 v[156:159], a[76:79], a[28:31], v[156:159]\n"
"v_mfma_f32_16x16x32_bf16 v[152:155], a[76:79], a[24:27], v[152:155]\n"
"ds_read_b64_tr_b16 a[96:97], v16 offset:160\n"
"v_mfma_f32_16x16x32_bf16 v[152:155], a[72:75], a[8:11], v[152:155]\n"
"ds_read_b64_tr_b16 a[98:99], v16 offset:3744\n"
"v_mfma_f32_16x16x32_bf16 v[148:151], a[72:75], a[4:7], v[148:151]\n"
"v_mfma_f32_16x16x32_bf16 v[148:151], a[76:79], a[20:23], v[148:151]\n"
"v_mfma_f32_16x16x32_bf16 v[144:147], a[76:79], a[16:19], v[144:147]\n"
"ds_read_b64_tr_b16 a[100:101], v16 offset:7328\n"
"v_mfma_f32_16x16x32_bf16 v[144:147], a[72:75], a[0:3], v[144:147]\n"
"ds_read_b64_tr_b16 a[102:103], v16 offset:10912\n"
"v_mfma_f32_16x16x32_bf16 v[160:163], a[80:83], a[0:3], v[160:163]\n"
"ds_read_b64_tr_b16 a[64:65], v16 offset:14496\n"
"v_mfma_f32_16x16x32_bf16 v[160:163], a[84:87], a[16:19], v[160:163]\n"
"v_mfma_f32_16x16x32_bf16 v[164:167], a[84:87], a[20:23], v[164:167]\n"
"v_mfma_f32_16x16x32_bf16 v[164:167], a[80:83], a[4:7], v[164:167]\n"
"ds_read_b64_tr_b16 a[66:67], v16 offset:18080\n"
"v_mfma_f32_16x16x32_bf16 v[168:171], a[80:83], a[8:11], v[168:171]\n"
"ds_read_b64_tr_b16 a[68:69], v16 offset:21664\n"
"v_mfma_f32_16x16x32_bf16 v[168:171], a[84:87], a[24:27], v[168:171]\n"
"v_mfma_f32_16x16x32_bf16 v[172:175], a[84:87], a[28:31], v[172:175]\n"
"ds_read_b64_tr_b16 a[70:71], v16 offset:25248\n"
"v_mfma_f32_16x16x32_bf16 v[172:175], a[80:83], a[12:15], v[172:175]\n"
"s_waitcnt lgkmcnt(8)\n"
"v_mfma_f32_16x16x32_bf16 v[188:191], a[88:91], a[12:15], v[188:191]\n"
"ds_read_b64_tr_b16 a[72:73], v16 offset:192\n"
"v_mfma_f32_16x16x32_bf16 v[188:191], a[92:95], a[28:31], v[188:191]\n"
"v_mfma_f32_16x16x32_bf16 v[184:187], a[92:95], a[24:27], v[184:187]\n"
"ds_read_b64_tr_b16 a[74:75], v16 offset:3776\n"
"v_mfma_f32_16x16x32_bf16 v[184:187], a[88:91], a[8:11], v[184:187]\n"
"ds_read_b64_tr_b16 a[76:77], v16 offset:7360\n"
"v_mfma_f32_16x16x32_bf16 v[180:183], a[88:91], a[4:7], v[180:183]\n"
"v_mfma_f32_16x16x32_bf16 v[180:183], a[92:95], a[20:23], v[180:183]\n"
"v_mfma_f32_16x16x32_bf16 v[176:179], a[92:95], a[16:19], v[176:179]\n"
"ds_read_b64_tr_b16 a[78:79], v16 offset:10944\n"
"v_mfma_f32_16x16x32_bf16 v[176:179], a[88:91], a[0:3], v[176:179]\n"
"ds_read_b64_tr_b16 a[80:81], v16 offset:14528\n"
"s_waitcnt lgkmcnt(9)\n"
"v_mfma_f32_16x16x32_bf16 v[192:195], a[96:99], a[0:3], v[192:195]\n"
"ds_read_b64_tr_b16 a[82:83], v16 offset:18112\n"
"v_mfma_f32_16x16x32_bf16 v[192:195], a[100:103], a[16:19], v[192:195]\n"
"v_mfma_f32_16x16x32_bf16 v[196:199], a[100:103], a[20:23], v[196:199]\n"
"v_mfma_f32_16x16x32_bf16 v[196:199], a[96:99], a[4:7], v[196:199]\n"
"ds_read_b64_tr_b16 a[84:85], v16 offset:21696\n"
"v_mfma_f32_16x16x32_bf16 v[200:203], a[96:99], a[8:11], v[200:203]\n"
"ds_read_b64_tr_b16 a[86:87], v16 offset:25280\n"
"v_mfma_f32_16x16x32_bf16 v[200:203], a[100:103], a[24:27], v[200:203]\n"
"v_mfma_f32_16x16x32_bf16 v[204:207], a[100:103], a[28:31], v[204:207]\n"
"v_mfma_f32_16x16x32_bf16 v[204:207], a[96:99], a[12:15], v[204:207]\n"
"s_waitcnt lgkmcnt(8)\n"
"v_mfma_f32_16x16x32_bf16 v[220:223], a[64:67], a[12:15], v[220:223]\n"
"v_mfma_f32_16x16x32_bf16 v[220:223], a[68:71], a[28:31], v[220:223]\n"
"v_mfma_f32_16x16x32_bf16 v[216:219], a[68:71], a[24:27], v[216:219]\n"
"v_mfma_f32_16x16x32_bf16 v[216:219], a[64:67], a[8:11], v[216:219]\n"
"v_mfma_f32_16x16x32_bf16 v[212:215], a[64:67], a[4:7], v[212:215]\n"
"v_mfma_f32_16x16x32_bf16 v[212:215], a[68:71], a[20:23], v[212:215]\n"
"v_mfma_f32_16x16x32_bf16 v[208:211], a[68:71], a[16:19], v[208:211]\n"
"s_add_u32 m0, s78, 0x2100\n"
"s_add_u32 s80, s96, 3\n"
"v_mfma_f32_16x16x32_bf16 v[208:211], a[64:67], a[0:3], v[208:211]\n"
"s_waitcnt vmcnt(25) lgkmcnt(0)\n"
"s_barrier\n"
"v_mfma_f32_16x16x32_bf16 v[224:227], a[72:75], a[0:3], v[224:227]\n"
"ds_read_b64_tr_b16 a[88:89], v16 offset:28672\n"
"s_and_b32 s80, s80, 3\n"
"v_mfma_f32_16x16x32_bf16 v[224:227], a[76:79], a[16:19], v[224:227]\n"
"v_mfma_f32_16x16x32_bf16 v[228:231], a[76:79], a[20:23], v[228:231]\n"
"ds_read_b64_tr_b16 a[90:91], v16 offset:32256\n"
"s_add_u32 s80, s80, 8\n"
"v_mfma_f32_16x16x32_bf16 v[228:231], a[72:75], a[4:7], v[228:231]\n"
"s_lshl_b32 s80, s80, 7\n"
"v_mfma_f32_16x16x32_bf16 v[232:235], a[72:75], a[8:11], v[232:235]\n"
"ds_read_b64_tr_b16 a[92:93], v16 offset:35840\n"
"v_mfma_f32_16x16x32_bf16 v[232:235], a[76:79], a[24:27], v[232:235]\n"
"buffer_load_dwordx4 v8, s[36:39], s80 offen offset:0 lds\n"
"v_mfma_f32_16x16x32_bf16 v[236:239], a[76:79], a[28:31], v[236:239]\n"
"ds_read_b64_tr_b16 a[94:95], v16 offset:39424\n"
"v_mfma_f32_16x16x32_bf16 v[236:239], a[72:75], a[12:15], v[236:239]\n"
"v_mfma_f32_16x16x32_bf16 v[252:255], a[80:83], a[12:15], v[252:255]\n"
"ds_read_b64_tr_b16 a[96:97], v16 offset:43008\n"
"v_mfma_f32_16x16x32_bf16 v[252:255], a[84:87], a[28:31], v[252:255]\n"
"buffer_load_dwordx4 v9, s[36:39], s80 offen offset:1024 lds\n"
"v_mfma_f32_16x16x32_bf16 v[248:251], a[84:87], a[24:27], v[248:251]\n"
"ds_read_b64_tr_b16 a[98:99], v16 offset:46592\n"
"v_mfma_f32_16x16x32_bf16 v[248:251], a[80:83], a[8:11], v[248:251]\n"
"v_mfma_f32_16x16x32_bf16 v[244:247], a[80:83], a[4:7], v[244:247]\n"
"ds_read_b64_tr_b16 a[100:101], v16 offset:50176\n"
"v_mfma_f32_16x16x32_bf16 v[244:247], a[84:87], a[20:23], v[244:247]\n"
"buffer_load_dwordx4 v10, s[36:39], s80 offen offset:2048 lds\n"
"v_mfma_f32_16x16x32_bf16 v[240:243], a[84:87], a[16:19], v[240:243]\n"
"ds_read_b64_tr_b16 a[102:103], v16 offset:53760\n"
"v_mfma_f32_16x16x32_bf16 v[240:243], a[80:83], a[0:3], v[240:243]\n"
"s_waitcnt lgkmcnt(4)\n"
"v_mfma_f32_16x16x32_bf16 v[32:35], a[88:91], a[32:35], v[32:35]\n"
"ds_read_b64_tr_b16 a[64:65], v16 offset:28704\n"
"v_mfma_f32_16x16x32_bf16 v[32:35], a[92:95], a[48:51], v[32:35]\n"
"buffer_load_dwordx4 v11, s[36:39], s80 offen offset:3072 lds\n"
"v_mfma_f32_16x16x32_bf16 v[36:39], a[92:95], a[52:55], v[36:39]\n"
"ds_read_b64_tr_b16 a[66:67], v16 offset:32288\n"
"v_mfma_f32_16x16x32_bf16 v[36:39], a[88:91], a[36:39], v[36:39]\n"
"ds_read_b64_tr_b16 a[68:69], v16 offset:35872\n"
"s_add_u32 m0, s78, 0x3100\n"
"v_mfma_f32_16x16x32_bf16 v[40:43], a[88:91], a[40:43], v[40:43]\n"
"s_or_b32 s80, s80, 0x10000\n"
"v_mfma_f32_16x16x32_bf16 v[40:43], a[92:95], a[56:59], v[40:43]\n"
"buffer_load_dwordx4 v8, s[36:39], s80 offen offset:0 lds\n"
"v_mfma_f32_16x16x32_bf16 v[44:47], a[92:95], a[60:63], v[44:47]\n"
"ds_read_b64_tr_b16 a[70:71], v16 offset:39456\n"
"v_mfma_f32_16x16x32_bf16 v[44:47], a[88:91], a[44:47], v[44:47]\n"
"ds_read_b64_tr_b16 a[72:73], v16 offset:43040\n"
"s_waitcnt lgkmcnt(5)\n"
"v_mfma_f32_16x16x32_bf16 v[60:63], a[96:99], a[44:47], v[60:63]\n"
"ds_read_b64_tr_b16 a[74:75], v16 offset:46624\n"
"v_mfma_f32_16x16x32_bf16 v[60:63], a[100:103], a[60:63], v[60:63]\n"
"buffer_load_dwordx4 v9, s[36:39], s80 offen offset:1024 lds\n"
"v_mfma_f32_16x16x32_bf16 v[56:59], a[100:103], a[56:59], v[56:59]\n"
"v_mfma_f32_16x16x32_bf16 v[56:59], a[96:99], a[40:43], v[56:59]\n"
"ds_read_b64_tr_b16 a[76:77], v16 offset:50208\n"
"v_mfma_f32_16x16x32_bf16 v[52:55], a[96:99], a[36:39], v[52:55]\n"
"ds_read_b64_tr_b16 a[78:79], v16 offset:53792\n"
"s_waitcnt vmcnt(22)\n"
"v_mfma_f32_16x16x32_bf16 v[52:55], a[100:103], a[52:55], v[52:55]\n"
"buffer_load_dwordx4 v10, s[36:39], s80 offen offset:2048 lds\n"
"v_mfma_f32_16x16x32_bf16 v[48:51], a[100:103], a[48:51], v[48:51]\n"
"ds_read_b64_tr_b16 a[80:81], v16 offset:28736\n"
"v_mfma_f32_16x16x32_bf16 v[48:51], a[96:99], a[32:35], v[48:51]\n"
"ds_read_b128 a[0:3], v18 offset:0\n"
"s_waitcnt lgkmcnt(6)\n"
"v_mfma_f32_16x16x32_bf16 v[64:67], a[64:67], a[32:35], v[64:67]\n"
"ds_read_b64_tr_b16 a[82:83], v16 offset:32320\n"
"v_mfma_f32_16x16x32_bf16 v[64:67], a[68:71], a[48:51], v[64:67]\n"
"buffer_load_dwordx4 v11, s[36:39], s80 offen offset:3072 lds\n"
"v_mfma_f32_16x16x32_bf16 v[68:71], a[68:71], a[52:55], v[68:71]\n"
"ds_read_b64_tr_b16 a[84:85], v16 offset:35904\n"
"s_add_u32 m0, s77, 0x0\n"
"v_mfma_f32_16x16x32_bf16 v[68:71], a[64:67], a[36:39], v[68:71]\n"
"ds_read_b64_tr_b16 a[86:87], v16 offset:39488\n"
"s_add_u32 s79, s96, 3\n"
"v_mfma_f32_16x16x32_bf16 v[72:75], a[64:67], a[40:43], v[72:75]\n"
"ds_read_b128 a[4:7], v18 offset:2048\n"
"s_and_b32 s79, s79, 3\n"
"v_mfma_f32_16x16x32_bf16 v[72:75], a[68:71], a[56:59], v[72:75]\n"
"v_mfma_f32_16x16x32_bf16 v[76:79], a[68:71], a[60:63], v[76:79]\n"
"ds_read_b64_tr_b16 a[88:89], v16 offset:43072\n"
"s_add_u32 s79, s79, 8\n"
"v_mfma_f32_16x16x32_bf16 v[76:79], a[64:67], a[44:47], v[76:79]\n"
"ds_read_b64_tr_b16 a[90:91], v16 offset:46656\n"
"s_waitcnt lgkmcnt(8)\n"
"v_mfma_f32_16x16x32_bf16 v[92:95], a[72:75], a[44:47], v[92:95]\n"
"ds_read_b64_tr_b16 a[92:93], v16 offset:50240\n"
"s_mul_i32 s79, s79, 0x70000\n"
"v_mfma_f32_16x16x32_bf16 v[92:95], a[76:79], a[60:63], v[92:95]\n"
"buffer_load_dwordx4 v1, s[40:43], s79 offen offset:0 lds\n"
"v_mfma_f32_16x16x32_bf16 v[88:91], a[76:79], a[56:59], v[88:91]\n"
"ds_read_b128 a[8:11], v18 offset:4096\n"
"v_mfma_f32_16x16x32_bf16 v[88:91], a[72:75], a[40:43], v[88:91]\n"
"ds_read_b64_tr_b16 a[94:95], v16 offset:53824\n"
"v_mfma_f32_16x16x32_bf16 v[84:87], a[72:75], a[36:39], v[84:87]\n"
"ds_read_b64_tr_b16 a[96:97], v16 offset:28768\n"
"v_mfma_f32_16x16x32_bf16 v[84:87], a[76:79], a[52:55], v[84:87]\n"
"buffer_load_dwordx4 v2, s[40:43], s79 offen offset:1024 lds\n"
"v_mfma_f32_16x16x32_bf16 v[80:83], a[76:79], a[48:51], v[80:83]\n"
"ds_read_b128 a[12:15], v18 offset:6144\n"
"v_mfma_f32_16x16x32_bf16 v[80:83], a[72:75], a[32:35], v[80:83]\n"
"ds_read_b64_tr_b16 a[98:99], v16 offset:32352\n"
"s_waitcnt lgkmcnt(9)\n"
"v_mfma_f32_16x16x32_bf16 v[96:99], a[80:83], a[32:35], v[96:99]\n"
"ds_read_b64_tr_b16 a[100:101], v16 offset:35936\n"
"v_mfma_f32_16x16x32_bf16 v[96:99], a[84:87], a[48:51], v[96:99]\n"
"buffer_load_dwordx4 v3, s[40:43], s79 offen offset:2048 lds\n"
"v_mfma_f32_16x16x32_bf16 v[100:103], a[84:87], a[52:55], v[100:103]\n"
"ds_read_b64_tr_b16 a[102:103], v16 offset:39520\n"
"v_mfma_f32_16x16x32_bf16 v[100:103], a[80:83], a[36:39], v[100:103]\n"
"ds_read_b128 a[16:19], v19 offset:0\n"
"v_mfma_f32_16x16x32_bf16 v[104:107], a[80:83], a[40:43], v[104:107]\n"
"ds_read_b64_tr_b16 a[64:65], v16 offset:43104\n"
"v_mfma_f32_16x16x32_bf16 v[104:107], a[84:87], a[56:59], v[104:107]\n"
"buffer_load_dwordx4 v4, s[40:43], s79 offen offset:3072 lds\n"
"v_mfma_f32_16x16x32_bf16 v[108:111], a[84:87], a[60:63], v[108:111]\n"
"ds_read_b64_tr_b16 a[66:67], v16 offset:46688\n"
"v_mfma_f32_16x16x32_bf16 v[108:111], a[80:83], a[44:47], v[108:111]\n"
"ds_read_b64_tr_b16 a[68:69], v16 offset:50272\n"
"s_waitcnt lgkmcnt(9)\n"
"v_mfma_f32_16x16x32_bf16 v[124:127], a[88:91], a[44:47], v[124:127]\n"
"ds_read_b128 a[20:23], v19 offset:2048\n"
"s_add_u32 m0, s77, 0x1000\n"
"v_mfma_f32_16x16x32_bf16 v[124:127], a[92:95], a[60:63], v[124:127]\n"
"buffer_load_dwordx4 v5, s[40:43], s79 offen offset:0 lds\n"
"v_mfma_f32_16x16x32_bf16 v[120:123], a[92:95], a[56:59], v[120:123]\n"
"ds_read_b64_tr_b16 a[70:71], v16 offset:53856\n"
"v_mfma_f32_16x16x32_bf16 v[120:123], a[88:91], a[40:43], v[120:123]\n"
"ds_read_b64_tr_b16 a[72:73], v16 offset:28800\n"
"v_mfma_f32_16x16x32_bf16 v[116:119], a[88:91], a[36:39], v[116:119]\n"
"ds_read_b64_tr_b16 a[74:75], v16 offset:32384\n"
"v_mfma_f32_16x16x32_bf16 v[116:119], a[92:95], a[52:55], v[116:119]\n"
"buffer_load_dwordx4 v6, s[40:43], s79 offen offset:1024 lds\n"
"v_mfma_f32_16x16x32_bf16 v[112:115], a[92:95], a[48:51], v[112:115]\n"
"ds_read_b128 a[24:27], v19 offset:4096\n"
"v_mfma_f32_16x16x32_bf16 v[112:115], a[88:91], a[32:35], v[112:115]\n"
"ds_read_b64_tr_b16 a[76:77], v16 offset:35968\n"
"s_waitcnt lgkmcnt(10)\n"
"v_mfma_f32_16x16x32_bf16 v[128:131], a[96:99], a[32:35], v[128:131]\n"
"ds_read_b64_tr_b16 a[78:79], v16 offset:39552\n"
"v_mfma_f32_16x16x32_bf16 v[128:131], a[100:103], a[48:51], v[128:131]\n"
"buffer_load_dwordx4 v7, s[40:43], s79 offen offset:2048 lds\n"
"v_mfma_f32_16x16x32_bf16 v[132:135], a[100:103], a[52:55], v[132:135]\n"
"ds_read_b64_tr_b16 a[80:81], v16 offset:43136\n"
"v_mfma_f32_16x16x32_bf16 v[132:135], a[96:99], a[36:39], v[132:135]\n"
"ds_read_b128 a[28:31], v19 offset:6144\n"
"v_mfma_f32_16x16x32_bf16 v[136:139], a[96:99], a[40:43], v[136:139]\n"
"ds_read_b64_tr_b16 a[82:83], v16 offset:46720\n"
"s_add_u32 m0, s78, 0x0\n"
"v_mfma_f32_16x16x32_bf16 v[136:139], a[100:103], a[56:59], v[136:139]\n"
"v_mfma_f32_16x16x32_bf16 v[140:143], a[100:103], a[60:63], v[140:143]\n"
"ds_read_b64_tr_b16 a[84:85], v16 offset:50304\n"
"s_add_u32 s80, s96, 4\n"
"v_mfma_f32_16x16x32_bf16 v[140:143], a[96:99], a[44:47], v[140:143]\n"
"ds_read_b64_tr_b16 a[86:87], v16 offset:53888\n"
"s_waitcnt lgkmcnt(10)\n"
"v_mfma_f32_16x16x32_bf16 v[156:159], a[64:67], a[44:47], v[156:159]\n"
"s_waitcnt lgkmcnt(3)\n"
"s_and_b32 s80, s80, 3\n"
"v_mfma_f32_16x16x32_bf16 v[156:159], a[68:71], a[60:63], v[156:159]\n"
"v_mfma_f32_16x16x32_bf16 v[152:155], a[68:71], a[56:59], v[152:155]\n"
"ds_read_b64_tr_b16 a[88:89], v16 offset:28832\n"
"s_add_u32 s80, s80, 8\n"
"v_mfma_f32_16x16x32_bf16 v[152:155], a[64:67], a[40:43], v[152:155]\n"
"ds_read_b64_tr_b16 a[90:91], v16 offset:32416\n"
"s_lshl_b32 s80, s80, 7\n"
"v_mfma_f32_16x16x32_bf16 v[148:151], a[64:67], a[36:39], v[148:151]\n"
"buffer_load_dwordx4 v8, s[36:39], s80 offen offset:0 lds\n"
"v_mfma_f32_16x16x32_bf16 v[148:151], a[68:71], a[52:55], v[148:151]\n"
"v_mfma_f32_16x16x32_bf16 v[144:147], a[68:71], a[48:51], v[144:147]\n"
"ds_read_b64_tr_b16 a[92:93], v16 offset:36000\n"
"v_mfma_f32_16x16x32_bf16 v[144:147], a[64:67], a[32:35], v[144:147]\n"
"ds_read_b64_tr_b16 a[94:95], v16 offset:39584\n"
"s_waitcnt vmcnt(24)\n"
"v_accvgpr_read_b32 v17, a201\n"
"s_nop 0\n"
"v_readfirstlane_b32 s85, v17\n"
"s_nop 3\n"
"s_cmp_eq_u32 s85, s13\n"
"s_cbranch_scc1 .Lfk_hd2\n"
"s_mov_b32 s35, 0x10000\n"
".Lfk_hs2:\n"
"v_lshl_add_u32 v17, v0, 2, s28\n"
"buffer_load_dword a200, v17, s[64:67], 0 offen offset:1024 sc1\n"
"s_waitcnt vmcnt(0)\n"
"buffer_store_dword a200, v17, s[64:67], 0 offen offset:2048 sc1\n"
"s_waitcnt vmcnt(0)\n"
"v_mov_b32 v17, s92\n"
"buffer_load_dword v17, v17, s[64:67], 0 offen offset:2048 sc1\n"
"s_waitcnt vmcnt(0)\n"
"v_readfirstlane_b32 s85, v17\n"
"s_nop 3\n"
"s_cmp_eq_u32 s85, s13\n"
"s_cbranch_scc1 .Lfk_hd2\n"
"s_sub_u32 s35, s35, 1\n"
"s_cmp_lg_u32 s35, 0\n"
"s_cbranch_scc1 .Lfk_hs2\n"
".Lfk_hd2:\n"
"v_mov_b32 v17, s92\n"
"v_accvgpr_write_b32 a201, 1\n"
"buffer_store_dword a201, v17, s[64:67], 0 offen offset:3072 sc1\n"
"v_mfma_f32_16x16x32_bf16 v[160:163], a[72:75], a[32:35], v[160:163]\n"
"ds_read_b64_tr_b16 a[96:97], v16 offset:43168\n"
"v_mfma_f32_16x16x32_bf16 v[160:163], a[76:79], a[48:51], v[160:163]\n"
"buffer_load_dwordx4 v9, s[36:39], s80 offen offset:1024 lds\n"
"v_mfma_f32_16x16x32_bf16 v[164:167], a[76:79], a[52:55], v[164:167]\n"
"v_mfma_f32_16x16x32_bf16 v[164:167], a[72:75], a[36:39], v[164:167]\n"
"ds_read_b64_tr_b16 a[98:99], v16 offset:46752\n"
"v_mfma_f32_16x16x32_bf16 v[168:171], a[72:75], a[40:43], v[168:171]\n"
"ds_read_b64_tr_b16 a[100:101], v16 offset:50336\n"
"v_mfma_f32_16x16x32_bf16 v[168:171], a[76:79], a[56:59], v[168:171]\n"
"buffer_load_dwordx4 v10, s[36:39], s80 offen offset:2048 lds\n"
"v_mfma_f32_16x16x32_bf16 v[172:175], a[76:79], a[60:63], v[172:175]\n"
"ds_read_b64_tr_b16 a[102:103], v16 offset:53920\n"
"s_waitcnt vmcnt(19)\n"
"v_mfma_f32_16x16x32_bf16 v[172:175], a[72:75], a[44:47], v[172:175]\n"
"v_lshrrev_b32 v30, 4, v0\n"
"s_waitcnt lgkmcnt(8)\n"
"v_mfma_f32_16x16x32_bf16 v[188:191], a[80:83], a[44:47], v[188:191]\n"
"ds_read_b64_tr_b16 a[64:65], v16 offset:28864\n"
"v_or_b32 v30, s12, v30\n"
"v_mfma_f32_16x16x32_bf16 v[188:191], a[84:87], a[60:63], v[188:191]\n"
"buffer_load_dwordx4 v11, s[36:39], s80 offen offset:3072 lds\n"
"v_mfma_f32_16x16x32_bf16 v[184:187], a[84:87], a[56:59], v[184:187]\n"
"ds_read_b64_tr_b16 a[66:67], v16 offset:32448\n"
"v_cmp_ne_u32 vcc, 0, v30\n"
"v_mfma_f32_16x16x32_bf16 v[184:187], a[80:83], a[40:43], v[184:187]\n"
"ds_read_b64_tr_b16 a[68:69], v16 offset:36032\n"
"s_add_u32 m0, s78, 0x1000\n"
"v_mfma_f32_16x16x32_bf16 v[180:183], a[80:83], a[36:39], v[180:183]\n"
"s_or_b32 s80, s80, 0x10000\n"
"s_lshl_b32 s81, s11, 18\n"
"v_mfma_f32_16x16x32_bf16 v[180:183], a[84:87], a[52:55], v[180:183]\n"
"buffer_load_dwordx4 v8, s[36:39], s80 offen offset:0 lds\n"
"v_mfma_f32_16x16x32_bf16 v[176:179], a[84:87], a[48:51], v[176:179]\n"
"ds_read_b64_tr_b16 a[70:71], v16 offset:39616\n"
"s_lshl_b32 s82, s12, 6\n"
"v_mfma_f32_16x16x32_bf16 v[176:179], a[80:83], a[32:35], v[176:179]\n"
"ds_read_b64_tr_b16 a[72:73], v16 offset:43200\n"
"s_waitcnt lgkmcnt(9)\n"
"v_mfma_f32_16x16x32_bf16 v[192:195], a[88:91], a[32:35], v[192:195]\n"
"ds_read_b64_tr_b16 a[74:75], v16 offset:46784\n"
"s_sub_u32 s81, s81, s82\n"
"v_mfma_f32_16x16x32_bf16 v[192:195], a[92:95], a[48:51], v[192:195]\n"
"buffer_load_dwordx4 v9, s[36:39], s80 offen offset:1024 lds\n"
"v_mfma_f32_16x16x32_bf16 v[196:199], a[92:95], a[52:55], v[196:199]\n"
"s_add_u32 s81, s81, 0x100fc0\n"
"v_accvgpr_read_b32 v14, a142\n"
"v_mfma_f32_16x16x32_bf16 v[196:199], a[88:91], a[36:39], v[196:199]\n"
"ds_read_b64_tr_b16 a[76:77], v16 offset:50368\n"
"v_lshlrev_b32 v13, 16, v14\n"
"v_mfma_f32_16x16x32_bf16 v[200:203], a[88:91], a[40:43], v[200:203]\n"
"ds_read_b64_tr_b16 a[78:79], v16 offset:53952\n"
"v_and_b32 v14, 0xffff0000, v14\n"
"v_mfma_f32_16x16x32_bf16 v[200:203], a[92:95], a[56:59], v[200:203]\n"
"buffer_load_dwordx4 v10, s[36:39], s80 offen offset:2048 lds\n"
"v_mfma_f32_16x16x32_bf16 v[204:207], a[92:95], a[60:63], v[204:207]\n"
"v_accvgpr_read_b32 v28, a143\n"
"v_lshlrev_b32 v15, 16, v28\n"
"v_mfma_f32_16x16x32_bf16 v[204:207], a[88:91], a[44:47], v[204:207]\n"
"v_and_b32 v28, 0xffff0000, v28\n"
"s_waitcnt lgkmcnt(8)\n"
"v_mfma_f32_16x16x32_bf16 v[220:223], a[96:99], a[44:47], v[220:223]\n"
"v_add_u32 v16, 0xe000, v16\n"
"v_accvgpr_read_b32 v24, a104\n"
"v_mfma_f32_16x16x32_bf16 v[220:223], a[100:103], a[60:63], v[220:223]\n"
"buffer_load_dwordx4 v11, s[36:39], s80 offen offset:3072 lds\n"
"v_mfma_f32_16x16x32_bf16 v[216:219], a[100:103], a[56:59], v[216:219]\n"
"v_lshlrev_b32 v24, 16, v24\n"
"v_cndmask_b32 v24, 0, v24, vcc\n"
"v_mfma_f32_16x16x32_bf16 v[216:219], a[96:99], a[40:43], v[216:219]\n"
"s_add_u32 m0, s77, 0x7000\n"
"s_add_u32 s79, s96, 4\n"
"v_mfma_f32_16x16x32_bf16 v[212:215], a[96:99], a[36:39], v[212:215]\n"
"s_and_b32 s79, s79, 3\n"
"s_add_u32 s79, s79, 8\n"
"v_mfma_f32_16x16x32_bf16 v[212:215], a[100:103], a[52:55], v[212:215]\n"
"v_accvgpr_read_b32 v25, a106\n"
"v_lshlrev_b32 v25, 16, v25\n"
"v_mfma_f32_16x16x32_bf16 v[208:211], a[100:103], a[48:51], v[208:211]\n"
"s_mul_i32 s79, s79, 0x70000\n"
"v_cndmask_b32 v25, 0, v25, vcc\n"
"v_mfma_f32_16x16x32_bf16 v[208:211], a[96:99], a[32:35], v[208:211]\n"
"s_waitcnt lgkmcnt(0)\n"
"s_barrier\n"
"v_mfma_f32_16x16x32_bf16 v[224:227], a[64:67], a[32:35], v[224:227]\n"
"ds_read_b64_tr_b16 a[80:81], v16 offset:0\n"
"v_accvgpr_read_b32 v26, a108\n"
"v_mfma_f32_16x16x32_bf16 v[224:227], a[68:71], a[48:51], v[224:227]\n"
"buffer_load_dwordx4 v1, s[40:43], s79 offen offset:0 lds\n"
"v_mfma_f32_16x16x32_bf16 v[228:231], a[68:71], a[52:55], v[228:231]\n"
"ds_read_b64_tr_b16 a[82:83], v16 offset:3584\n"
"v_lshlrev_b32 v26, 16, v26\n"
"v_mfma_f32_16x16x32_bf16 v[228:231], a[64:67], a[36:39], v[228:231]\n"
"v_cndmask_b32 v26, 0, v26, vcc\n"
"v_accvgpr_read_b32 v27, a110\n"
"v_mfma_f32_16x16x32_bf16 v[232:235], a[64:67], a[40:43], v[232:235]\n"
"ds_read_b64_tr_b16 a[84:85], v16 offset:7168\n"
"v_lshlrev_b32 v27, 16, v27\n"
"v_mfma_f32_16x16x32_bf16 v[232:235], a[68:71], a[56:59], v[232:235]\n"
"buffer_load_dwordx4 v2, s[40:43], s79 offen offset:1024 lds\n"
"v_mfma_f32_16x16x32_bf16 v[236:239], a[68:71], a[60:63], v[236:239]\n"
"ds_read_b64_tr_b16 a[86:87], v16 offset:10752\n"
"v_mul_f32 v30, v28, v24\n"
"v_mfma_f32_16x16x32_bf16 v[236:239], a[64:67], a[44:47], v[236:239]\n"
"v_accvgpr_read_b32 v24, a126\n"
"v_mfma_f32_16x16x32_bf16 v[252:255], a[72:75], a[44:47], v[252:255]\n"
"ds_read_b64_tr_b16 a[88:89], v16 offset:14336\n"
"v_lshlrev_b32 v24, 16, v24\n"
"v_mfma_f32_16x16x32_bf16 v[252:255], a[76:79], a[60:63], v[252:255]\n"
"buffer_load_dwordx4 v3, s[40:43], s79 offen offset:2048 lds\n"
"v_mfma_f32_16x16x32_bf16 v[248:251], a[76:79], a[56:59], v[248:251]\n"
"ds_read_b64_tr_b16 a[90:91], v16 offset:17920\n"
"v_fmac_f32 v30, v15, v25\n"
"v_mfma_f32_16x16x32_bf16 v[248:251], a[72:75], a[40:43], v[248:251]\n"
"v_fmac_f32 v30, v14, v26\n"
"v_fmac_f32 v30, v13, v27\n"
"v_mfma_f32_16x16x32_bf16 v[244:247], a[72:75], a[36:39], v[244:247]\n"
"ds_read_b64_tr_b16 a[92:93], v16 offset:21504\n"
"v_mul_f32 v30, v30, v24\n"
"v_mfma_f32_16x16x32_bf16 v[244:247], a[76:79], a[52:55], v[244:247]\n"
"buffer_load_dwordx4 v4, s[40:43], s79 offen offset:3072 lds\n"
"v_mfma_f32_16x16x32_bf16 v[240:243], a[76:79], a[48:51], v[240:243]\n"
"ds_read_b64_tr_b16 a[94:95], v16 offset:25088\n"
"v_accvgpr_read_b32 v24, a112\n"
"v_mfma_f32_16x16x32_bf16 v[240:243], a[72:75], a[32:35], v[240:243]\n"
"v_lshlrev_b32 v24, 16, v24\n"
"v_mul_f32 v31, v28, v25\n"
"s_waitcnt lgkmcnt(4)\n"
"v_mfma_f32_16x16x32_bf16 v[32:35], a[80:83], a[0:3], v[32:35]\n"
"ds_read_b64_tr_b16 a[96:97], v16 offset:32\n"
"s_add_u32 m0, s77, 0x8000\n"
"v_mfma_f32_16x16x32_bf16 v[32:35], a[84:87], a[16:19], v[32:35]\n"
"buffer_load_dwordx4 v5, s[40:43], s79 offen offset:0 lds\n"
"v_mfma_f32_16x16x32_bf16 v[36:39], a[84:87], a[20:23], v[36:39]\n"
"ds_read_b64_tr_b16 a[98:99], v16 offset:3616\n"
"v_accvgpr_read_b32 v25, a128\n"
"v_mfma_f32_16x16x32_bf16 v[36:39], a[80:83], a[4:7], v[36:39]\n"
"ds_read_b64_tr_b16 a[100:101], v16 offset:7200\n"
"v_lshlrev_b32 v25, 16, v25\n"
".Lfk_hopw1:\n"
".Lfk_hop2280:\n"
"s_cbranch_execz .Lfk_hop2864\n"
"v_mfma_f32_16x16x32_bf16 v[40:43], a[80:83], a[8:11], v[40:43]\n"
"v_fmac_f32 v31, v15, v26\n"
"v_fmac_f32 v31, v14, v27\n"
"v_mfma_f32_16x16x32_bf16 v[40:43], a[84:87], a[24:27], v[40:43]\n"
"buffer_load_dwordx4 v6, s[40:43], s79 offen offset:1024 lds\n"
"v_mfma_f32_16x16x32_bf16 v[44:47], a[84:87], a[28:31], v[44:47]\n"
"ds_read_b64_tr_b16 a[102:103], v16 offset:10784\n"
"v_fmac_f32 v31, v13, v24\n"
"v_mfma_f32_16x16x32_bf16 v[44:47], a[80:83], a[12:15], v[44:47]\n"
"ds_read_b64_tr_b16 a[64:65], v16 offset:14368\n"
"s_waitcnt lgkmcnt(5)\n"
"v_mfma_f32_16x16x32_bf16 v[60:63], a[88:91], a[12:15], v[60:63]\n"
"ds_read_b64_tr_b16 a[66:67], v16 offset:17952\n"
"v_mul_f32 v31, v31, v25\n"
"v_mfma_f32_16x16x32_bf16 v[60:63], a[92:95], a[28:31], v[60:63]\n"
"buffer_load_dwordx4 v7, s[40:43], s79 offen offset:2048 lds\n"
"v_mfma_f32_16x16x32_bf16 v[56:59], a[92:95], a[24:27], v[56:59]\n"
"v_cvt_pk_bf16_f32 v23, v31, v30\n"
"v_accvgpr_read_b32 v25, a114\n"
"v_mfma_f32_16x16x32_bf16 v[56:59], a[88:91], a[8:11], v[56:59]\n"
"ds_read_b64_tr_b16 a[68:69], v16 offset:21536\n"
"v_lshlrev_b32 v25, 16, v25\n"
"v_mfma_f32_16x16x32_bf16 v[52:55], a[88:91], a[4:7], v[52:55]\n"
"ds_read_b64_tr_b16 a[70:71], v16 offset:25120\n"
"s_waitcnt vmcnt(23)\n"
"v_mfma_f32_16x16x32_bf16 v[52:55], a[92:95], a[20:23], v[52:55]\n"
"v_mul_f32 v30, v28, v26\n"
"v_accvgpr_read_b32 v26, a130\n"
"v_mfma_f32_16x16x32_bf16 v[48:51], a[92:95], a[16:19], v[48:51]\n"
"ds_read_b64_tr_b16 a[72:73], v16 offset:64\n"
"v_lshlrev_b32 v26, 16, v26\n"
"v_mfma_f32_16x16x32_bf16 v[48:51], a[88:91], a[0:3], v[48:51]\n"
"ds_read_b128 a[32:35], v18 offset:8448\n"
"s_waitcnt lgkmcnt(6)\n"
"v_mfma_f32_16x16x32_bf16 v[64:67], a[96:99], a[0:3], v[64:67]\n"
"ds_read_b64_tr_b16 a[74:75], v16 offset:3648\n"
"v_fmac_f32 v30, v15, v27\n"
"v_mfma_f32_16x16x32_bf16 v[64:67], a[100:103], a[16:19], v[64:67]\n"
"v_fmac_f32 v30, v14, v24\n"
"v_fmac_f32 v30, v13, v25\n"
"v_mfma_f32_16x16x32_bf16 v[68:71], a[100:103], a[20:23], v[68:71]\n"
"ds_read_b64_tr_b16 a[76:77], v16 offset:7232\n"
"v_mul_f32 v30, v30, v26\n"
"v_mfma_f32_16x16x32_bf16 v[68:71], a[96:99], a[4:7], v[68:71]\n"
"ds_read_b64_tr_b16 a[78:79], v16 offset:10816\n"
"v_accvgpr_read_b32 v26, a116\n"
"v_mfma_f32_16x16x32_bf16 v[72:75], a[96:99], a[8:11], v[72:75]\n"
"ds_read_b128 a[36:39], v18 offset:10496\n"
"v_lshlrev_b32 v26, 16, v26\n"
"v_mfma_f32_16x16x32_bf16 v[72:75], a[100:103], a[24:27], v[72:75]\n"
"v_mul_f32 v31, v28, v27\n"
"v_accvgpr_read_b32 v27, a132\n"
"v_mfma_f32_16x16x32_bf16 v[76:79], a[100:103], a[28:31], v[76:79]\n"
"ds_read_b64_tr_b16 a[80:81], v16 offset:14400\n"
"v_lshlrev_b32 v27, 16, v27\n"
"v_mfma_f32_16x16x32_bf16 v[76:79], a[96:99], a[12:15], v[76:79]\n"
"ds_read_b64_tr_b16 a[82:83], v16 offset:17984\n"
"s_waitcnt lgkmcnt(8)\n"
"v_mfma_f32_16x16x32_bf16 v[92:95], a[64:67], a[12:15], v[92:95]\n"
"ds_read_b64_tr_b16 a[84:85], v16 offset:21568\n"
"v_fmac_f32 v31, v15, v24\n"
"v_mfma_f32_16x16x32_bf16 v[92:95], a[68:71], a[28:31], v[92:95]\n"
"v_fmac_f32 v31, v14, v25\n"
"v_fmac_f32 v31, v13, v26\n"
"v_mfma_f32_16x16x32_bf16 v[88:91], a[68:71], a[24:27], v[88:91]\n"
"ds_read_b128 a[40:43], v18 offset:12544\n"
"v_mul_f32 v31, v31, v27\n"
"v_mfma_f32_16x16x32_bf16 v[88:91], a[64:67], a[8:11], v[88:91]\n"
"ds_read_b64_tr_b16 a[86:87], v16 offset:25152\n"
"v_cvt_pk_bf16_f32 v22, v31, v30\n"
"v_mfma_f32_16x16x32_bf16 v[84:87], a[64:67], a[4:7], v[84:87]\n"
"ds_read_b64_tr_b16 a[88:89], v16 offset:96\n"
"v_accvgpr_read_b32 v27, a118\n"
"v_mfma_f32_16x16x32_bf16 v[84:87], a[68:71], a[20:23], v[84:87]\n"
"v_lshlrev_b32 v27, 16, v27\n"
"v_mul_f32 v30, v28, v24\n"
"v_mfma_f32_16x16x32_bf16 v[80:83], a[68:71], a[16:19], v[80:83]\n"
"ds_read_b128 a[44:47], v18 offset:14592\n"
"v_accvgpr_read_b32 v24, a134\n"
"v_mfma_f32_16x16x32_bf16 v[80:83], a[64:67], a[0:3], v[80:83]\n"
"ds_read_b64_tr_b16 a[90:91], v16 offset:3680\n"
"s_waitcnt lgkmcnt(9)\n"
"v_mfma_f32_16x16x32_bf16 v[96:99], a[72:75], a[0:3], v[96:99]\n"
"ds_read_b64_tr_b16 a[92:93], v16 offset:7264\n"
"v_lshlrev_b32 v24, 16, v24\n"
"v_mfma_f32_16x16x32_bf16 v[96:99], a[76:79], a[16:19], v[96:99]\n"
"v_fmac_f32 v30, v15, v25\n"
"v_fmac_f32 v30, v14, v26\n"
"v_mfma_f32_16x16x32_bf16 v[100:103], a[76:79], a[20:23], v[100:103]\n"
"ds_read_b64_tr_b16 a[94:95], v16 offset:10848\n"
"v_fmac_f32 v30, v13, v27\n"
"v_mfma_f32_16x16x32_bf16 v[100:103], a[72:75], a[4:7], v[100:103]\n"
"ds_read_b128 a[48:51], v19 offset:8448\n"
"v_mul_f32 v30, v30, v24\n"
"v_mfma_f32_16x16x32_bf16 v[104:107], a[72:75], a[8:11], v[104:107]\n"
"ds_read_b64_tr_b16 a[96:97], v16 offset:14432\n"
"v_accvgpr_read_b32 v24, a120\n"
"v_mfma_f32_16x16x32_bf16 v[104:107], a[76:79], a[24:27], v[104:107]\n"
"v_lshlrev_b32 v24, 16, v24\n"
"v_mul_f32 v31, v28, v25\n"
"v_mfma_f32_16x16x32_bf16 v[108:111], a[76:79], a[28:31], v[108:111]\n"
"ds_read_b64_tr_b16 a[98:99], v16 offset:18016\n"
"v_accvgpr_read_b32 v25, a136\n"
"v_mfma_f32_16x16x32_bf16 v[108:111], a[72:75], a[12:15], v[108:111]\n"
"ds_read_b64_tr_b16 a[100:101], v16 offset:21600\n"
"s_waitcnt lgkmcnt(9)\n"
"v_mfma_f32_16x16x32_bf16 v[124:127], a[80:83], a[12:15], v[124:127]\n"
"ds_read_b128 a[52:55], v19 offset:10496\n"
"v_lshlrev_b32 v25, 16, v25\n"
"v_mfma_f32_16x16x32_bf16 v[124:127], a[84:87], a[28:31], v[124:127]\n"
"v_fmac_f32 v31, v15, v26\n"
"v_fmac_f32 v31, v14, v27\n"
"v_mfma_f32_16x16x32_bf16 v[120:123], a[84:87], a[24:27], v[120:123]\n"
"ds_read_b64_tr_b16 a[102:103], v16 offset:25184\n"
"v_fmac_f32 v31, v13, v24\n"
"v_mfma_f32_16x16x32_bf16 v[120:123], a[80:83], a[8:11], v[120:123]\n"
"ds_read_b64_tr_b16 a[64:65], v16 offset:128\n"
"v_mul_f32 v31, v31, v25\n"
"v_mfma_f32_16x16x32_bf16 v[116:119], a[80:83], a[4:7], v[116:119]\n"
"ds_read_b64_tr_b16 a[66:67], v16 offset:3712\n"
"v_cvt_pk_bf16_f32 v21, v31, v30\n"
"v_mfma_f32_16x16x32_bf16 v[116:119], a[84:87], a[20:23], v[116:119]\n"
"v_accvgpr_read_b32 v25, a122\n"
"v_lshlrev_b32 v25, 16, v25\n"
"v_mfma_f32_16x16x32_bf16 v[112:115], a[84:87], a[16:19], v[112:115]\n"
"ds_read_b128 a[56:59], v19 offset:12544\n"
"v_mul_f32 v30, v28, v26\n"
"v_mfma_f32_16x16x32_bf16 v[112:115], a[80:83], a[0:3], v[112:115]\n"
"ds_read_b64_tr_b16 a[68:69], v16 offset:7296\n"
"s_waitcnt lgkmcnt(10)\n"
"v_mfma_f32_16x16x32_bf16 v[128:131], a[88:91], a[0:3], v[128:131]\n"
"ds_read_b64_tr_b16 a[70:71], v16 offset:10880\n"
"v_accvgpr_read_b32 v26, a138\n"
"v_mfma_f32_16x16x32_bf16 v[128:131], a[92:95], a[16:19], v[128:131]\n"
"v_lshlrev_b32 v26, 16, v26\n"
"v_fmac_f32 v30, v15, v27\n"
"v_mfma_f32_16x16x32_bf16 v[132:135], a[92:95], a[20:23], v[132:135]\n"
"ds_read_b64_tr_b16 a[72:73], v16 offset:14464\n"
"v_fmac_f32 v30, v14, v24\n"
"v_mfma_f32_16x16x32_bf16 v[132:135], a[88:91], a[4:7], v[132:135]\n"
"ds_read_b128 a[60:63], v19 offset:14592\n"
"v_fmac_f32 v30, v13, v25\n"
"v_mfma_f32_16x16x32_bf16 v[136:139], a[88:91], a[8:11], v[136:139]\n"
"ds_read_b64_tr_b16 a[74:75], v16 offset:18048\n"
"s_add_u32 m0, s78, 0x2100\n"
"v_mfma_f32_16x16x32_bf16 v[136:139], a[92:95], a[24:27], v[136:139]\n"
"v_mul_f32 v30, v30, v26\n"
"v_accvgpr_read_b32 v26, a124\n"
"v_mfma_f32_16x16x32_bf16 v[140:143], a[92:95], a[28:31], v[140:143]\n"
"ds_read_b64_tr_b16 a[76:77], v16 offset:21632\n"
"s_add_u32 s80, s96, 5\n"
"v_mfma_f32_16x16x32_bf16 v[140:143], a[88:91], a[12:15], v[140:143]\n"
"ds_read_b64_tr_b16 a[78:79], v16 offset:25216\n"
"s_waitcnt lgkmcnt(10)\n"
"v_mfma_f32_16x16x32_bf16 v[156:159], a[96:99], a[12:15], v[156:159]\n"
"s_waitcnt lgkmcnt(3)\n"
"s_and_b32 s80, s80, 3\n"
"v_mfma_f32_16x16x32_bf16 v[156:159], a[100:103], a[28:31], v[156:159]\n"
"v_lshlrev_b32 v26, 16, v26\n"
"v_mul_f32 v31, v28, v27\n"
"v_mfma_f32_16x16x32_bf16 v[152:155], a[100:103], a[24:27], v[152:155]\n"
"ds_read_b64_tr_b16 a[80:81], v16 offset:160\n"
"s_add_u32 s80, s80, 8\n"
"v_mfma_f32_16x16x32_bf16 v[152:155], a[96:99], a[8:11], v[152:155]\n"
"ds_read_b64_tr_b16 a[82:83], v16 offset:3744\n"
"s_lshl_b32 s80, s80, 7\n"
"v_mfma_f32_16x16x32_bf16 v[148:151], a[96:99], a[4:7], v[148:151]\n"
"buffer_load_dwordx4 v8, s[36:39], s80 offen offset:0 lds\n"
"v_mfma_f32_16x16x32_bf16 v[148:151], a[100:103], a[20:23], v[148:151]\n"
"v_accvgpr_read_b32 v27, a140\n"
"v_lshlrev_b32 v27, 16, v27\n"
"v_mfma_f32_16x16x32_bf16 v[144:147], a[100:103], a[16:19], v[144:147]\n"
"ds_read_b64_tr_b16 a[84:85], v16 offset:7328\n"
"v_fmac_f32 v31, v15, v24\n"
"v_mfma_f32_16x16x32_bf16 v[144:147], a[96:99], a[0:3], v[144:147]\n"
"ds_read_b64_tr_b16 a[86:87], v16 offset:10912\n"
"v_mfma_f32_16x16x32_bf16 v[160:163], a[64:67], a[0:3], v[160:163]\n"
"ds_read_b64_tr_b16 a[88:89], v16 offset:14496\n"
"v_fmac_f32 v31, v14, v25\n"
"v_mfma_f32_16x16x32_bf16 v[160:163], a[68:71], a[16:19], v[160:163]\n"
"buffer_load_dwordx4 v9, s[36:39], s80 offen offset:1024 lds\n"
"v_mfma_f32_16x16x32_bf16 v[164:167], a[68:71], a[20:23], v[164:167]\n"
"v_fmac_f32 v31, v13, v26\n"
"v_mul_f32 v31, v31, v27\n"
"v_mfma_f32_16x16x32_bf16 v[164:167], a[64:67], a[4:7], v[164:167]\n"
"ds_read_b64_tr_b16 a[90:91], v16 offset:18080\n"
"v_cvt_pk_bf16_f32 v20, v31, v30\n"
"v_mfma_f32_16x16x32_bf16 v[168:171], a[64:67], a[8:11], v[168:171]\n"
"ds_read_b64_tr_b16 a[92:93], v16 offset:21664\n"
"v_add_u32 v30, s18, v12\n"
"v_mfma_f32_16x16x32_bf16 v[168:171], a[68:71], a[24:27], v[168:171]\n"
"buffer_load_dwordx4 v10, s[36:39], s80 offen offset:2048 lds\n"
"v_mfma_f32_16x16x32_bf16 v[172:175], a[68:71], a[28:31], v[172:175]\n"
"ds_read_b64_tr_b16 a[94:95], v16 offset:25248\n"
"v_accvgpr_read_b32 v31, a192\n"
"v_mfma_f32_16x16x32_bf16 v[172:175], a[64:67], a[12:15], v[172:175]\n"
"ds_write_b64 v30, v[20:21] offset:0\n"
"s_waitcnt lgkmcnt(9)\n"
"v_mfma_f32_16x16x32_bf16 v[188:191], a[72:75], a[12:15], v[188:191]\n"
"ds_read_b64_tr_b16 a[96:97], v16 offset:192\n"
"v_mfma_f32_16x16x32_bf16 v[188:191], a[76:79], a[28:31], v[188:191]\n"
"buffer_load_dwordx4 v11, s[36:39], s80 offen offset:3072 lds\n"
"v_mfma_f32_16x16x32_bf16 v[184:187], a[76:79], a[24:27], v[184:187]\n"
"ds_read_b64_tr_b16 a[98:99], v16 offset:3776\n"
"v_mfma_f32_16x16x32_bf16 v[184:187], a[72:75], a[8:11], v[184:187]\n"
"ds_read_b64_tr_b16 a[100:101], v16 offset:7360\n"
"s_add_u32 m0, s78, 0x3100\n"
"v_mfma_f32_16x16x32_bf16 v[180:183], a[72:75], a[4:7], v[180:183]\n"
"s_or_b32 s80, s80, 0x10000\n"
"ds_write_b64 v30, v[22:23] offset:8\n"
"v_mfma_f32_16x16x32_bf16 v[180:183], a[76:79], a[20:23], v[180:183]\n"
"buffer_load_dwordx4 v8, s[36:39], s80 offen offset:0 lds\n"
"v_mfma_f32_16x16x32_bf16 v[176:179], a[76:79], a[16:19], v[176:179]\n"
"ds_read_b64_tr_b16 a[102:103], v16 offset:10944\n"
"v_mfma_f32_16x16x32_bf16 v[176:179], a[72:75], a[0:3], v[176:179]\n"
"ds_read_b64_tr_b16 a[64:65], v16 offset:14528\n"
"s_waitcnt lgkmcnt(11)\n"
"v_mfma_f32_16x16x32_bf16 v[192:195], a[80:83], a[0:3], v[192:195]\n"
"ds_read_b64_tr_b16 a[66:67], v16 offset:18112\n"
"v_mfma_f32_16x16x32_bf16 v[192:195], a[84:87], a[16:19], v[192:195]\n"
"buffer_load_dwordx4 v9, s[36:39], s80 offen offset:1024 lds\n"
"v_mfma_f32_16x16x32_bf16 v[196:199], a[84:87], a[20:23], v[196:199]\n"
"ds_read_b128 a[176:179], v31 offset:0\n"
"v_accvgpr_read_b32 v14, a144\n"
"v_mfma_f32_16x16x32_bf16 v[196:199], a[80:83], a[4:7], v[196:199]\n"
"ds_read_b64_tr_b16 a[68:69], v16 offset:21696\n"
"v_lshlrev_b32 v13, 16, v14\n"
"v_mfma_f32_16x16x32_bf16 v[200:203], a[80:83], a[8:11], v[200:203]\n"
"ds_read_b64_tr_b16 a[70:71], v16 offset:25280\n"
"v_and_b32 v14, 0xffff0000, v14\n"
"v_mfma_f32_16x16x32_bf16 v[200:203], a[84:87], a[24:27], v[200:203]\n"
"buffer_load_dwordx4 v10, s[36:39], s80 offen offset:2048 lds\n"
"v_mfma_f32_16x16x32_bf16 v[204:207], a[84:87], a[28:31], v[204:207]\n"
"v_accvgpr_read_b32 v28, a145\n"
"v_lshlrev_b32 v15, 16, v28\n"
"v_mfma_f32_16x16x32_bf16 v[204:207], a[80:83], a[12:15], v[204:207]\n"
"v_and_b32 v28, 0xffff0000, v28\n"
"s_waitcnt lgkmcnt(11)\n"
"v_mfma_f32_16x16x32_bf16 v[220:223], a[88:91], a[12:15], v[220:223]\n"
"v_subrev_u32 v16, 0xe000, v16\n"
"v_accvgpr_read_b32 v24, a104\n"
"v_mfma_f32_16x16x32_bf16 v[220:223], a[92:95], a[28:31], v[220:223]\n"
"buffer_load_dwordx4 v11, s[36:39], s80 offen offset:3072 lds\n"
"v_mfma_f32_16x16x32_bf16 v[216:219], a[92:95], a[24:27], v[216:219]\n"
"v_and_b32 v24, 0xffff0000, v24\n"
"v_cndmask_b32 v24, 0, v24, vcc\n"
"v_mfma_f32_16x16x32_bf16 v[216:219], a[88:91], a[8:11], v[216:219]\n"
"s_add_u32 m0, s77, 0xe000\n"
"s_add_u32 s79, s96, 5\n"
"v_mfma_f32_16x16x32_bf16 v[212:215], a[88:91], a[4:7], v[212:215]\n"
"s_and_b32 s79, s79, 3\n"
"s_add_u32 s79, s79, 8\n"
"v_mfma_f32_16x16x32_bf16 v[212:215], a[92:95], a[20:23], v[212:215]\n"
"v_accvgpr_read_b32 v25, a106\n"
"v_and_b32 v25, 0xffff0000, v25\n"
"v_mfma_f32_16x16x32_bf16 v[208:211], a[92:95], a[16:19], v[208:211]\n"
"s_mul_i32 s79, s79, 0x70000\n"
"v_cndmask_b32 v25, 0, v25, vcc\n"
"v_mfma_f32_16x16x32_bf16 v[208:211], a[88:91], a[0:3], v[208:211]\n"
"s_waitcnt vmcnt(24) lgkmcnt(0)\n"
"s_barrier\n"
"v_mfma_f32_16x16x32_bf16 v[224:227], a[96:99], a[0:3], v[224:227]\n"
"ds_read_b64_tr_b16 a[72:73], v16 offset:0\n"
"v_accvgpr_read_b32 v26, a108\n"
"v_mfma_f32_16x16x32_bf16 v[224:227], a[100:103], a[16:19], v[224:227]\n"
"buffer_load_dwordx4 v1, s[40:43], s79 offen offset:0 lds\n"
"v_mfma_f32_16x16x32_bf16 v[228:231], a[100:103], a[20:23], v[228:231]\n"
"ds_read_b64_tr_b16 a[74:75], v16 offset:3584\n"
"v_and_b32 v26, 0xffff0000, v26\n"
"v_mfma_f32_16x16x32_bf16 v[228:231], a[96:99], a[4:7], v[228:231]\n"
"v_cndmask_b32 v26, 0, v26, vcc\n"
"v_accvgpr_read_b32 v27, a110\n"
"v_mfma_f32_16x16x32_bf16 v[232:235], a[96:99], a[8:11], v[232:235]\n"
"ds_read_b64_tr_b16 a[76:77], v16 offset:7168\n"
"v_and_b32 v27, 0xffff0000, v27\n"
"v_mfma_f32_16x16x32_bf16 v[232:235], a[100:103], a[24:27], v[232:235]\n"
"buffer_load_dwordx4 v2, s[40:43], s79 offen offset:1024 lds\n"
"v_mfma_f32_16x16x32_bf16 v[236:239], a[100:103], a[28:31], v[236:239]\n"
"ds_read_b64_tr_b16 a[78:79], v16 offset:10752\n"
"v_mul_f32 v30, v28, v24\n"
"v_mfma_f32_16x16x32_bf16 v[236:239], a[96:99], a[12:15], v[236:239]\n"
"v_accvgpr_read_b32 v24, a126\n"
"v_mfma_f32_16x16x32_bf16 v[252:255], a[64:67], a[12:15], v[252:255]\n"
"ds_read_b64_tr_b16 a[80:81], v16 offset:14336\n"
"v_and_b32 v24, 0xffff0000, v24\n"
"v_mfma_f32_16x16x32_bf16 v[252:255], a[68:71], a[28:31], v[252:255]\n"
"buffer_load_dwordx4 v3, s[40:43], s79 offen offset:2048 lds\n"
"v_mfma_f32_16x16x32_bf16 v[248:251], a[68:71], a[24:27], v[248:251]\n"
"ds_read_b64_tr_b16 a[82:83], v16 offset:17920\n"
"v_fmac_f32 v30, v15, v25\n"
"v_mfma_f32_16x16x32_bf16 v[248:251], a[64:67], a[8:11], v[248:251]\n"
"v_fmac_f32 v30, v14, v26\n"
"v_fmac_f32 v30, v13, v27\n"
"v_mfma_f32_16x16x32_bf16 v[244:247], a[64:67], a[4:7], v[244:247]\n"
"ds_read_b64_tr_b16 a[84:85], v16 offset:21504\n"
"v_mul_f32 v30, v30, v24\n"
"v_mfma_f32_16x16x32_bf16 v[244:247], a[68:71], a[20:23], v[244:247]\n"
"buffer_load_dwordx4 v4, s[40:43], s79 offen offset:3072 lds\n"
"v_mfma_f32_16x16x32_bf16 v[240:243], a[68:71], a[16:19], v[240:243]\n"
"ds_read_b64_tr_b16 a[86:87], v16 offset:25088\n"
"v_accvgpr_read_b32 v24, a112\n"
"v_mfma_f32_16x16x32_bf16 v[240:243], a[64:67], a[0:3], v[240:243]\n"
"v_and_b32 v24, 0xffff0000, v24\n"
"v_mul_f32 v31, v28, v25\n"
"s_waitcnt lgkmcnt(4)\n"
"v_mfma_f32_16x16x32_bf16 v[32:35], a[72:75], a[32:35], v[32:35]\n"
"ds_read_b64_tr_b16 a[88:89], v16 offset:32\n"
"s_add_u32 m0, s77, 0xf000\n"
"v_mfma_f32_16x16x32_bf16 v[32:35], a[76:79], a[48:51], v[32:35]\n"
"buffer_load_dwordx4 v5, s[40:43], s79 offen offset:0 lds\n"
"v_mfma_f32_16x16x32_bf16 v[36:39], a[76:79], a[52:55], v[36:39]\n"
"ds_read_b64_tr_b16 a[90:91], v16 offset:3616\n"
"v_accvgpr_read_b32 v25, a128\n"
"v_mfma_f32_16x16x32_bf16 v[36:39], a[72:75], a[36:39], v[36:39]\n"
"ds_read_b64_tr_b16 a[92:93], v16 offset:7200\n"
"v_and_b32 v25, 0xffff0000, v25\n"
"v_mfma_f32_16x16x32_bf16 v[40:43], a[72:75], a[40:43], v[40:43]\n"
"v_fmac_f32 v31, v15, v26\n"
"v_fmac_f32 v31, v14, v27\n"
"v_mfma_f32_16x16x32_bf16 v[40:43], a[76:79], a[56:59], v[40:43]\n"
"buffer_load_dwordx4 v6, s[40:43], s79 offen offset:1024 lds\n"
"v_mfma_f32_16x16x32_bf16 v[44:47], a[76:79], a[60:63], v[44:47]\n"
"ds_read_b64_tr_b16 a[94:95], v16 offset:10784\n"
"v_fmac_f32 v31, v13, v24\n"
"v_mfma_f32_16x16x32_bf16 v[44:47], a[72:75], a[44:47], v[44:47]\n"
"ds_read_b64_tr_b16 a[96:97], v16 offset:14368\n"
"s_waitcnt lgkmcnt(5)\n"
"v_mfma_f32_16x16x32_bf16 v[60:63], a[80:83], a[44:47], v[60:63]\n"
"ds_read_b64_tr_b16 a[98:99], v16 offset:17952\n"
"v_mul_f32 v31, v31, v25\n"
"v_mfma_f32_16x16x32_bf16 v[60:63], a[84:87], a[60:63], v[60:63]\n"
"buffer_load_dwordx4 v7, s[40:43], s79 offen offset:2048 lds\n"
"v_mfma_f32_16x16x32_bf16 v[56:59], a[84:87], a[56:59], v[56:59]\n"
"v_cvt_pk_bf16_f32 v23, v31, v30\n"
"v_accvgpr_read_b32 v25, a114\n"
"v_mfma_f32_16x16x32_bf16 v[56:59], a[80:83], a[40:43], v[56:59]\n"
"ds_read_b64_tr_b16 a[100:101], v16 offset:21536\n"
"v_and_b32 v25, 0xffff0000, v25\n"
"v_mfma_f32_16x16x32_bf16 v[52:55], a[80:83], a[36:39], v[52:55]\n"
"ds_read_b64_tr_b16 a[102:103], v16 offset:25120\n"
"s_waitcnt vmcnt(22)\n"
"v_mfma_f32_16x16x32_bf16 v[52:55], a[84:87], a[52:55], v[52:55]\n"
"v_mul_f32 v30, v28, v26\n"
"v_accvgpr_read_b32 v26, a130\n"
"v_mfma_f32_16x16x32_bf16 v[48:51], a[84:87], a[48:51], v[48:51]\n"
"ds_read_b64_tr_b16 a[64:65], v16 offset:64\n"
"v_and_b32 v26, 0xffff0000, v26\n"
"v_mfma_f32_16x16x32_bf16 v[48:51], a[80:83], a[32:35], v[48:51]\n"
"ds_read_b128 a[0:3], v18 offset:0\n"
"s_waitcnt lgkmcnt(6)\n"
"v_mfma_f32_16x16x32_bf16 v[64:67], a[88:91], a[32:35], v[64:67]\n"
"ds_read_b64_tr_b16 a[66:67], v16 offset:3648\n"
"v_fmac_f32 v30, v15, v27\n"
"v_mfma_f32_16x16x32_bf16 v[64:67], a[92:95], a[48:51], v[64:67]\n"
"v_fmac_f32 v30, v14, v24\n"
"v_fmac_f32 v30, v13, v25\n"
"v_mfma_f32_16x16x32_bf16 v[68:71], a[92:95], a[52:55], v[68:71]\n"
"ds_read_b64_tr_b16 a[68:69], v16 offset:7232\n"
"v_mul_f32 v30, v30, v26\n"
"v_mfma_f32_16x16x32_bf16 v[68:71], a[88:91], a[36:39], v[68:71]\n"
"ds_read_b64_tr_b16 a[70:71], v16 offset:10816\n"
"v_accvgpr_read_b32 v26, a116\n"
"v_mfma_f32_16x16x32_bf16 v[72:75], a[88:91], a[40:43], v[72:75]\n"
"ds_read_b128 a[4:7], v18 offset:2048\n"
"v_and_b32 v26, 0xffff0000, v26\n"
"v_mfma_f32_16x16x32_bf16 v[72:75], a[92:95], a[56:59], v[72:75]\n"
"v_mul_f32 v31, v28, v27\n"
"v_accvgpr_read_b32 v27, a132\n"
"v_mfma_f32_16x16x32_bf16 v[76:79], a[92:95], a[60:63], v[76:79]\n"
"ds_read_b64_tr_b16 a[72:73], v16 offset:14400\n"
"s_lshl_b32 s83, s72, 5\n"
"v_mfma_f32_16x16x32_bf16 v[76:79], a[88:91], a[44:47], v[76:79]\n"
"ds_read_b64_tr_b16 a[74:75], v16 offset:17984\n"
"s_waitcnt lgkmcnt(8)\n"
"v_mfma_f32_16x16x32_bf16 v[92:95], a[96:99], a[44:47], v[92:95]\n"
"ds_read_b64_tr_b16 a[76:77], v16 offset:21568\n"
"s_add_u32 s83, s83, s73\n"
"v_mfma_f32_16x16x32_bf16 v[92:95], a[100:103], a[60:63], v[92:95]\n"
"s_lshl2_add_u32 s83, s83, s34\n"
"s_lshl_b32 s83, s83, 12\n"
"v_mfma_f32_16x16x32_bf16 v[88:91], a[100:103], a[56:59], v[88:91]\n"
"ds_read_b128 a[8:11], v18 offset:4096\n"
"v_and_b32 v27, 0xffff0000, v27\n"
"v_mfma_f32_16x16x32_bf16 v[88:91], a[96:99], a[40:43], v[88:91]\n"
"ds_read_b64_tr_b16 a[78:79], v16 offset:25152\n"
"v_fmac_f32 v31, v15, v24\n"
"v_mfma_f32_16x16x32_bf16 v[84:87], a[96:99], a[36:39], v[84:87]\n"
"ds_read_b64_tr_b16 a[80:81], v16 offset:96\n"
"v_fmac_f32 v31, v14, v25\n"
"v_mfma_f32_16x16x32_bf16 v[84:87], a[100:103], a[52:55], v[84:87]\n"
"buffer_load_dwordx4 a[232:235], v12, s[36:39], s83 offen offset:0\n"
"v_mfma_f32_16x16x32_bf16 v[80:83], a[100:103], a[48:51], v[80:83]\n"
"ds_read_b128 a[12:15], v18 offset:6144\n"
"v_fmac_f32 v31, v13, v26\n"
"v_mfma_f32_16x16x32_bf16 v[80:83], a[96:99], a[32:35], v[80:83]\n"
"ds_read_b64_tr_b16 a[82:83], v16 offset:3680\n"
"s_waitcnt lgkmcnt(9)\n"
"v_mfma_f32_16x16x32_bf16 v[96:99], a[64:67], a[32:35], v[96:99]\n"
"ds_read_b64_tr_b16 a[84:85], v16 offset:7264\n"
"v_mul_f32 v31, v31, v27\n"
"v_mfma_f32_16x16x32_bf16 v[96:99], a[68:71], a[48:51], v[96:99]\n"
"buffer_load_dwordx4 a[236:239], v12, s[36:39], s83 offen offset:1024\n"
"v_mfma_f32_16x16x32_bf16 v[100:103], a[68:71], a[52:55], v[100:103]\n"
"ds_read_b64_tr_b16 a[86:87], v16 offset:10848\n"
"v_cvt_pk_bf16_f32 v22, v31, v30\n"
"v_mfma_f32_16x16x32_bf16 v[100:103], a[64:67], a[36:39], v[100:103]\n"
"ds_read_b128 a[16:19], v19 offset:0\n"
"v_accvgpr_read_b32 v27, a118\n"
"v_mfma_f32_16x16x32_bf16 v[104:107], a[64:67], a[40:43], v[104:107]\n"
"ds_read_b64_tr_b16 a[88:89], v16 offset:14432\n"
"v_and_b32 v27, 0xffff0000, v27\n"
"v_mfma_f32_16x16x32_bf16 v[104:107], a[68:71], a[56:59], v[104:107]\n"
"buffer_load_dwordx4 a[240:243], v12, s[36:39], s83 offen offset:2048\n"
"v_mfma_f32_16x16x32_bf16 v[108:111], a[68:71], a[60:63], v[108:111]\n"
"ds_read_b64_tr_b16 a[90:91], v16 offset:18016\n"
"v_mul_f32 v30, v28, v24\n"
"v_mfma_f32_16x16x32_bf16 v[108:111], a[64:67], a[44:47], v[108:111]\n"
"ds_read_b64_tr_b16 a[92:93], v16 offset:21600\n"
"s_waitcnt lgkmcnt(9)\n"
"v_mfma_f32_16x16x32_bf16 v[124:127], a[72:75], a[44:47], v[124:127]\n"
"ds_read_b128 a[20:23], v19 offset:2048\n"
"v_accvgpr_read_b32 v24, a134\n"
"v_mfma_f32_16x16x32_bf16 v[124:127], a[76:79], a[60:63], v[124:127]\n"
"buffer_load_dwordx4 a[244:247], v12, s[36:39], s83 offen offset:3072\n"
"v_mfma_f32_16x16x32_bf16 v[120:123], a[76:79], a[56:59], v[120:123]\n"
"ds_read_b64_tr_b16 a[94:95], v16 offset:25184\n"
"v_and_b32 v24, 0xffff0000, v24\n"
"v_mfma_f32_16x16x32_bf16 v[120:123], a[72:75], a[40:43], v[120:123]\n"
"ds_read_b64_tr_b16 a[96:97], v16 offset:128\n"
"v_fmac_f32 v30, v15, v25\n"
"v_mfma_f32_16x16x32_bf16 v[116:119], a[72:75], a[36:39], v[116:119]\n"
"ds_read_b64_tr_b16 a[98:99], v16 offset:3712\n"
"v_fmac_f32 v30, v14, v26\n"
"v_mfma_f32_16x16x32_bf16 v[116:119], a[76:79], a[52:55], v[116:119]\n"
"global_load_dwordx4 a[248:251], v12, s[26:27] offset:0\n"
"v_mfma_f32_16x16x32_bf16 v[112:115], a[76:79], a[48:51], v[112:115]\n"
"ds_read_b128 a[24:27], v19 offset:4096\n"
"v_fmac_f32 v30, v13, v27\n"
"v_mfma_f32_16x16x32_bf16 v[112:115], a[72:75], a[32:35], v[112:115]\n"
"ds_read_b64_tr_b16 a[100:101], v16 offset:7296\n"
"s_waitcnt lgkmcnt(10)\n"
"v_mfma_f32_16x16x32_bf16 v[128:131], a[80:83], a[32:35], v[128:131]\n"
"ds_read_b64_tr_b16 a[102:103], v16 offset:10880\n"
"v_mul_f32 v30, v30, v24\n"
"v_mfma_f32_16x16x32_bf16 v[128:131], a[84:87], a[48:51], v[128:131]\n"
"global_load_dwordx4 a[252:255], v12, s[26:27] offset:1024\n"
"v_mfma_f32_16x16x32_bf16 v[132:135], a[84:87], a[52:55], v[132:135]\n"
"ds_read_b64_tr_b16 a[64:65], v16 offset:14464\n"
"v_accvgpr_read_b32 v24, a120\n"
"v_mfma_f32_16x16x32_bf16 v[132:135], a[80:83], a[36:39], v[132:135]\n"
"ds_read_b128 a[28:31], v19 offset:6144\n"
"v_and_b32 v24, 0xffff0000, v24\n"
"v_mfma_f32_16x16x32_bf16 v[136:139], a[80:83], a[40:43], v[136:139]\n"
"ds_read_b64_tr_b16 a[66:67], v16 offset:18048\n"
"s_add_u32 m0, s78, 0x0\n"
"v_mfma_f32_16x16x32_bf16 v[136:139], a[84:87], a[56:59], v[136:139]\n"
"v_mul_f32 v31, v28, v25\n"
"v_accvgpr_read_b32 v25, a136\n"
"v_mfma_f32_16x16x32_bf16 v[140:143], a[84:87], a[60:63], v[140:143]\n"
"ds_read_b64_tr_b16 a[68:69], v16 offset:21632\n"
"s_add_u32 s80, s96, 3\n"
"v_mfma_f32_16x16x32_bf16 v[140:143], a[80:83], a[44:47], v[140:143]\n"
"ds_read_b64_tr_b16 a[70:71], v16 offset:25216\n"
"s_waitcnt lgkmcnt(10)\n"
"v_mfma_f32_16x16x32_bf16 v[156:159], a[88:91], a[44:47], v[156:159]\n"
"s_waitcnt lgkmcnt(3)\n"
"s_and_b32 s80, s80, 3\n"
"v_mfma_f32_16x16x32_bf16 v[156:159], a[92:95], a[60:63], v[156:159]\n"
"v_and_b32 v25, 0xffff0000, v25\n"
"v_fmac_f32 v31, v15, v26\n"
"v_mfma_f32_16x16x32_bf16 v[152:155], a[92:95], a[56:59], v[152:155]\n"
"ds_read_b64_tr_b16 a[72:73], v16 offset:160\n"
"s_add_u32 s80, s80, 12\n"
"v_mfma_f32_16x16x32_bf16 v[152:155], a[88:91], a[40:43], v[152:155]\n"
"ds_read_b64_tr_b16 a[74:75], v16 offset:3744\n"
"s_lshl_b32 s80, s80, 7\n"
"v_mfma_f32_16x16x32_bf16 v[148:151], a[88:91], a[36:39], v[148:151]\n"
"buffer_load_dwordx4 v8, s[36:39], s80 offen offset:0 lds\n"
"v_mfma_f32_16x16x32_bf16 v[148:151], a[92:95], a[52:55], v[148:151]\n"
"v_fmac_f32 v31, v14, v27\n"
"v_fmac_f32 v31, v13, v24\n"
"v_mfma_f32_16x16x32_bf16 v[144:147], a[92:95], a[48:51], v[144:147]\n"
"ds_read_b64_tr_b16 a[76:77], v16 offset:7328\n"
"v_mul_f32 v31, v31, v25\n"
"v_mfma_f32_16x16x32_bf16 v[144:147], a[88:91], a[32:35], v[144:147]\n"
"ds_read_b64_tr_b16 a[78:79], v16 offset:10912\n"
"v_mfma_f32_16x16x32_bf16 v[160:163], a[96:99], a[32:35], v[160:163]\n"
"ds_read_b64_tr_b16 a[80:81], v16 offset:14496\n"
"v_cvt_pk_bf16_f32 v21, v31, v30\n"
"v_mfma_f32_16x16x32_bf16 v[160:163], a[100:103], a[48:51], v[160:163]\n"
"v_accvgpr_read_b32 v25, a122\n"
"v_and_b32 v25, 0xffff0000, v25\n"
"v_mfma_f32_16x16x32_bf16 v[164:167], a[100:103], a[52:55], v[164:167]\n"
"v_mul_f32 v30, v28, v26\n"
"v_accvgpr_read_b32 v26, a138\n"
"v_mfma_f32_16x16x32_bf16 v[164:167], a[96:99], a[36:39], v[164:167]\n"
"ds_read_b64_tr_b16 a[82:83], v16 offset:18080\n"
"v_and_b32 v26, 0xffff0000, v26\n"
"v_mfma_f32_16x16x32_bf16 v[168:171], a[96:99], a[40:43], v[168:171]\n"
"ds_read_b64_tr_b16 a[84:85], v16 offset:21664\n"
"v_fmac_f32 v30, v15, v27\n"
"v_mfma_f32_16x16x32_bf16 v[168:171], a[100:103], a[56:59], v[168:171]\n"
"buffer_load_dwordx4 v9, s[36:39], s80 offen offset:1024 lds\n"
"v_mfma_f32_16x16x32_bf16 v[172:175], a[100:103], a[60:63], v[172:175]\n"
"ds_read_b64_tr_b16 a[86:87], v16 offset:25248\n"
"v_fmac_f32 v30, v14, v24\n"
"v_mfma_f32_16x16x32_bf16 v[172:175], a[96:99], a[44:47], v[172:175]\n"
"v_fmac_f32 v30, v13, v25\n"
"s_waitcnt lgkmcnt(8)\n"
"v_mfma_f32_16x16x32_bf16 v[188:191], a[64:67], a[44:47], v[188:191]\n"
"ds_read_b64_tr_b16 a[88:89], v16 offset:192\n"
"v_mul_f32 v30, v30, v26\n"
"v_mfma_f32_16x16x32_bf16 v[188:191], a[68:71], a[60:63], v[188:191]\n"
"v_accvgpr_read_b32 v26, a124\n"
"v_and_b32 v26, 0xffff0000, v26\n"
"v_mfma_f32_16x16x32_bf16 v[184:187], a[68:71], a[56:59], v[184:187]\n"
"ds_read_b64_tr_b16 a[90:91], v16 offset:3776\n"
"v_mul_f32 v31, v28, v27\n"
"v_mfma_f32_16x16x32_bf16 v[184:187], a[64:67], a[40:43], v[184:187]\n"
"ds_read_b64_tr_b16 a[92:93], v16 offset:7360\n"
"v_accvgpr_read_b32 v27, a140\n"
"v_mfma_f32_16x16x32_bf16 v[180:183], a[64:67], a[36:39], v[180:183]\n"
"v_and_b32 v27, 0xffff0000, v27\n"
"v_fmac_f32 v31, v15, v24\n"
"v_mfma_f32_16x16x32_bf16 v[180:183], a[68:71], a[52:55], v[180:183]\n"
"buffer_load_dwordx4 v10, s[36:39], s80 offen offset:2048 lds\n"
"v_mfma_f32_16x16x32_bf16 v[176:179], a[68:71], a[48:51], v[176:179]\n"
"ds_read_b64_tr_b16 a[94:95], v16 offset:10944\n"
"v_fmac_f32 v31, v14, v25\n"
"v_mfma_f32_16x16x32_bf16 v[176:179], a[64:67], a[32:35], v[176:179]\n"
"ds_read_b64_tr_b16 a[96:97], v16 offset:14528\n"
"s_waitcnt lgkmcnt(9)\n"
"v_mfma_f32_16x16x32_bf16 v[192:195], a[72:75], a[32:35], v[192:195]\n"
"ds_read_b64_tr_b16 a[98:99], v16 offset:18112\n"
"v_fmac_f32 v31, v13, v26\n"
"v_mfma_f32_16x16x32_bf16 v[192:195], a[76:79], a[48:51], v[192:195]\n"
"v_mul_f32 v31, v31, v27\n"
"v_cvt_pk_bf16_f32 v20, v31, v30\n"
"v_mfma_f32_16x16x32_bf16 v[196:199], a[76:79], a[52:55], v[196:199]\n"
"v_add_u32 v30, s18, v12\n"
"v_accvgpr_read_b32 v31, a192\n"
"v_mfma_f32_16x16x32_bf16 v[196:199], a[72:75], a[36:39], v[196:199]\n"
"ds_read_b64_tr_b16 a[100:101], v16 offset:21696\n"
"v_mfma_f32_16x16x32_bf16 v[200:203], a[72:75], a[40:43], v[200:203]\n"
"ds_read_b64_tr_b16 a[102:103], v16 offset:25280\n"
"v_mfma_f32_16x16x32_bf16 v[200:203], a[76:79], a[56:59], v[200:203]\n"
"buffer_load_dwordx4 v11, s[36:39], s80 offen offset:3072 lds\n"
"v_mfma_f32_16x16x32_bf16 v[204:207], a[76:79], a[60:63], v[204:207]\n"
"ds_write_b64 v30, v[20:21] offset:1024\n"
"v_mfma_f32_16x16x32_bf16 v[204:207], a[72:75], a[44:47], v[204:207]\n"
"ds_write_b64 v30, v[22:23] offset:1032\n"
"s_waitcnt lgkmcnt(10)\n"
"v_mfma_f32_16x16x32_bf16 v[220:223], a[80:83], a[44:47], v[220:223]\n"
"ds_read_b128 a[180:183], v31 offset:1024\n"
"v_accvgpr_read_b32 v14, a146\n"
"v_mfma_f32_16x16x32_bf16 v[220:223], a[84:87], a[60:63], v[220:223]\n"
"v_lshlrev_b32 v13, 16, v14\n"
"v_and_b32 v14, 0xffff0000, v14\n"
"v_mfma_f32_16x16x32_bf16 v[216:219], a[84:87], a[56:59], v[216:219]\n"
"v_accvgpr_read_b32 v28, a147\n"
"v_lshlrev_b32 v15, 16, v28\n"
"v_mfma_f32_16x16x32_bf16 v[216:219], a[80:83], a[40:43], v[216:219]\n"
"s_add_u32 m0, s78, 0x1000\n"
"s_or_b32 s80, s80, 0x10000\n"
"v_mfma_f32_16x16x32_bf16 v[212:215], a[80:83], a[36:39], v[212:215]\n"
"v_and_b32 v28, 0xffff0000, v28\n"
"v_accvgpr_read_b32 v24, a105\n"
"v_mfma_f32_16x16x32_bf16 v[212:215], a[84:87], a[52:55], v[212:215]\n"
"buffer_load_dwordx4 v8, s[36:39], s80 offen offset:0 lds\n"
"v_mfma_f32_16x16x32_bf16 v[208:211], a[84:87], a[48:51], v[208:211]\n"
"v_lshlrev_b32 v24, 16, v24\n"
"v_cndmask_b32 v24, 0, v24, vcc\n"
"v_mfma_f32_16x16x32_bf16 v[208:211], a[80:83], a[32:35], v[208:211]\n"
"s_waitcnt vmcnt(26) lgkmcnt(0)\n"
"s_barrier\n"
"v_mfma_f32_16x16x32_bf16 v[224:227], a[88:91], a[32:35], v[224:227]\n"
"ds_read_b64_tr_b16 a[64:65], v16 offset:28672\n"
"v_accvgpr_read_b32 v25, a107\n"
"v_mfma_f32_16x16x32_bf16 v[224:227], a[92:95], a[48:51], v[224:227]\n"
"v_lshlrev_b32 v25, 16, v25\n"
"v_cndmask_b32 v25, 0, v25, vcc\n"
"v_mfma_f32_16x16x32_bf16 v[228:231], a[92:95], a[52:55], v[228:231]\n"
"ds_read_b64_tr_b16 a[66:67], v16 offset:32256\n"
"v_accvgpr_read_b32 v26, a109\n"
".Lfk_hop2864:\n"
"s_cbranch_execz .Lfk_hop3450\n"
"v_mfma_f32_16x16x32_bf16 v[228:231], a[88:91], a[36:39], v[228:231]\n"
"v_lshlrev_b32 v26, 16, v26\n"
"v_cndmask_b32 v26, 0, v26, vcc\n"
"v_mfma_f32_16x16x32_bf16 v[232:235], a[88:91], a[40:43], v[232:235]\n"
"ds_read_b64_tr_b16 a[68:69], v16 offset:35840\n"
"v_accvgpr_read_b32 v27, a111\n"
"v_mfma_f32_16x16x32_bf16 v[232:235], a[92:95], a[56:59], v[232:235]\n"
"buffer_load_dwordx4 v9, s[36:39], s80 offen offset:1024 lds\n"
"v_mfma_f32_16x16x32_bf16 v[236:239], a[92:95], a[60:63], v[236:239]\n"
"ds_read_b64_tr_b16 a[70:71], v16 offset:39424\n"
"v_lshlrev_b32 v27, 16, v27\n"
"v_mfma_f32_16x16x32_bf16 v[236:239], a[88:91], a[44:47], v[236:239]\n"
"v_mul_f32 v30, v28, v24\n"
"v_mfma_f32_16x16x32_bf16 v[252:255], a[96:99], a[44:47], v[252:255]\n"
"ds_read_b64_tr_b16 a[72:73], v16 offset:43008\n"
"v_accvgpr_read_b32 v24, a127\n"
"v_mfma_f32_16x16x32_bf16 v[252:255], a[100:103], a[60:63], v[252:255]\n"
"v_lshlrev_b32 v24, 16, v24\n"
"v_fmac_f32 v30, v15, v25\n"
"v_mfma_f32_16x16x32_bf16 v[248:251], a[100:103], a[56:59], v[248:251]\n"
"ds_read_b64_tr_b16 a[74:75], v16 offset:46592\n"
"v_fmac_f32 v30, v14, v26\n"
"v_mfma_f32_16x16x32_bf16 v[248:251], a[96:99], a[40:43], v[248:251]\n"
"v_fmac_f32 v30, v13, v27\n"
"v_mul_f32 v30, v30, v24\n"
"v_mfma_f32_16x16x32_bf16 v[244:247], a[96:99], a[36:39], v[244:247]\n"
"ds_read_b64_tr_b16 a[76:77], v16 offset:50176\n"
"v_accvgpr_read_b32 v24, a113\n"
"v_mfma_f32_16x16x32_bf16 v[244:247], a[100:103], a[52:55], v[244:247]\n"
"buffer_load_dwordx4 v10, s[36:39], s80 offen offset:2048 lds\n"
"v_mfma_f32_16x16x32_bf16 v[240:243], a[100:103], a[48:51], v[240:243]\n"
"ds_read_b64_tr_b16 a[78:79], v16 offset:53760\n"
"v_lshlrev_b32 v24, 16, v24\n"
"v_mfma_f32_16x16x32_bf16 v[240:243], a[96:99], a[32:35], v[240:243]\n"
"v_mul_f32 v31, v28, v25\n"
"v_accvgpr_read_b32 v25, a129\n"
"s_waitcnt lgkmcnt(4)\n"
"v_mfma_f32_16x16x32_bf16 v[32:35], a[64:67], a[0:3], v[32:35]\n"
"ds_read_b64_tr_b16 a[80:81], v16 offset:28704\n"
"v_lshlrev_b32 v25, 16, v25\n"
"v_mfma_f32_16x16x32_bf16 v[32:35], a[68:71], a[16:19], v[32:35]\n"
"v_fmac_f32 v31, v15, v26\n"
"v_fmac_f32 v31, v14, v27\n"
"v_mfma_f32_16x16x32_bf16 v[36:39], a[68:71], a[20:23], v[36:39]\n"
"ds_read_b64_tr_b16 a[82:83], v16 offset:32288\n"
"v_fmac_f32 v31, v13, v24\n"
"v_mfma_f32_16x16x32_bf16 v[36:39], a[64:67], a[4:7], v[36:39]\n"
"ds_read_b64_tr_b16 a[84:85], v16 offset:35872\n"
"v_mul_f32 v31, v31, v25\n"
"v_mfma_f32_16x16x32_bf16 v[40:43], a[64:67], a[8:11], v[40:43]\n"
"v_cvt_pk_bf16_f32 v23, v31, v30\n"
"v_accvgpr_read_b32 v25, a115\n"
"v_mfma_f32_16x16x32_bf16 v[40:43], a[68:71], a[24:27], v[40:43]\n"
"buffer_load_dwordx4 v11, s[36:39], s80 offen offset:3072 lds\n"
"v_mfma_f32_16x16x32_bf16 v[44:47], a[68:71], a[28:31], v[44:47]\n"
"ds_read_b64_tr_b16 a[86:87], v16 offset:39456\n"
"v_lshlrev_b32 v25, 16, v25\n"
"v_mfma_f32_16x16x32_bf16 v[44:47], a[64:67], a[12:15], v[44:47]\n"
"ds_read_b64_tr_b16 a[88:89], v16 offset:43040\n"
"s_waitcnt lgkmcnt(5)\n"
"v_mfma_f32_16x16x32_bf16 v[60:63], a[72:75], a[12:15], v[60:63]\n"
"ds_read_b64_tr_b16 a[90:91], v16 offset:46624\n"
"s_add_u32 m0, s77, 0x0\n"
"v_mfma_f32_16x16x32_bf16 v[60:63], a[76:79], a[28:31], v[60:63]\n"
"v_mul_f32 v30, v28, v26\n"
"v_accvgpr_read_b32 v26, a131\n"
"v_mfma_f32_16x16x32_bf16 v[56:59], a[76:79], a[24:27], v[56:59]\n"
"s_add_u32 s79, s96, 3\n"
"s_and_b32 s79, s79, 3\n"
"v_mfma_f32_16x16x32_bf16 v[56:59], a[72:75], a[8:11], v[56:59]\n"
"ds_read_b64_tr_b16 a[92:93], v16 offset:50208\n"
"s_add_u32 s79, s79, 12\n"
"v_mfma_f32_16x16x32_bf16 v[52:55], a[72:75], a[4:7], v[52:55]\n"
"ds_read_b64_tr_b16 a[94:95], v16 offset:53792\n"
"s_waitcnt vmcnt(21)\n"
"v_mfma_f32_16x16x32_bf16 v[52:55], a[76:79], a[20:23], v[52:55]\n"
"v_lshlrev_b32 v26, 16, v26\n"
"v_fmac_f32 v30, v15, v27\n"
"v_mfma_f32_16x16x32_bf16 v[48:51], a[76:79], a[16:19], v[48:51]\n"
"ds_read_b64_tr_b16 a[96:97], v16 offset:28736\n"
"s_mul_i32 s79, s79, 0x70000\n"
"v_mfma_f32_16x16x32_bf16 v[48:51], a[72:75], a[0:3], v[48:51]\n"
"ds_read_b128 a[32:35], v18 offset:8448\n"
"s_waitcnt lgkmcnt(6)\n"
"v_mfma_f32_16x16x32_bf16 v[64:67], a[80:83], a[0:3], v[64:67]\n"
"ds_read_b64_tr_b16 a[98:99], v16 offset:32320\n"
"v_fmac_f32 v30, v14, v24\n"
"v_mfma_f32_16x16x32_bf16 v[64:67], a[84:87], a[16:19], v[64:67]\n"
"buffer_load_dwordx4 v1, s[40:43], s79 offen offset:0 lds\n"
"v_mfma_f32_16x16x32_bf16 v[68:71], a[84:87], a[20:23], v[68:71]\n"
"ds_read_b64_tr_b16 a[100:101], v16 offset:35904\n"
"v_fmac_f32 v30, v13, v25\n"
"v_mfma_f32_16x16x32_bf16 v[68:71], a[80:83], a[4:7], v[68:71]\n"
"ds_read_b64_tr_b16 a[102:103], v16 offset:39488\n"
"v_mul_f32 v30, v30, v26\n"
"v_mfma_f32_16x16x32_bf16 v[72:75], a[80:83], a[8:11], v[72:75]\n"
"ds_read_b128 a[36:39], v18 offset:10496\n"
"v_accvgpr_read_b32 v26, a117\n"
"v_mfma_f32_16x16x32_bf16 v[72:75], a[84:87], a[24:27], v[72:75]\n"
"v_lshlrev_b32 v26, 16, v26\n"
"v_mul_f32 v31, v28, v27\n"
"v_mfma_f32_16x16x32_bf16 v[76:79], a[84:87], a[28:31], v[76:79]\n"
"ds_read_b64_tr_b16 a[64:65], v16 offset:43072\n"
"v_accvgpr_read_b32 v27, a133\n"
"v_mfma_f32_16x16x32_bf16 v[76:79], a[80:83], a[12:15], v[76:79]\n"
"ds_read_b64_tr_b16 a[66:67], v16 offset:46656\n"
"s_waitcnt lgkmcnt(8)\n"
"v_mfma_f32_16x16x32_bf16 v[92:95], a[88:91], a[12:15], v[92:95]\n"
"ds_read_b64_tr_b16 a[68:69], v16 offset:50240\n"
"v_lshlrev_b32 v27, 16, v27\n"
"v_mfma_f32_16x16x32_bf16 v[92:95], a[92:95], a[28:31], v[92:95]\n"
"buffer_load_dwordx4 v2, s[40:43], s79 offen offset:1024 lds\n"
"v_mfma_f32_16x16x32_bf16 v[88:91], a[92:95], a[24:27], v[88:91]\n"
"ds_read_b128 a[40:43], v18 offset:12544\n"
"v_fmac_f32 v31, v15, v24\n"
"v_mfma_f32_16x16x32_bf16 v[88:91], a[88:91], a[8:11], v[88:91]\n"
"ds_read_b64_tr_b16 a[70:71], v16 offset:53824\n"
"v_fmac_f32 v31, v14, v25\n"
"v_mfma_f32_16x16x32_bf16 v[84:87], a[88:91], a[4:7], v[84:87]\n"
"ds_read_b64_tr_b16 a[72:73], v16 offset:28768\n"
"v_fmac_f32 v31, v13, v26\n"
"v_mfma_f32_16x16x32_bf16 v[84:87], a[92:95], a[20:23], v[84:87]\n"
"v_mul_f32 v31, v31, v27\n"
"v_cvt_pk_bf16_f32 v22, v31, v30\n"
"v_mfma_f32_16x16x32_bf16 v[80:83], a[92:95], a[16:19], v[80:83]\n"
"ds_read_b128 a[44:47], v18 offset:14592\n"
"v_accvgpr_read_b32 v27, a119\n"
"v_mfma_f32_16x16x32_bf16 v[80:83], a[88:91], a[0:3], v[80:83]\n"
"ds_read_b64_tr_b16 a[74:75], v16 offset:32352\n"
"s_waitcnt lgkmcnt(9)\n"
"v_mfma_f32_16x16x32_bf16 v[96:99], a[96:99], a[0:3], v[96:99]\n"
"ds_read_b64_tr_b16 a[76:77], v16 offset:35936\n"
"v_lshlrev_b32 v27, 16, v27\n"
"v_mfma_f32_16x16x32_bf16 v[96:99], a[100:103], a[16:19], v[96:99]\n"
"buffer_load_dwordx4 v3, s[40:43], s79 offen offset:2048 lds\n"
"v_mfma_f32_16x16x32_bf16 v[100:103], a[100:103], a[20:23], v[100:103]\n"
"ds_read_b64_tr_b16 a[78:79], v16 offset:39520\n"
"v_mul_f32 v30, v28, v24\n"
"v_mfma_f32_16x16x32_bf16 v[100:103], a[96:99], a[4:7], v[100:103]\n"
"ds_read_b128 a[48:51], v19 offset:8448\n"
"v_accvgpr_read_b32 v24, a135\n"
"v_mfma_f32_16x16x32_bf16 v[104:107], a[96:99], a[8:11], v[104:107]\n"
"ds_read_b64_tr_b16 a[80:81], v16 offset:43104\n"
"v_lshlrev_b32 v24, 16, v24\n"
"v_mfma_f32_16x16x32_bf16 v[104:107], a[100:103], a[24:27], v[104:107]\n"
"v_fmac_f32 v30, v15, v25\n"
"v_fmac_f32 v30, v14, v26\n"
"v_mfma_f32_16x16x32_bf16 v[108:111], a[100:103], a[28:31], v[108:111]\n"
"ds_read_b64_tr_b16 a[82:83], v16 offset:46688\n"
"v_fmac_f32 v30, v13, v27\n"
"v_mfma_f32_16x16x32_bf16 v[108:111], a[96:99], a[12:15], v[108:111]\n"
"ds_read_b64_tr_b16 a[84:85], v16 offset:50272\n"
"s_waitcnt lgkmcnt(9)\n"
"v_mfma_f32_16x16x32_bf16 v[124:127], a[64:67], a[12:15], v[124:127]\n"
"ds_read_b128 a[52:55], v19 offset:10496\n"
"v_mul_f32 v30, v30, v24\n"
"v_mfma_f32_16x16x32_bf16 v[124:127], a[68:71], a[28:31], v[124:127]\n"
"buffer_load_dwordx4 v4, s[40:43], s79 offen offset:3072 lds\n"
"v_mfma_f32_16x16x32_bf16 v[120:123], a[68:71], a[24:27], v[120:123]\n"
"ds_read_b64_tr_b16 a[86:87], v16 offset:53856\n"
"v_accvgpr_read_b32 v24, a121\n"
"v_mfma_f32_16x16x32_bf16 v[120:123], a[64:67], a[8:11], v[120:123]\n"
"ds_read_b64_tr_b16 a[88:89], v16 offset:28800\n"
"v_lshlrev_b32 v24, 16, v24\n"
"v_mfma_f32_16x16x32_bf16 v[116:119], a[64:67], a[4:7], v[116:119]\n"
"ds_read_b64_tr_b16 a[90:91], v16 offset:32384\n"
"s_lshr_b32 s24, s33, 3\n"
"v_mfma_f32_16x16x32_bf16 v[116:119], a[68:71], a[20:23], v[116:119]\n"
"s_lshl2_add_u32 s24, s24, s34\n"
"v_mul_f32 v31, v28, v25\n"
"v_mfma_f32_16x16x32_bf16 v[112:115], a[68:71], a[16:19], v[112:115]\n"
"ds_read_b128 a[56:59], v19 offset:12544\n"
"v_accvgpr_read_b32 v25, a137\n"
"v_mfma_f32_16x16x32_bf16 v[112:115], a[64:67], a[0:3], v[112:115]\n"
"ds_read_b64_tr_b16 a[92:93], v16 offset:35968\n"
"s_waitcnt lgkmcnt(10)\n"
"v_mfma_f32_16x16x32_bf16 v[128:131], a[72:75], a[0:3], v[128:131]\n"
"ds_read_b64_tr_b16 a[94:95], v16 offset:39552\n"
"s_add_u32 m0, s77, 0x1000\n"
"v_mfma_f32_16x16x32_bf16 v[128:131], a[76:79], a[16:19], v[128:131]\n"
"buffer_load_dwordx4 v5, s[40:43], s79 offen offset:0 lds\n"
"v_mfma_f32_16x16x32_bf16 v[132:135], a[76:79], a[20:23], v[132:135]\n"
"ds_read_b64_tr_b16 a[96:97], v16 offset:43136\n"
"s_lshl_b32 s24, s24, 7\n"
"v_mfma_f32_16x16x32_bf16 v[132:135], a[72:75], a[4:7], v[132:135]\n"
"ds_read_b128 a[60:63], v19 offset:14592\n"
"v_mov_b32 v17, s24\n"
"v_mfma_f32_16x16x32_bf16 v[136:139], a[72:75], a[8:11], v[136:139]\n"
"ds_read_b64_tr_b16 a[98:99], v16 offset:46720\n"
"v_lshlrev_b32 v25, 16, v25\n"
"v_mfma_f32_16x16x32_bf16 v[136:139], a[76:79], a[24:27], v[136:139]\n"
"s_getpc_b64 s[24:25]\n"
".Lfk_ct3:\n"
"s_add_u32 s24, s24, 0xbb8\n"
"s_addc_u32 s25, s25, 0\n"
"global_load_dword a196, v17, s[24:25]\n"
"v_mfma_f32_16x16x32_bf16 v[140:143], a[76:79], a[28:31], v[140:143]\n"
"ds_read_b64_tr_b16 a[100:101], v16 offset:50304\n"
"v_fmac_f32 v31, v15, v26\n"
"v_mfma_f32_16x16x32_bf16 v[140:143], a[72:75], a[12:15], v[140:143]\n"
"ds_read_b64_tr_b16 a[102:103], v16 offset:53888\n"
"s_waitcnt lgkmcnt(10)\n"
"v_mfma_f32_16x16x32_bf16 v[156:159], a[80:83], a[12:15], v[156:159]\n"
"s_waitcnt lgkmcnt(3)\n"
"v_fmac_f32 v31, v14, v27\n"
"v_mfma_f32_16x16x32_bf16 v[156:159], a[84:87], a[28:31], v[156:159]\n"
"buffer_load_dwordx4 v6, s[40:43], s79 offen offset:1024 lds\n"
"v_mfma_f32_16x16x32_bf16 v[152:155], a[84:87], a[24:27], v[152:155]\n"
"ds_read_b64_tr_b16 a[64:65], v16 offset:28832\n"
"v_fmac_f32 v31, v13, v24\n"
"v_mfma_f32_16x16x32_bf16 v[152:155], a[80:83], a[8:11], v[152:155]\n"
"ds_read_b64_tr_b16 a[66:67], v16 offset:32416\n"
"v_mul_f32 v31, v31, v25\n"
"v_mfma_f32_16x16x32_bf16 v[148:151], a[80:83], a[4:7], v[148:151]\n"
"v_cvt_pk_bf16_f32 v21, v31, v30\n"
"v_accvgpr_read_b32 v25, a123\n"
"v_mfma_f32_16x16x32_bf16 v[148:151], a[84:87], a[20:23], v[148:151]\n"
"v_lshlrev_b32 v25, 16, v25\n"
"v_mul_f32 v30, v28, v26\n"
"v_mfma_f32_16x16x32_bf16 v[144:147], a[84:87], a[16:19], v[144:147]\n"
"ds_read_b64_tr_b16 a[68:69], v16 offset:36000\n"
"v_accvgpr_read_b32 v26, a139\n"
"v_mfma_f32_16x16x32_bf16 v[144:147], a[80:83], a[0:3], v[144:147]\n"
"ds_read_b64_tr_b16 a[70:71], v16 offset:39584\n"
"v_mfma_f32_16x16x32_bf16 v[160:163], a[88:91], a[0:3], v[160:163]\n"
"ds_read_b64_tr_b16 a[72:73], v16 offset:43168\n"
"v_lshlrev_b32 v26, 16, v26\n"
"v_mfma_f32_16x16x32_bf16 v[160:163], a[92:95], a[16:19], v[160:163]\n"
"buffer_load_dwordx4 v7, s[40:43], s79 offen offset:2048 lds\n"
"v_mfma_f32_16x16x32_bf16 v[164:167], a[92:95], a[20:23], v[164:167]\n"
"v_fmac_f32 v30, v15, v27\n"
"v_fmac_f32 v30, v14, v24\n"
"v_mfma_f32_16x16x32_bf16 v[164:167], a[88:91], a[4:7], v[164:167]\n"
"ds_read_b64_tr_b16 a[74:75], v16 offset:46752\n"
"v_fmac_f32 v30, v13, v25\n"
"v_mfma_f32_16x16x32_bf16 v[168:171], a[88:91], a[8:11], v[168:171]\n"
"ds_read_b64_tr_b16 a[76:77], v16 offset:50336\n"
"s_add_u32 m0, s78, 0x2100\n"
"v_mfma_f32_16x16x32_bf16 v[168:171], a[92:95], a[24:27], v[168:171]\n"
"v_mul_f32 v30, v30, v26\n"
"v_accvgpr_read_b32 v26, a125\n"
"v_mfma_f32_16x16x32_bf16 v[172:175], a[92:95], a[28:31], v[172:175]\n"
"ds_read_b64_tr_b16 a[78:79], v16 offset:53920\n"
"s_add_u32 s80, s96, 4\n"
"v_mfma_f32_16x16x32_bf16 v[172:175], a[88:91], a[12:15], v[172:175]\n"
"s_and_b32 s80, s80, 3\n"
"s_waitcnt lgkmcnt(8)\n"
"v_mfma_f32_16x16x32_bf16 v[188:191], a[96:99], a[12:15], v[188:191]\n"
"ds_read_b64_tr_b16 a[80:81], v16 offset:28864\n"
"s_add_u32 s80, s80, 12\n"
"v_mfma_f32_16x16x32_bf16 v[188:191], a[100:103], a[28:31], v[188:191]\n"
"v_lshlrev_b32 v26, 16, v26\n"
"v_mul_f32 v31, v28, v27\n"
"v_mfma_f32_16x16x32_bf16 v[184:187], a[100:103], a[24:27], v[184:187]\n"
"ds_read_b64_tr_b16 a[82:83], v16 offset:32448\n"
"s_lshl_b32 s80, s80, 7\n"
"v_mfma_f32_16x16x32_bf16 v[184:187], a[96:99], a[8:11], v[184:187]\n"
"ds_read_b64_tr_b16 a[84:85], v16 offset:36032\n"
"v_accvgpr_read_b32 v27, a141\n"
"v_mfma_f32_16x16x32_bf16 v[180:183], a[96:99], a[4:7], v[180:183]\n"
"buffer_load_dwordx4 v8, s[36:39], s80 offen offset:0 lds\n"
"v_mfma_f32_16x16x32_bf16 v[180:183], a[100:103], a[20:23], v[180:183]\n"
"v_lshlrev_b32 v27, 16, v27\n"
"v_fmac_f32 v31, v15, v24\n"
"v_mfma_f32_16x16x32_bf16 v[176:179], a[100:103], a[16:19], v[176:179]\n"
"ds_read_b64_tr_b16 a[86:87], v16 offset:39616\n"
"v_fmac_f32 v31, v14, v25\n"
"v_mfma_f32_16x16x32_bf16 v[176:179], a[96:99], a[0:3], v[176:179]\n"
"ds_read_b64_tr_b16 a[88:89], v16 offset:43200\n"
"s_waitcnt lgkmcnt(9)\n"
"v_mfma_f32_16x16x32_bf16 v[192:195], a[64:67], a[0:3], v[192:195]\n"
"ds_read_b64_tr_b16 a[90:91], v16 offset:46784\n"
"v_fmac_f32 v31, v13, v26\n"
"v_mfma_f32_16x16x32_bf16 v[192:195], a[68:71], a[16:19], v[192:195]\n"
"buffer_load_dwordx4 v9, s[36:39], s80 offen offset:1024 lds\n"
"v_mfma_f32_16x16x32_bf16 v[196:199], a[68:71], a[20:23], v[196:199]\n"
"v_mul_f32 v31, v31, v27\n"
"v_cvt_pk_bf16_f32 v20, v31, v30\n"
"v_mfma_f32_16x16x32_bf16 v[196:199], a[64:67], a[4:7], v[196:199]\n"
"ds_read_b64_tr_b16 a[92:93], v16 offset:50368\n"
"v_add_u32 v30, s18, v12\n"
"v_mfma_f32_16x16x32_bf16 v[200:203], a[64:67], a[8:11], v[200:203]\n"
"ds_read_b64_tr_b16 a[94:95], v16 offset:53952\n"
"v_accvgpr_read_b32 v31, a192\n"
"v_mfma_f32_16x16x32_bf16 v[200:203], a[68:71], a[24:27], v[200:203]\n"
"buffer_load_dwordx4 v10, s[36:39], s80 offen offset:2048 lds\n"
"v_mfma_f32_16x16x32_bf16 v[204:207], a[68:71], a[28:31], v[204:207]\n"
"ds_write_b64 v30, v[20:21] offset:0\n"
"v_mfma_f32_16x16x32_bf16 v[204:207], a[64:67], a[12:15], v[204:207]\n"
"ds_write_b64 v30, v[22:23] offset:8\n"
"s_waitcnt lgkmcnt(10)\n"
"v_mfma_f32_16x16x32_bf16 v[220:223], a[72:75], a[12:15], v[220:223]\n"
"ds_read_b128 a[184:187], v31 offset:0\n"
"v_add_u32 v16, 0xe000, v16\n"
"v_mfma_f32_16x16x32_bf16 v[220:223], a[76:79], a[28:31], v[220:223]\n"
"buffer_load_dwordx4 v11, s[36:39], s80 offen offset:3072 lds\n"
"v_mfma_f32_16x16x32_bf16 v[216:219], a[76:79], a[24:27], v[216:219]\n"
"v_accvgpr_read_b32 v14, a148\n"
"v_lshlrev_b32 v13, 16, v14\n"
"v_mfma_f32_16x16x32_bf16 v[216:219], a[72:75], a[8:11], v[216:219]\n"
"s_add_u32 m0, s78, 0x3100\n"
"s_or_b32 s80, s80, 0x10000\n"
"v_mfma_f32_16x16x32_bf16 v[212:215], a[72:75], a[4:7], v[212:215]\n"
"v_and_b32 v14, 0xffff0000, v14\n"
"v_accvgpr_read_b32 v28, a149\n"
"v_mfma_f32_16x16x32_bf16 v[212:215], a[76:79], a[20:23], v[212:215]\n"
"buffer_load_dwordx4 v8, s[36:39], s80 offen offset:0 lds\n"
"v_mfma_f32_16x16x32_bf16 v[208:211], a[76:79], a[16:19], v[208:211]\n"
"v_lshlrev_b32 v15, 16, v28\n"
"v_and_b32 v28, 0xffff0000, v28\n"
"v_mfma_f32_16x16x32_bf16 v[208:211], a[72:75], a[0:3], v[208:211]\n"
"s_waitcnt vmcnt(27) lgkmcnt(0)\n"
"s_barrier\n"
"v_mfma_f32_16x16x32_bf16 v[224:227], a[80:83], a[0:3], v[224:227]\n"
"ds_read_b64_tr_b16 a[96:97], v16 offset:0\n"
"v_accvgpr_read_b32 v24, a105\n"
"v_mfma_f32_16x16x32_bf16 v[224:227], a[84:87], a[16:19], v[224:227]\n"
"buffer_load_dwordx4 v9, s[36:39], s80 offen offset:1024 lds\n"
"v_mfma_f32_16x16x32_bf16 v[228:231], a[84:87], a[20:23], v[228:231]\n"
"ds_read_b64_tr_b16 a[98:99], v16 offset:3584\n"
"v_and_b32 v24, 0xffff0000, v24\n"
"v_mfma_f32_16x16x32_bf16 v[228:231], a[80:83], a[4:7], v[228:231]\n"
"v_cndmask_b32 v24, 0, v24, vcc\n"
"v_accvgpr_read_b32 v25, a107\n"
"v_mfma_f32_16x16x32_bf16 v[232:235], a[80:83], a[8:11], v[232:235]\n"
"ds_read_b64_tr_b16 a[100:101], v16 offset:7168\n"
"v_and_b32 v25, 0xffff0000, v25\n"
"v_mfma_f32_16x16x32_bf16 v[232:235], a[84:87], a[24:27], v[232:235]\n"
"buffer_load_dwordx4 v10, s[36:39], s80 offen offset:2048 lds\n"
"v_mfma_f32_16x16x32_bf16 v[236:239], a[84:87], a[28:31], v[236:239]\n"
"ds_read_b64_tr_b16 a[102:103], v16 offset:10752\n"
"v_cndmask_b32 v25, 0, v25, vcc\n"
"v_mfma_f32_16x16x32_bf16 v[236:239], a[80:83], a[12:15], v[236:239]\n"
"v_accvgpr_read_b32 v26, a109\n"
"v_mfma_f32_16x16x32_bf16 v[252:255], a[88:91], a[12:15], v[252:255]\n"
"ds_read_b64_tr_b16 a[64:65], v16 offset:14336\n"
"v_and_b32 v26, 0xffff0000, v26\n"
"v_mfma_f32_16x16x32_bf16 v[252:255], a[92:95], a[28:31], v[252:255]\n"
"buffer_load_dwordx4 v11, s[36:39], s80 offen offset:3072 lds\n"
"v_mfma_f32_16x16x32_bf16 v[248:251], a[92:95], a[24:27], v[248:251]\n"
"ds_read_b64_tr_b16 a[66:67], v16 offset:17920\n"
"s_add_u32 m0, s77, 0x7000\n"
"v_mfma_f32_16x16x32_bf16 v[248:251], a[88:91], a[8:11], v[248:251]\n"
"s_add_u32 s79, s96, 4\n"
"s_and_b32 s79, s79, 3\n"
"v_mfma_f32_16x16x32_bf16 v[244:247], a[88:91], a[4:7], v[244:247]\n"
"ds_read_b64_tr_b16 a[68:69], v16 offset:21504\n"
"s_add_u32 s79, s79, 12\n"
"v_mfma_f32_16x16x32_bf16 v[244:247], a[92:95], a[20:23], v[244:247]\n"
"v_cndmask_b32 v26, 0, v26, vcc\n"
"v_accvgpr_read_b32 v27, a111\n"
"v_mfma_f32_16x16x32_bf16 v[240:243], a[92:95], a[16:19], v[240:243]\n"
"ds_read_b64_tr_b16 a[70:71], v16 offset:25088\n"
"s_mul_i32 s79, s79, 0x70000\n"
"v_mfma_f32_16x16x32_bf16 v[240:243], a[88:91], a[0:3], v[240:243]\n"
"buffer_load_dwordx4 v1, s[40:43], s79 offen offset:0 lds\n"
"s_waitcnt lgkmcnt(4)\n"
"v_mfma_f32_16x16x32_bf16 v[32:35], a[96:99], a[32:35], v[32:35]\n"
"ds_read_b64_tr_b16 a[72:73], v16 offset:32\n"
"v_and_b32 v27, 0xffff0000, v27\n"
"v_mfma_f32_16x16x32_bf16 v[32:35], a[100:103], a[48:51], v[32:35]\n"
"v_mul_f32 v30, v28, v24\n"
"v_accvgpr_read_b32 v24, a127\n"
"v_mfma_f32_16x16x32_bf16 v[36:39], a[100:103], a[52:55], v[36:39]\n"
"ds_read_b64_tr_b16 a[74:75], v16 offset:3616\n"
"v_and_b32 v24, 0xffff0000, v24\n"
"v_mfma_f32_16x16x32_bf16 v[36:39], a[96:99], a[36:39], v[36:39]\n"
"ds_read_b64_tr_b16 a[76:77], v16 offset:7200\n"
"v_fmac_f32 v30, v15, v25\n"
"v_mfma_f32_16x16x32_bf16 v[40:43], a[96:99], a[40:43], v[40:43]\n"
"buffer_load_dwordx4 v2, s[40:43], s79 offen offset:1024 lds\n"
"v_mfma_f32_16x16x32_bf16 v[40:43], a[100:103], a[56:59], v[40:43]\n"
"v_fmac_f32 v30, v14, v26\n"
"v_fmac_f32 v30, v13, v27\n"
"v_mfma_f32_16x16x32_bf16 v[44:47], a[100:103], a[60:63], v[44:47]\n"
"ds_read_b64_tr_b16 a[78:79], v16 offset:10784\n"
"v_mul_f32 v30, v30, v24\n"
"v_mfma_f32_16x16x32_bf16 v[44:47], a[96:99], a[44:47], v[44:47]\n"
"ds_read_b64_tr_b16 a[80:81], v16 offset:14368\n"
"s_waitcnt lgkmcnt(5)\n"
"v_mfma_f32_16x16x32_bf16 v[60:63], a[64:67], a[44:47], v[60:63]\n"
"ds_read_b64_tr_b16 a[82:83], v16 offset:17952\n"
"v_accvgpr_read_b32 v24, a113\n"
"v_mfma_f32_16x16x32_bf16 v[60:63], a[68:71], a[60:63], v[60:63]\n"
"buffer_load_dwordx4 v3, s[40:43], s79 offen offset:2048 lds\n"
"v_mfma_f32_16x16x32_bf16 v[56:59], a[68:71], a[56:59], v[56:59]\n"
"v_and_b32 v24, 0xffff0000, v24\n"
"v_mul_f32 v31, v28, v25\n"
"v_mfma_f32_16x16x32_bf16 v[56:59], a[64:67], a[40:43], v[56:59]\n"
"ds_read_b64_tr_b16 a[84:85], v16 offset:21536\n"
"v_accvgpr_read_b32 v25, a129\n"
"v_mfma_f32_16x16x32_bf16 v[52:55], a[64:67], a[36:39], v[52:55]\n"
"ds_read_b64_tr_b16 a[86:87], v16 offset:25120\n"
"s_waitcnt vmcnt(19)\n"
"v_mfma_f32_16x16x32_bf16 v[52:55], a[68:71], a[52:55], v[52:55]\n"
"buffer_load_dwordx4 v4, s[40:43], s79 offen offset:3072 lds\n"
"v_mfma_f32_16x16x32_bf16 v[48:51], a[68:71], a[48:51], v[48:51]\n"
"ds_read_b64_tr_b16 a[88:89], v16 offset:64\n"
"v_and_b32 v25, 0xffff0000, v25\n"
"v_mfma_f32_16x16x32_bf16 v[48:51], a[64:67], a[32:35], v[48:51]\n"
"ds_read_b128 a[0:3], v18 offset:0\n"
"s_waitcnt lgkmcnt(6)\n"
"v_mfma_f32_16x16x32_bf16 v[64:67], a[72:75], a[32:35], v[64:67]\n"
"ds_read_b64_tr_b16 a[90:91], v16 offset:3648\n"
"s_add_u32 m0, s77, 0x8000\n"
"v_mfma_f32_16x16x32_bf16 v[64:67], a[76:79], a[48:51], v[64:67]\n"
"buffer_load_dwordx4 v5, s[40:43], s79 offen offset:0 lds\n"
"v_mfma_f32_16x16x32_bf16 v[68:71], a[76:79], a[52:55], v[68:71]\n"
"ds_read_b64_tr_b16 a[92:93], v16 offset:7232\n"
"v_fmac_f32 v31, v15, v26\n"
"v_mfma_f32_16x16x32_bf16 v[68:71], a[72:75], a[36:39], v[68:71]\n"
"ds_read_b64_tr_b16 a[94:95], v16 offset:10816\n"
"v_fmac_f32 v31, v14, v27\n"
"v_mfma_f32_16x16x32_bf16 v[72:75], a[72:75], a[40:43], v[72:75]\n"
"ds_read_b128 a[4:7], v18 offset:2048\n"
"v_fmac_f32 v31, v13, v24\n"
"v_mfma_f32_16x16x32_bf16 v[72:75], a[76:79], a[56:59], v[72:75]\n"
"buffer_load_dwordx4 v6, s[40:43], s79 offen offset:1024 lds\n"
"v_mfma_f32_16x16x32_bf16 v[76:79], a[76:79], a[60:63], v[76:79]\n"
"ds_read_b64_tr_b16 a[96:97], v16 offset:14400\n"
"v_mul_f32 v31, v31, v25\n"
"v_mfma_f32_16x16x32_bf16 v[76:79], a[72:75], a[44:47], v[76:79]\n"
"ds_read_b64_tr_b16 a[98:99], v16 offset:17984\n"
"s_waitcnt lgkmcnt(8)\n"
"v_mfma_f32_16x16x32_bf16 v[92:95], a[80:83], a[44:47], v[92:95]\n"
"ds_read_b64_tr_b16 a[100:101], v16 offset:21568\n"
"v_cvt_pk_bf16_f32 v23, v31, v30\n"
"v_mfma_f32_16x16x32_bf16 v[92:95], a[84:87], a[60:63], v[92:95]\n"
"buffer_load_dwordx4 v7, s[40:43], s79 offen offset:2048 lds\n"
"v_mfma_f32_16x16x32_bf16 v[88:91], a[84:87], a[56:59], v[88:91]\n"
"ds_read_b128 a[8:11], v18 offset:4096\n"
"v_accvgpr_read_b32 v25, a115\n"
"v_mfma_f32_16x16x32_bf16 v[88:91], a[80:83], a[40:43], v[88:91]\n"
"ds_read_b64_tr_b16 a[102:103], v16 offset:25152\n"
"v_and_b32 v25, 0xffff0000, v25\n"
"v_mfma_f32_16x16x32_bf16 v[84:87], a[80:83], a[36:39], v[84:87]\n"
"ds_read_b64_tr_b16 a[64:65], v16 offset:96\n"
"v_mul_f32 v30, v28, v26\n"
"v_mfma_f32_16x16x32_bf16 v[84:87], a[84:87], a[52:55], v[84:87]\n"
"v_accvgpr_read_b32 v26, a131\n"
"v_and_b32 v26, 0xffff0000, v26\n"
"v_mfma_f32_16x16x32_bf16 v[80:83], a[84:87], a[48:51], v[80:83]\n"
"ds_read_b128 a[12:15], v18 offset:6144\n"
"v_fmac_f32 v30, v15, v27\n"
"v_mfma_f32_16x16x32_bf16 v[80:83], a[80:83], a[32:35], v[80:83]\n"
"ds_read_b64_tr_b16 a[66:67], v16 offset:3680\n"
"s_waitcnt lgkmcnt(9)\n"
"v_mfma_f32_16x16x32_bf16 v[96:99], a[88:91], a[32:35], v[96:99]\n"
"ds_read_b64_tr_b16 a[68:69], v16 offset:7264\n"
"v_fmac_f32 v30, v14, v24\n"
"v_mfma_f32_16x16x32_bf16 v[96:99], a[92:95], a[48:51], v[96:99]\n"
"v_fmac_f32 v30, v13, v25\n"
"v_mul_f32 v30, v30, v26\n"
"v_mfma_f32_16x16x32_bf16 v[100:103], a[92:95], a[52:55], v[100:103]\n"
"ds_read_b64_tr_b16 a[70:71], v16 offset:10848\n"
"v_accvgpr_read_b32 v26, a117\n"
"v_mfma_f32_16x16x32_bf16 v[100:103], a[88:91], a[36:39], v[100:103]\n"
"ds_read_b128 a[16:19], v19 offset:0\n"
"v_and_b32 v26, 0xffff0000, v26\n"
"v_mfma_f32_16x16x32_bf16 v[104:107], a[88:91], a[40:43], v[104:107]\n"
"ds_read_b64_tr_b16 a[72:73], v16 offset:14432\n"
"v_mul_f32 v31, v28, v27\n"
"v_mfma_f32_16x16x32_bf16 v[104:107], a[92:95], a[56:59], v[104:107]\n"
"v_accvgpr_read_b32 v27, a133\n"
"v_and_b32 v27, 0xffff0000, v27\n"
"v_mfma_f32_16x16x32_bf16 v[108:111], a[92:95], a[60:63], v[108:111]\n"
"ds_read_b64_tr_b16 a[74:75], v16 offset:18016\n"
"v_fmac_f32 v31, v15, v24\n"
"v_mfma_f32_16x16x32_bf16 v[108:111], a[88:91], a[44:47], v[108:111]\n"
"ds_read_b64_tr_b16 a[76:77], v16 offset:21600\n"
"s_waitcnt lgkmcnt(9)\n"
"v_mfma_f32_16x16x32_bf16 v[124:127], a[96:99], a[44:47], v[124:127]\n"
"ds_read_b128 a[20:23], v19 offset:2048\n"
"v_fmac_f32 v31, v14, v25\n"
"v_mfma_f32_16x16x32_bf16 v[124:127], a[100:103], a[60:63], v[124:127]\n"
"v_fmac_f32 v31, v13, v26\n"
"v_mul_f32 v31, v31, v27\n"
"v_mfma_f32_16x16x32_bf16 v[120:123], a[100:103], a[56:59], v[120:123]\n"
"ds_read_b64_tr_b16 a[78:79], v16 offset:25184\n"
"v_cvt_pk_bf16_f32 v22, v31, v30\n"
"v_mfma_f32_16x16x32_bf16 v[120:123], a[96:99], a[40:43], v[120:123]\n"
"ds_read_b64_tr_b16 a[80:81], v16 offset:128\n"
"v_accvgpr_read_b32 v27, a119\n"
"v_mfma_f32_16x16x32_bf16 v[116:119], a[96:99], a[36:39], v[116:119]\n"
"ds_read_b64_tr_b16 a[82:83], v16 offset:3712\n"
"v_and_b32 v27, 0xffff0000, v27\n"
"v_mfma_f32_16x16x32_bf16 v[116:119], a[100:103], a[52:55], v[116:119]\n"
"v_mul_f32 v30, v28, v24\n"
"v_accvgpr_read_b32 v24, a135\n"
"v_mfma_f32_16x16x32_bf16 v[112:115], a[100:103], a[48:51], v[112:115]\n"
"ds_read_b128 a[24:27], v19 offset:4096\n"
"v_and_b32 v24, 0xffff0000, v24\n"
"v_mfma_f32_16x16x32_bf16 v[112:115], a[96:99], a[32:35], v[112:115]\n"
"ds_read_b64_tr_b16 a[84:85], v16 offset:7296\n"
"s_waitcnt lgkmcnt(10)\n"
"v_mfma_f32_16x16x32_bf16 v[128:131], a[64:67], a[32:35], v[128:131]\n"
"ds_read_b64_tr_b16 a[86:87], v16 offset:10880\n"
"v_fmac_f32 v30, v15, v25\n"
"v_mfma_f32_16x16x32_bf16 v[128:131], a[68:71], a[48:51], v[128:131]\n"
"v_fmac_f32 v30, v14, v26\n"
"v_fmac_f32 v30, v13, v27\n"
"v_mfma_f32_16x16x32_bf16 v[132:135], a[68:71], a[52:55], v[132:135]\n"
"ds_read_b64_tr_b16 a[88:89], v16 offset:14464\n"
"v_mul_f32 v30, v30, v24\n"
"v_mfma_f32_16x16x32_bf16 v[132:135], a[64:67], a[36:39], v[132:135]\n"
"ds_read_b128 a[28:31], v19 offset:6144\n"
"v_accvgpr_read_b32 v24, a121\n"
"v_mfma_f32_16x16x32_bf16 v[136:139], a[64:67], a[40:43], v[136:139]\n"
"ds_read_b64_tr_b16 a[90:91], v16 offset:18048\n"
"s_add_u32 m0, s78, 0x0\n"
"v_mfma_f32_16x16x32_bf16 v[136:139], a[68:71], a[56:59], v[136:139]\n"
"v_and_b32 v24, 0xffff0000, v24\n"
"v_mul_f32 v31, v28, v25\n"
"v_mfma_f32_16x16x32_bf16 v[140:143], a[68:71], a[60:63], v[140:143]\n"
"ds_read_b64_tr_b16 a[92:93], v16 offset:21632\n"
"s_add_u32 s80, s96, 5\n"
"v_mfma_f32_16x16x32_bf16 v[140:143], a[64:67], a[44:47], v[140:143]\n"
"ds_read_b64_tr_b16 a[94:95], v16 offset:25216\n"
"s_waitcnt lgkmcnt(10)\n"
"v_mfma_f32_16x16x32_bf16 v[156:159], a[72:75], a[44:47], v[156:159]\n"
"s_waitcnt lgkmcnt(3)\n"
"s_and_b32 s80, s80, 3\n"
"v_mfma_f32_16x16x32_bf16 v[156:159], a[76:79], a[60:63], v[156:159]\n"
"v_accvgpr_read_b32 v25, a137\n"
"v_and_b32 v25, 0xffff0000, v25\n"
"v_mfma_f32_16x16x32_bf16 v[152:155], a[76:79], a[56:59], v[152:155]\n"
"ds_read_b64_tr_b16 a[96:97], v16 offset:160\n"
"s_add_u32 s80, s80, 12\n"
"v_mfma_f32_16x16x32_bf16 v[152:155], a[72:75], a[40:43], v[152:155]\n"
"ds_read_b64_tr_b16 a[98:99], v16 offset:3744\n"
"s_lshl_b32 s80, s80, 7\n"
"v_mfma_f32_16x16x32_bf16 v[148:151], a[72:75], a[36:39], v[148:151]\n"
"buffer_load_dwordx4 v8, s[36:39], s80 offen offset:0 lds\n"
"v_mfma_f32_16x16x32_bf16 v[148:151], a[76:79], a[52:55], v[148:151]\n"
"v_fmac_f32 v31, v15, v26\n"
"v_fmac_f32 v31, v14, v27\n"
"v_mfma_f32_16x16x32_bf16 v[144:147], a[76:79], a[48:51], v[144:147]\n"
"ds_read_b64_tr_b16 a[100:101], v16 offset:7328\n"
"v_fmac_f32 v31, v13, v24\n"
"v_mfma_f32_16x16x32_bf16 v[144:147], a[72:75], a[32:35], v[144:147]\n"
"ds_read_b64_tr_b16 a[102:103], v16 offset:10912\n"
"v_mfma_f32_16x16x32_bf16 v[160:163], a[80:83], a[32:35], v[160:163]\n"
"ds_read_b64_tr_b16 a[64:65], v16 offset:14496\n"
"v_mul_f32 v31, v31, v25\n"
"v_mfma_f32_16x16x32_bf16 v[160:163], a[84:87], a[48:51], v[160:163]\n"
"buffer_load_dwordx4 v9, s[36:39], s80 offen offset:1024 lds\n"
"v_mfma_f32_16x16x32_bf16 v[164:167], a[84:87], a[52:55], v[164:167]\n"
"v_cvt_pk_bf16_f32 v21, v31, v30\n"
"v_accvgpr_read_b32 v25, a123\n"
"v_mfma_f32_16x16x32_bf16 v[164:167], a[80:83], a[36:39], v[164:167]\n"
"ds_read_b64_tr_b16 a[66:67], v16 offset:18080\n"
"v_and_b32 v25, 0xffff0000, v25\n"
"v_mfma_f32_16x16x32_bf16 v[168:171], a[80:83], a[40:43], v[168:171]\n"
"ds_read_b64_tr_b16 a[68:69], v16 offset:21664\n"
"v_mul_f32 v30, v28, v26\n"
"v_mfma_f32_16x16x32_bf16 v[168:171], a[84:87], a[56:59], v[168:171]\n"
"buffer_load_dwordx4 v10, s[36:39], s80 offen offset:2048 lds\n"
"v_mfma_f32_16x16x32_bf16 v[172:175], a[84:87], a[60:63], v[172:175]\n"
"ds_read_b64_tr_b16 a[70:71], v16 offset:25248\n"
"v_accvgpr_read_b32 v26, a139\n"
"v_mfma_f32_16x16x32_bf16 v[172:175], a[80:83], a[44:47], v[172:175]\n"
"v_and_b32 v26, 0xffff0000, v26\n"
"s_waitcnt lgkmcnt(8)\n"
"v_mfma_f32_16x16x32_bf16 v[188:191], a[88:91], a[44:47], v[188:191]\n"
"ds_read_b64_tr_b16 a[72:73], v16 offset:192\n"
"v_fmac_f32 v30, v15, v27\n"
"v_mfma_f32_16x16x32_bf16 v[188:191], a[92:95], a[60:63], v[188:191]\n"
"buffer_load_dwordx4 v11, s[36:39], s80 offen offset:3072 lds\n"
"v_mfma_f32_16x16x32_bf16 v[184:187], a[92:95], a[56:59], v[184:187]\n"
"ds_read_b64_tr_b16 a[74:75], v16 offset:3776\n"
"v_fmac_f32 v30, v14, v24\n"
"v_mfma_f32_16x16x32_bf16 v[184:187], a[88:91], a[40:43], v[184:187]\n"
"ds_read_b64_tr_b16 a[76:77], v16 offset:7360\n"
"s_add_u32 m0, s78, 0x1000\n"
"v_mfma_f32_16x16x32_bf16 v[180:183], a[88:91], a[36:39], v[180:183]\n"
"s_or_b32 s80, s80, 0x10000\n"
"v_fmac_f32 v30, v13, v25\n"
"v_mfma_f32_16x16x32_bf16 v[180:183], a[92:95], a[52:55], v[180:183]\n"
"buffer_load_dwordx4 v8, s[36:39], s80 offen offset:0 lds\n"
"v_mfma_f32_16x16x32_bf16 v[176:179], a[92:95], a[48:51], v[176:179]\n"
"ds_read_b64_tr_b16 a[78:79], v16 offset:10944\n"
"v_mul_f32 v30, v30, v26\n"
"v_mfma_f32_16x16x32_bf16 v[176:179], a[88:91], a[32:35], v[176:179]\n"
"ds_read_b64_tr_b16 a[80:81], v16 offset:14528\n"
"s_waitcnt lgkmcnt(9)\n"
"v_mfma_f32_16x16x32_bf16 v[192:195], a[96:99], a[32:35], v[192:195]\n"
".Lfk_hop3450:\n"
"s_cbranch_execz .Lfk_hop4022\n"
"ds_read_b64_tr_b16 a[82:83], v16 offset:18112\n"
"v_accvgpr_read_b32 v26, a125\n"
"v_mfma_f32_16x16x32_bf16 v[192:195], a[100:103], a[48:51], v[192:195]\n"
"buffer_load_dwordx4 v9, s[36:39], s80 offen offset:1024 lds\n"
"v_mfma_f32_16x16x32_bf16 v[196:199], a[100:103], a[52:55], v[196:199]\n"
"v_and_b32 v26, 0xffff0000, v26\n"
"v_mul_f32 v31, v28, v27\n"
"v_mfma_f32_16x16x32_bf16 v[196:199], a[96:99], a[36:39], v[196:199]\n"
"ds_read_b64_tr_b16 a[84:85], v16 offset:21696\n"
"v_accvgpr_read_b32 v27, a141\n"
"v_mfma_f32_16x16x32_bf16 v[200:203], a[96:99], a[40:43], v[200:203]\n"
"ds_read_b64_tr_b16 a[86:87], v16 offset:25280\n"
"v_and_b32 v27, 0xffff0000, v27\n"
"v_mfma_f32_16x16x32_bf16 v[200:203], a[100:103], a[56:59], v[200:203]\n"
"buffer_load_dwordx4 v10, s[36:39], s80 offen offset:2048 lds\n"
"v_mfma_f32_16x16x32_bf16 v[204:207], a[100:103], a[60:63], v[204:207]\n"
"v_fmac_f32 v31, v15, v24\n"
"v_fmac_f32 v31, v14, v25\n"
"v_mfma_f32_16x16x32_bf16 v[204:207], a[96:99], a[44:47], v[204:207]\n"
"v_fmac_f32 v31, v13, v26\n"
"s_waitcnt lgkmcnt(8)\n"
"v_mfma_f32_16x16x32_bf16 v[220:223], a[64:67], a[44:47], v[220:223]\n"
"v_subrev_u32 v16, 0xe000, v16\n"
"v_mul_f32 v31, v31, v27\n"
"v_mfma_f32_16x16x32_bf16 v[220:223], a[68:71], a[60:63], v[220:223]\n"
"buffer_load_dwordx4 v11, s[36:39], s80 offen offset:3072 lds\n"
"v_mfma_f32_16x16x32_bf16 v[216:219], a[68:71], a[56:59], v[216:219]\n"
"v_cvt_pk_bf16_f32 v20, v31, v30\n"
"v_add_u32 v30, s18, v12\n"
"v_mfma_f32_16x16x32_bf16 v[216:219], a[64:67], a[40:43], v[216:219]\n"
"s_add_u32 m0, s77, 0xe000\n"
"s_add_u32 s79, s96, 5\n"
"v_mfma_f32_16x16x32_bf16 v[212:215], a[64:67], a[36:39], v[212:215]\n"
"s_and_b32 s79, s79, 3\n"
"s_add_u32 s79, s79, 12\n"
"v_mfma_f32_16x16x32_bf16 v[212:215], a[68:71], a[52:55], v[212:215]\n"
"v_accvgpr_read_b32 v31, a192\n"
"v_mfma_f32_16x16x32_bf16 v[208:211], a[68:71], a[48:51], v[208:211]\n"
"s_mul_i32 s79, s79, 0x70000\n"
"ds_write_b64 v30, v[20:21] offset:1024\n"
"v_mfma_f32_16x16x32_bf16 v[208:211], a[64:67], a[32:35], v[208:211]\n"
"s_waitcnt vmcnt(23) lgkmcnt(0)\n"
"s_barrier\n"
"v_mfma_f32_16x16x32_bf16 v[224:227], a[72:75], a[32:35], v[224:227]\n"
"ds_read_b64_tr_b16 a[88:89], v16 offset:0\n"
"v_mfma_f32_16x16x32_bf16 v[224:227], a[76:79], a[48:51], v[224:227]\n"
"buffer_load_dwordx4 v1, s[40:43], s79 offen offset:0 lds\n"
"v_mfma_f32_16x16x32_bf16 v[228:231], a[76:79], a[52:55], v[228:231]\n"
"ds_read_b64_tr_b16 a[90:91], v16 offset:3584\n"
"v_mfma_f32_16x16x32_bf16 v[228:231], a[72:75], a[36:39], v[228:231]\n"
"ds_write_b64 v30, v[22:23] offset:1032\n"
"v_mfma_f32_16x16x32_bf16 v[232:235], a[72:75], a[40:43], v[232:235]\n"
"ds_read_b64_tr_b16 a[92:93], v16 offset:7168\n"
"v_mfma_f32_16x16x32_bf16 v[232:235], a[76:79], a[56:59], v[232:235]\n"
"buffer_load_dwordx4 v2, s[40:43], s79 offen offset:1024 lds\n"
"v_mfma_f32_16x16x32_bf16 v[236:239], a[76:79], a[60:63], v[236:239]\n"
"ds_read_b64_tr_b16 a[94:95], v16 offset:10752\n"
"v_mfma_f32_16x16x32_bf16 v[236:239], a[72:75], a[44:47], v[236:239]\n"
"ds_read_b128 a[188:191], v31 offset:1024\n"
"v_mfma_f32_16x16x32_bf16 v[252:255], a[80:83], a[44:47], v[252:255]\n"
"ds_read_b64_tr_b16 a[96:97], v16 offset:14336\n"
"v_accvgpr_read_b32 v30, a193\n"
"v_mfma_f32_16x16x32_bf16 v[252:255], a[84:87], a[60:63], v[252:255]\n"
"buffer_load_dwordx4 v3, s[40:43], s79 offen offset:2048 lds\n"
"v_mfma_f32_16x16x32_bf16 v[248:251], a[84:87], a[56:59], v[248:251]\n"
"ds_read_b64_tr_b16 a[98:99], v16 offset:17920\n"
"v_add_u32 v30, s81, v30\n"
"v_mfma_f32_16x16x32_bf16 v[248:251], a[80:83], a[40:43], v[248:251]\n"
"s_waitcnt lgkmcnt(2)\n"
"s_xor_b32 s82, s33, 1\n"
"v_mfma_f32_16x16x32_bf16 v[244:247], a[80:83], a[36:39], v[244:247]\n"
"ds_read_b64_tr_b16 a[100:101], v16 offset:21504\n"
"s_lshl2_add_u32 s23, s82, s34\n"
"v_mfma_f32_16x16x32_bf16 v[244:247], a[84:87], a[52:55], v[244:247]\n"
"buffer_load_dwordx4 v4, s[40:43], s79 offen offset:3072 lds\n"
"v_mfma_f32_16x16x32_bf16 v[240:243], a[84:87], a[48:51], v[240:243]\n"
"ds_read_b64_tr_b16 a[102:103], v16 offset:25088\n"
"s_lshl_b32 s23, s23, 12\n"
"v_mfma_f32_16x16x32_bf16 v[240:243], a[80:83], a[32:35], v[240:243]\n"
"s_lshl2_add_u32 s18, s33, s34\n"
"s_lshl_b32 s18, s18, 12\n"
"v_mfma_f32_16x16x32_bf16 v[32:35], a[88:91], a[0:3], v[32:35]\n"
"ds_read_b64_tr_b16 a[64:65], v16 offset:32\n"
"s_add_u32 m0, s77, 0xf000\n"
"v_mfma_f32_16x16x32_bf16 v[32:35], a[92:95], a[16:19], v[32:35]\n"
"buffer_load_dwordx4 v5, s[40:43], s79 offen offset:0 lds\n"
"v_mfma_f32_16x16x32_bf16 v[36:39], a[92:95], a[20:23], v[36:39]\n"
"ds_read_b64_tr_b16 a[66:67], v16 offset:3616\n"
"s_lshl_b32 s99, s33, 2\n"
"v_mfma_f32_16x16x32_bf16 v[36:39], a[88:91], a[4:7], v[36:39]\n"
"ds_read_b64_tr_b16 a[68:69], v16 offset:7200\n"
"s_add_u32 s99, s99, 16384\n"
"v_mfma_f32_16x16x32_bf16 v[40:43], a[88:91], a[8:11], v[40:43]\n"
"s_add_u32 s56, s30, 0x2000000\n"
"s_addc_u32 s57, s31, 0\n"
"v_mfma_f32_16x16x32_bf16 v[40:43], a[92:95], a[24:27], v[40:43]\n"
"buffer_load_dwordx4 v6, s[40:43], s79 offen offset:1024 lds\n"
"v_mfma_f32_16x16x32_bf16 v[44:47], a[92:95], a[28:31], v[44:47]\n"
"ds_read_b64_tr_b16 a[70:71], v16 offset:10784\n"
"s_and_b32 s57, s57, 0xffff\n"
"v_mfma_f32_16x16x32_bf16 v[44:47], a[88:91], a[12:15], v[44:47]\n"
"ds_read_b64_tr_b16 a[72:73], v16 offset:14368\n"
"s_waitcnt lgkmcnt(5)\n"
"v_mfma_f32_16x16x32_bf16 v[60:63], a[96:99], a[12:15], v[60:63]\n"
"ds_read_b64_tr_b16 a[74:75], v16 offset:17952\n"
"s_mov_b32 s58, 0x400000\n"
"v_mfma_f32_16x16x32_bf16 v[60:63], a[100:103], a[28:31], v[60:63]\n"
"buffer_load_dwordx4 v7, s[40:43], s79 offen offset:2048 lds\n"
"v_mfma_f32_16x16x32_bf16 v[56:59], a[100:103], a[24:27], v[56:59]\n"
"buffer_store_dwordx4 a[176:179], v30, s[64:67], 0 offen sc1\n"
"v_mfma_f32_16x16x32_bf16 v[56:59], a[96:99], a[8:11], v[56:59]\n"
"ds_read_b64_tr_b16 a[76:77], v16 offset:21536\n"
"v_mfma_f32_16x16x32_bf16 v[52:55], a[96:99], a[4:7], v[52:55]\n"
"ds_read_b64_tr_b16 a[78:79], v16 offset:25120\n"
"s_waitcnt vmcnt(23)\n"
"v_mfma_f32_16x16x32_bf16 v[52:55], a[100:103], a[20:23], v[52:55]\n"
"buffer_store_dwordx4 a[180:183], v30, s[64:67], s8 offen sc1\n"
"v_mfma_f32_16x16x32_bf16 v[48:51], a[100:103], a[16:19], v[48:51]\n"
"ds_read_b64_tr_b16 a[80:81], v16 offset:64\n"
"v_mfma_f32_16x16x32_bf16 v[48:51], a[96:99], a[0:3], v[48:51]\n"
"ds_read_b128 a[32:35], v18 offset:8448\n"
"s_waitcnt lgkmcnt(6)\n"
"v_mfma_f32_16x16x32_bf16 v[64:67], a[64:67], a[0:3], v[64:67]\n"
"ds_read_b64_tr_b16 a[82:83], v16 offset:3648\n"
"v_mfma_f32_16x16x32_bf16 v[64:67], a[68:71], a[16:19], v[64:67]\n"
"buffer_store_dwordx4 a[184:187], v30, s[64:67], s9 offen sc1\n"
"v_mfma_f32_16x16x32_bf16 v[68:71], a[68:71], a[20:23], v[68:71]\n"
"ds_read_b64_tr_b16 a[84:85], v16 offset:7232\n"
"v_mfma_f32_16x16x32_bf16 v[68:71], a[64:67], a[4:7], v[68:71]\n"
"ds_read_b64_tr_b16 a[86:87], v16 offset:10816\n"
"v_mfma_f32_16x16x32_bf16 v[72:75], a[64:67], a[8:11], v[72:75]\n"
"ds_read_b128 a[36:39], v18 offset:10496\n"
"v_mfma_f32_16x16x32_bf16 v[72:75], a[68:71], a[24:27], v[72:75]\n"
"buffer_store_dwordx4 a[188:191], v30, s[64:67], s10 offen sc1\n"
"v_mfma_f32_16x16x32_bf16 v[76:79], a[68:71], a[28:31], v[76:79]\n"
"ds_read_b64_tr_b16 a[88:89], v16 offset:14400\n"
"v_mfma_f32_16x16x32_bf16 v[76:79], a[64:67], a[12:15], v[76:79]\n"
"ds_read_b64_tr_b16 a[90:91], v16 offset:17984\n"
"s_waitcnt lgkmcnt(8)\n"
"v_mfma_f32_16x16x32_bf16 v[92:95], a[72:75], a[12:15], v[92:95]\n"
"ds_read_b64_tr_b16 a[92:93], v16 offset:21568\n"
"v_mfma_f32_16x16x32_bf16 v[92:95], a[76:79], a[28:31], v[92:95]\n"
"v_mfma_f32_16x16x32_bf16 v[88:91], a[76:79], a[24:27], v[88:91]\n"
"ds_read_b128 a[40:43], v18 offset:12544\n"
"v_mfma_f32_16x16x32_bf16 v[88:91], a[72:75], a[8:11], v[88:91]\n"
"ds_read_b64_tr_b16 a[94:95], v16 offset:25152\n"
"v_mfma_f32_16x16x32_bf16 v[84:87], a[72:75], a[4:7], v[84:87]\n"
"ds_read_b64_tr_b16 a[96:97], v16 offset:96\n"
"v_mfma_f32_16x16x32_bf16 v[84:87], a[76:79], a[20:23], v[84:87]\n"
"v_mfma_f32_16x16x32_bf16 v[80:83], a[76:79], a[16:19], v[80:83]\n"
"ds_read_b128 a[44:47], v18 offset:14592\n"
"v_mfma_f32_16x16x32_bf16 v[80:83], a[72:75], a[0:3], v[80:83]\n"
"ds_read_b64_tr_b16 a[98:99], v16 offset:3680\n"
"s_waitcnt lgkmcnt(9)\n"
"v_mfma_f32_16x16x32_bf16 v[96:99], a[80:83], a[0:3], v[96:99]\n"
"ds_read_b64_tr_b16 a[100:101], v16 offset:7264\n"
"v_mfma_f32_16x16x32_bf16 v[96:99], a[84:87], a[16:19], v[96:99]\n"
"v_mfma_f32_16x16x32_bf16 v[100:103], a[84:87], a[20:23], v[100:103]\n"
"ds_read_b64_tr_b16 a[102:103], v16 offset:10848\n"
"v_mfma_f32_16x16x32_bf16 v[100:103], a[80:83], a[4:7], v[100:103]\n"
"ds_read_b128 a[48:51], v19 offset:8448\n"
"v_mfma_f32_16x16x32_bf16 v[104:107], a[80:83], a[8:11], v[104:107]\n"
"ds_read_b64_tr_b16 a[64:65], v16 offset:14432\n"
"v_mfma_f32_16x16x32_bf16 v[104:107], a[84:87], a[24:27], v[104:107]\n"
"v_mfma_f32_16x16x32_bf16 v[108:111], a[84:87], a[28:31], v[108:111]\n"
"ds_read_b64_tr_b16 a[66:67], v16 offset:18016\n"
"v_mfma_f32_16x16x32_bf16 v[108:111], a[80:83], a[12:15], v[108:111]\n"
"ds_read_b64_tr_b16 a[68:69], v16 offset:21600\n"
"s_waitcnt lgkmcnt(9)\n"
"v_mfma_f32_16x16x32_bf16 v[124:127], a[88:91], a[12:15], v[124:127]\n"
"ds_read_b128 a[52:55], v19 offset:10496\n"
"v_mfma_f32_16x16x32_bf16 v[124:127], a[92:95], a[28:31], v[124:127]\n"
"v_mfma_f32_16x16x32_bf16 v[120:123], a[92:95], a[24:27], v[120:123]\n"
"ds_read_b64_tr_b16 a[70:71], v16 offset:25184\n"
"v_mfma_f32_16x16x32_bf16 v[120:123], a[88:91], a[8:11], v[120:123]\n"
"ds_read_b64_tr_b16 a[72:73], v16 offset:128\n"
"v_mfma_f32_16x16x32_bf16 v[116:119], a[88:91], a[4:7], v[116:119]\n"
"ds_read_b64_tr_b16 a[74:75], v16 offset:3712\n"
"s_lshr_b32 s24, s33, 3\n"
"v_mfma_f32_16x16x32_bf16 v[116:119], a[92:95], a[20:23], v[116:119]\n"
"s_lshl2_add_u32 s24, s24, s34\n"
"v_mfma_f32_16x16x32_bf16 v[112:115], a[92:95], a[16:19], v[112:115]\n"
"ds_read_b128 a[56:59], v19 offset:12544\n"
"v_mfma_f32_16x16x32_bf16 v[112:115], a[88:91], a[0:3], v[112:115]\n"
"ds_read_b64_tr_b16 a[76:77], v16 offset:7296\n"
"s_waitcnt lgkmcnt(10)\n"
"v_mfma_f32_16x16x32_bf16 v[128:131], a[96:99], a[0:3], v[128:131]\n"
"ds_read_b64_tr_b16 a[78:79], v16 offset:10880\n"
"s_lshl_b32 s24, s24, 7\n"
"v_mfma_f32_16x16x32_bf16 v[128:131], a[100:103], a[16:19], v[128:131]\n"
"v_mov_b32 v17, s24\n"
"v_mfma_f32_16x16x32_bf16 v[132:135], a[100:103], a[20:23], v[132:135]\n"
"ds_read_b64_tr_b16 a[80:81], v16 offset:14464\n"
"v_mfma_f32_16x16x32_bf16 v[132:135], a[96:99], a[4:7], v[132:135]\n"
"ds_read_b128 a[60:63], v19 offset:14592\n"
"v_mfma_f32_16x16x32_bf16 v[136:139], a[96:99], a[8:11], v[136:139]\n"
"ds_read_b64_tr_b16 a[82:83], v16 offset:18048\n"
"s_add_u32 m0, s78, 0x2100\n"
"v_mfma_f32_16x16x32_bf16 v[136:139], a[100:103], a[24:27], v[136:139]\n"
"s_getpc_b64 s[24:25]\n"
".Lfk_ct4:\n"
"s_add_u32 s24, s24, 0xbb8\n"
"s_addc_u32 s25, s25, 0\n"
"global_load_dword a196, v17, s[24:25]\n"
"v_mfma_f32_16x16x32_bf16 v[140:143], a[100:103], a[28:31], v[140:143]\n"
"ds_read_b64_tr_b16 a[84:85], v16 offset:21632\n"
"s_add_u32 s80, s96, 6\n"
"v_mfma_f32_16x16x32_bf16 v[140:143], a[96:99], a[12:15], v[140:143]\n"
"ds_read_b64_tr_b16 a[86:87], v16 offset:25216\n"
"s_waitcnt lgkmcnt(10)\n"
"v_mfma_f32_16x16x32_bf16 v[156:159], a[64:67], a[12:15], v[156:159]\n"
"s_waitcnt lgkmcnt(3)\n"
"s_and_b32 s80, s80, 3\n"
"v_mfma_f32_16x16x32_bf16 v[156:159], a[68:71], a[28:31], v[156:159]\n"
"v_mfma_f32_16x16x32_bf16 v[152:155], a[68:71], a[24:27], v[152:155]\n"
"ds_read_b64_tr_b16 a[88:89], v16 offset:160\n"
"s_add_u32 s80, s80, 12\n"
"v_mfma_f32_16x16x32_bf16 v[152:155], a[64:67], a[8:11], v[152:155]\n"
"ds_read_b64_tr_b16 a[90:91], v16 offset:3744\n"
"s_lshl_b32 s80, s80, 7\n"
"v_mfma_f32_16x16x32_bf16 v[148:151], a[64:67], a[4:7], v[148:151]\n"
"buffer_load_dwordx4 v8, s[36:39], s80 offen offset:0 lds\n"
"v_mfma_f32_16x16x32_bf16 v[148:151], a[68:71], a[20:23], v[148:151]\n"
"v_mfma_f32_16x16x32_bf16 v[144:147], a[68:71], a[16:19], v[144:147]\n"
"ds_read_b64_tr_b16 a[92:93], v16 offset:7328\n"
"v_mfma_f32_16x16x32_bf16 v[144:147], a[64:67], a[0:3], v[144:147]\n"
"ds_read_b64_tr_b16 a[94:95], v16 offset:10912\n"
"v_mfma_f32_16x16x32_bf16 v[160:163], a[72:75], a[0:3], v[160:163]\n"
"ds_read_b64_tr_b16 a[96:97], v16 offset:14496\n"
"v_mfma_f32_16x16x32_bf16 v[160:163], a[76:79], a[16:19], v[160:163]\n"
"buffer_load_dwordx4 v9, s[36:39], s80 offen offset:1024 lds\n"
"v_mfma_f32_16x16x32_bf16 v[164:167], a[76:79], a[20:23], v[164:167]\n"
"v_mfma_f32_16x16x32_bf16 v[164:167], a[72:75], a[4:7], v[164:167]\n"
"ds_read_b64_tr_b16 a[98:99], v16 offset:18080\n"
"v_mfma_f32_16x16x32_bf16 v[168:171], a[72:75], a[8:11], v[168:171]\n"
"ds_read_b64_tr_b16 a[100:101], v16 offset:21664\n"
"v_mfma_f32_16x16x32_bf16 v[168:171], a[76:79], a[24:27], v[168:171]\n"
"buffer_load_dwordx4 v10, s[36:39], s80 offen offset:2048 lds\n"
"v_mfma_f32_16x16x32_bf16 v[172:175], a[76:79], a[28:31], v[172:175]\n"
"ds_read_b64_tr_b16 a[102:103], v16 offset:25248\n"
"v_mfma_f32_16x16x32_bf16 v[172:175], a[72:75], a[12:15], v[172:175]\n"
"s_waitcnt lgkmcnt(8)\n"
"v_mfma_f32_16x16x32_bf16 v[188:191], a[80:83], a[12:15], v[188:191]\n"
"ds_read_b64_tr_b16 a[64:65], v16 offset:192\n"
"v_mfma_f32_16x16x32_bf16 v[188:191], a[84:87], a[28:31], v[188:191]\n"
"buffer_load_dwordx4 v11, s[36:39], s80 offen offset:3072 lds\n"
"v_mfma_f32_16x16x32_bf16 v[184:187], a[84:87], a[24:27], v[184:187]\n"
"ds_read_b64_tr_b16 a[66:67], v16 offset:3776\n"
"v_mfma_f32_16x16x32_bf16 v[184:187], a[80:83], a[8:11], v[184:187]\n"
"ds_read_b64_tr_b16 a[68:69], v16 offset:7360\n"
"s_add_u32 m0, s78, 0x3100\n"
"v_mfma_f32_16x16x32_bf16 v[180:183], a[80:83], a[4:7], v[180:183]\n"
"s_or_b32 s80, s80, 0x10000\n"
"v_mfma_f32_16x16x32_bf16 v[180:183], a[84:87], a[20:23], v[180:183]\n"
"buffer_load_dwordx4 v8, s[36:39], s80 offen offset:0 lds\n"
"v_mfma_f32_16x16x32_bf16 v[176:179], a[84:87], a[16:19], v[176:179]\n"
"ds_read_b64_tr_b16 a[70:71], v16 offset:10944\n"
"v_mfma_f32_16x16x32_bf16 v[176:179], a[80:83], a[0:3], v[176:179]\n"
"ds_read_b64_tr_b16 a[72:73], v16 offset:14528\n"
"s_waitcnt lgkmcnt(9)\n"
"v_mfma_f32_16x16x32_bf16 v[192:195], a[88:91], a[0:3], v[192:195]\n"
"ds_read_b64_tr_b16 a[74:75], v16 offset:18112\n"
"v_mfma_f32_16x16x32_bf16 v[192:195], a[92:95], a[16:19], v[192:195]\n"
"buffer_load_dwordx4 v9, s[36:39], s80 offen offset:1024 lds\n"
"v_mfma_f32_16x16x32_bf16 v[196:199], a[92:95], a[20:23], v[196:199]\n"
"v_mfma_f32_16x16x32_bf16 v[196:199], a[88:91], a[4:7], v[196:199]\n"
"ds_read_b64_tr_b16 a[76:77], v16 offset:21696\n"
"v_mfma_f32_16x16x32_bf16 v[200:203], a[88:91], a[8:11], v[200:203]\n"
"ds_read_b64_tr_b16 a[78:79], v16 offset:25280\n"
"v_mfma_f32_16x16x32_bf16 v[200:203], a[92:95], a[24:27], v[200:203]\n"
"buffer_load_dwordx4 v10, s[36:39], s80 offen offset:2048 lds\n"
"v_mfma_f32_16x16x32_bf16 v[204:207], a[92:95], a[28:31], v[204:207]\n"
"v_mfma_f32_16x16x32_bf16 v[204:207], a[88:91], a[12:15], v[204:207]\n"
"s_waitcnt lgkmcnt(8)\n"
"v_mfma_f32_16x16x32_bf16 v[220:223], a[96:99], a[12:15], v[220:223]\n"
"v_mfma_f32_16x16x32_bf16 v[220:223], a[100:103], a[28:31], v[220:223]\n"
"buffer_load_dwordx4 v11, s[36:39], s80 offen offset:3072 lds\n"
"v_mfma_f32_16x16x32_bf16 v[216:219], a[100:103], a[24:27], v[216:219]\n"
"v_mfma_f32_16x16x32_bf16 v[216:219], a[96:99], a[8:11], v[216:219]\n"
"s_add_u32 m0, s77, 0x0\n"
"s_add_u32 s79, s96, 6\n"
"v_mfma_f32_16x16x32_bf16 v[212:215], a[96:99], a[4:7], v[212:215]\n"
"s_and_b32 s79, s79, 3\n"
"s_add_u32 s79, s79, 12\n"
"v_mfma_f32_16x16x32_bf16 v[212:215], a[100:103], a[20:23], v[212:215]\n"
"v_mfma_f32_16x16x32_bf16 v[208:211], a[100:103], a[16:19], v[208:211]\n"
"s_mul_i32 s79, s79, 0x70000\n"
"v_mfma_f32_16x16x32_bf16 v[208:211], a[96:99], a[0:3], v[208:211]\n"
"s_waitcnt vmcnt(28) lgkmcnt(0)\n"
"s_barrier\n"
"v_mfma_f32_16x16x32_bf16 v[224:227], a[64:67], a[0:3], v[224:227]\n"
"ds_read_b64_tr_b16 a[80:81], v16 offset:28672\n"
"v_mfma_f32_16x16x32_bf16 v[224:227], a[68:71], a[16:19], v[224:227]\n"
"buffer_load_dwordx4 v1, s[40:43], s79 offen offset:0 lds\n"
"v_mfma_f32_16x16x32_bf16 v[228:231], a[68:71], a[20:23], v[228:231]\n"
"ds_read_b64_tr_b16 a[82:83], v16 offset:32256\n"
"v_mfma_f32_16x16x32_bf16 v[228:231], a[64:67], a[4:7], v[228:231]\n"
"v_mfma_f32_16x16x32_bf16 v[232:235], a[64:67], a[8:11], v[232:235]\n"
"ds_read_b64_tr_b16 a[84:85], v16 offset:35840\n"
"v_mfma_f32_16x16x32_bf16 v[232:235], a[68:71], a[24:27], v[232:235]\n"
"buffer_load_dwordx4 v2, s[40:43], s79 offen offset:1024 lds\n"
"v_mfma_f32_16x16x32_bf16 v[236:239], a[68:71], a[28:31], v[236:239]\n"
"ds_read_b64_tr_b16 a[86:87], v16 offset:39424\n"
"v_mfma_f32_16x16x32_bf16 v[236:239], a[64:67], a[12:15], v[236:239]\n"
"v_mfma_f32_16x16x32_bf16 v[252:255], a[72:75], a[12:15], v[252:255]\n"
"ds_read_b64_tr_b16 a[88:89], v16 offset:43008\n"
"v_mfma_f32_16x16x32_bf16 v[252:255], a[76:79], a[28:31], v[252:255]\n"
"buffer_load_dwordx4 v3, s[40:43], s79 offen offset:2048 lds\n"
"v_mfma_f32_16x16x32_bf16 v[248:251], a[76:79], a[24:27], v[248:251]\n"
"ds_read_b64_tr_b16 a[90:91], v16 offset:46592\n"
"v_mfma_f32_16x16x32_bf16 v[248:251], a[72:75], a[8:11], v[248:251]\n"
"v_mfma_f32_16x16x32_bf16 v[244:247], a[72:75], a[4:7], v[244:247]\n"
"ds_read_b64_tr_b16 a[92:93], v16 offset:50176\n"
"v_mfma_f32_16x16x32_bf16 v[244:247], a[76:79], a[20:23], v[244:247]\n"
"buffer_load_dwordx4 v4, s[40:43], s79 offen offset:3072 lds\n"
"v_mfma_f32_16x16x32_bf16 v[240:243], a[76:79], a[16:19], v[240:243]\n"
"ds_read_b64_tr_b16 a[94:95], v16 offset:53760\n"
"v_mfma_f32_16x16x32_bf16 v[240:243], a[72:75], a[0:3], v[240:243]\n"
"s_waitcnt lgkmcnt(4)\n"
"v_mfma_f32_16x16x32_bf16 v[32:35], a[80:83], a[32:35], v[32:35]\n"
"ds_read_b64_tr_b16 a[96:97], v16 offset:28704\n"
"s_add_u32 m0, s77, 0x1000\n"
"v_mfma_f32_16x16x32_bf16 v[32:35], a[84:87], a[48:51], v[32:35]\n"
"buffer_load_dwordx4 v5, s[40:43], s79 offen offset:0 lds\n"
"v_mfma_f32_16x16x32_bf16 v[36:39], a[84:87], a[52:55], v[36:39]\n"
"ds_read_b64_tr_b16 a[98:99], v16 offset:32288\n"
"v_lshl_add_u32 v17, v0, 2, s28\n"
"v_mfma_f32_16x16x32_bf16 v[36:39], a[80:83], a[36:39], v[36:39]\n"
"ds_read_b64_tr_b16 a[100:101], v16 offset:35872\n"
"v_mfma_f32_16x16x32_bf16 v[40:43], a[80:83], a[40:43], v[40:43]\n"
"buffer_load_dword a201, v17, s[64:67], 0 offen offset:3072 sc1\n"
"v_mfma_f32_16x16x32_bf16 v[40:43], a[84:87], a[56:59], v[40:43]\n"
"buffer_load_dwordx4 v6, s[40:43], s79 offen offset:1024 lds\n"
"v_mfma_f32_16x16x32_bf16 v[44:47], a[84:87], a[60:63], v[44:47]\n"
"ds_read_b64_tr_b16 a[102:103], v16 offset:39456\n"
"v_mfma_f32_16x16x32_bf16 v[44:47], a[80:83], a[44:47], v[44:47]\n"
"ds_read_b64_tr_b16 a[64:65], v16 offset:43040\n"
"s_waitcnt lgkmcnt(5)\n"
"v_mfma_f32_16x16x32_bf16 v[60:63], a[88:91], a[44:47], v[60:63]\n"
"ds_read_b64_tr_b16 a[66:67], v16 offset:46624\n"
"v_mfma_f32_16x16x32_bf16 v[60:63], a[92:95], a[60:63], v[60:63]\n"
"buffer_load_dwordx4 v7, s[40:43], s79 offen offset:2048 lds\n"
"v_mfma_f32_16x16x32_bf16 v[56:59], a[92:95], a[56:59], v[56:59]\n"
"v_and_b32 v20, 15, v0\n"
"v_lshrrev_b32 v21, 4, v0\n"
"v_mfma_f32_16x16x32_bf16 v[56:59], a[88:91], a[40:43], v[56:59]\n"
"ds_read_b64_tr_b16 a[68:69], v16 offset:50208\n"
"v_add_u32 v22, 1, v20\n"
"v_mfma_f32_16x16x32_bf16 v[52:55], a[88:91], a[36:39], v[52:55]\n"
"ds_read_b64_tr_b16 a[70:71], v16 offset:53792\n"
"s_waitcnt vmcnt(28)\n"
"v_mfma_f32_16x16x32_bf16 v[52:55], a[92:95], a[52:55], v[52:55]\n"
"v_and_b32 v23, 3, v22\n"
"v_mul_u32_u24 v23, 4160, v23\n"
"v_mfma_f32_16x16x32_bf16 v[48:51], a[92:95], a[48:51], v[48:51]\n"
"ds_read_b64_tr_b16 a[72:73], v16 offset:28736\n"
"v_and_b32 v22, 28, v22\n"
"v_mfma_f32_16x16x32_bf16 v[48:51], a[88:91], a[32:35], v[48:51]\n"
"ds_read_b128 a[0:3], v18 offset:0\n"
"s_waitcnt lgkmcnt(6)\n"
"v_mfma_f32_16x16x32_bf16 v[64:67], a[96:99], a[32:35], v[64:67]\n"
"ds_read_b64_tr_b16 a[74:75], v16 offset:32320\n"
"v_lshl_add_u32 v23, v22, 1, v23\n"
"v_mfma_f32_16x16x32_bf16 v[64:67], a[100:103], a[48:51], v[64:67]\n"
"buffer_load_dwordx4 a[204:207], v12, s[52:55], s19 offen offset:0\n"
"v_mfma_f32_16x16x32_bf16 v[68:71], a[100:103], a[52:55], v[68:71]\n"
"ds_read_b64_tr_b16 a[76:77], v16 offset:35904\n"
"v_lshl_add_u32 v23, v21, 4, v23\n"
"v_mfma_f32_16x16x32_bf16 v[68:71], a[96:99], a[36:39], v[68:71]\n"
"ds_read_b64_tr_b16 a[78:79], v16 offset:39488\n"
"v_add_u32 v3, s78, v23\n"
"v_mfma_f32_16x16x32_bf16 v[72:75], a[96:99], a[40:43], v[72:75]\n"
"ds_read_b128 a[4:7], v18 offset:2048\n"
"s_mul_i32 s84, s34, 5120\n"
"v_mfma_f32_16x16x32_bf16 v[72:75], a[100:103], a[56:59], v[72:75]\n"
"buffer_load_dwordx2 a[208:209], v12, s[52:55], s19 offen offset:16\n"
"v_mfma_f32_16x16x32_bf16 v[76:79], a[100:103], a[60:63], v[76:79]\n"
"ds_read_b64_tr_b16 a[80:81], v16 offset:43072\n"
"s_add_u32 s84, s84, 33248\n"
"v_mfma_f32_16x16x32_bf16 v[76:79], a[96:99], a[44:47], v[76:79]\n"
"ds_read_b64_tr_b16 a[82:83], v16 offset:46656\n"
"s_waitcnt lgkmcnt(8)\n"
"v_mfma_f32_16x16x32_bf16 v[92:95], a[64:67], a[44:47], v[92:95]\n"
"ds_read_b64_tr_b16 a[84:85], v16 offset:50240\n"
"v_lshlrev_b32 v22, 5, v20\n"
"v_mfma_f32_16x16x32_bf16 v[92:95], a[68:71], a[60:63], v[92:95]\n"
"buffer_load_dwordx4 a[210:213], v12, s[52:55], s19 offen offset:1024\n"
"v_mfma_f32_16x16x32_bf16 v[88:91], a[68:71], a[56:59], v[88:91]\n"
"ds_read_b128 a[8:11], v18 offset:4096\n"
"v_sub_u32 v22, s84, v22\n"
"v_mfma_f32_16x16x32_bf16 v[88:91], a[64:67], a[40:43], v[88:91]\n"
"ds_read_b64_tr_b16 a[86:87], v16 offset:53824\n"
"v_lshl_add_u32 v28, v21, 4, v22\n"
"v_mfma_f32_16x16x32_bf16 v[84:87], a[64:67], a[36:39], v[84:87]\n"
"ds_read_b64_tr_b16 a[88:89], v16 offset:28768\n"
"v_lshlrev_b32 v21, 3, v21\n"
"v_mfma_f32_16x16x32_bf16 v[84:87], a[68:71], a[52:55], v[84:87]\n"
"buffer_load_dwordx2 a[214:215], v12, s[52:55], s19 offen offset:1040\n"
"v_mfma_f32_16x16x32_bf16 v[80:83], a[68:71], a[48:51], v[80:83]\n"
"ds_read_b128 a[12:15], v18 offset:6144\n"
"v_add_u32 v21, v21, v20\n"
"v_mfma_f32_16x16x32_bf16 v[80:83], a[64:67], a[32:35], v[80:83]\n"
"ds_read_b64_tr_b16 a[90:91], v16 offset:32352\n"
"s_waitcnt lgkmcnt(9)\n"
"v_mfma_f32_16x16x32_bf16 v[96:99], a[72:75], a[32:35], v[96:99]\n"
"ds_read_b64_tr_b16 a[92:93], v16 offset:35936\n"
"v_sub_u32 v21, 15, v21\n"
"v_mfma_f32_16x16x32_bf16 v[96:99], a[76:79], a[48:51], v[96:99]\n"
"buffer_load_dwordx4 a[216:219], v12, s[52:55], s19 offen offset:2048\n"
"v_mfma_f32_16x16x32_bf16 v[100:103], a[76:79], a[52:55], v[100:103]\n"
"ds_read_b64_tr_b16 a[94:95], v16 offset:39520\n"
"v_and_b32 v22, 1, v21\n"
"v_mfma_f32_16x16x32_bf16 v[100:103], a[72:75], a[36:39], v[100:103]\n"
"ds_read_b128 a[16:19], v19 offset:0\n"
"v_lshlrev_b32 v22, 4, v22\n"
"v_mfma_f32_16x16x32_bf16 v[104:107], a[72:75], a[40:43], v[104:107]\n"
"ds_read_b64_tr_b16 a[96:97], v16 offset:43104\n"
"v_lshlrev_b32 v22, v22, s93\n"
"v_mfma_f32_16x16x32_bf16 v[104:107], a[76:79], a[56:59], v[104:107]\n"
"buffer_load_dwordx2 a[220:221], v12, s[52:55], s19 offen offset:2064\n"
"v_mfma_f32_16x16x32_bf16 v[108:111], a[76:79], a[60:63], v[108:111]\n"
"ds_read_b64_tr_b16 a[98:99], v16 offset:46688\n"
"v_ashrrev_i32 v21, 1, v21\n"
"v_mfma_f32_16x16x32_bf16 v[108:111], a[72:75], a[44:47], v[108:111]\n"
"ds_read_b64_tr_b16 a[100:101], v16 offset:50272\n"
"s_waitcnt lgkmcnt(9)\n"
"v_mfma_f32_16x16x32_bf16 v[124:127], a[80:83], a[44:47], v[124:127]\n"
"ds_read_b128 a[20:23], v19 offset:2048\n"
"v_cmp_eq_u32 vcc, 0, v21\n"
"v_mfma_f32_16x16x32_bf16 v[124:127], a[84:87], a[60:63], v[124:127]\n"
"buffer_load_dwordx4 a[222:225], v12, s[52:55], s19 offen offset:3072\n"
"v_mfma_f32_16x16x32_bf16 v[120:123], a[84:87], a[56:59], v[120:123]\n"
"ds_read_b64_tr_b16 a[102:103], v16 offset:53856\n"
"s_nop 0\n"
"v_mfma_f32_16x16x32_bf16 v[120:123], a[80:83], a[40:43], v[120:123]\n"
"ds_read_b64_tr_b16 a[64:65], v16 offset:28800\n"
"v_cndmask_b32 v8, 0, v22, vcc\n"
"v_mfma_f32_16x16x32_bf16 v[116:119], a[80:83], a[36:39], v[116:119]\n"
"ds_read_b64_tr_b16 a[66:67], v16 offset:32384\n"
"v_cmp_eq_u32 vcc, 1, v21\n"
"v_mfma_f32_16x16x32_bf16 v[116:119], a[84:87], a[52:55], v[116:119]\n"
"buffer_load_dwordx2 a[226:227], v12, s[52:55], s19 offen offset:3088\n"
"v_mfma_f32_16x16x32_bf16 v[112:115], a[84:87], a[48:51], v[112:115]\n"
"ds_read_b128 a[24:27], v19 offset:4096\n"
"s_nop 0\n"
"v_mfma_f32_16x16x32_bf16 v[112:115], a[80:83], a[32:35], v[112:115]\n"
"ds_read_b64_tr_b16 a[68:69], v16 offset:35968\n"
"s_waitcnt lgkmcnt(10)\n"
"v_mfma_f32_16x16x32_bf16 v[128:131], a[88:91], a[32:35], v[128:131]\n"
"ds_read_b64_tr_b16 a[70:71], v16 offset:39552\n"
"v_cndmask_b32 v9, 0, v22, vcc\n"
"v_mfma_f32_16x16x32_bf16 v[128:131], a[92:95], a[48:51], v[128:131]\n"
"v_cmp_eq_u32 vcc, 2, v21\n"
"s_nop 0\n"
"v_mfma_f32_16x16x32_bf16 v[132:135], a[92:95], a[52:55], v[132:135]\n"
"ds_read_b64_tr_b16 a[72:73], v16 offset:43136\n"
"v_cndmask_b32 v10, 0, v22, vcc\n"
"v_mfma_f32_16x16x32_bf16 v[132:135], a[88:91], a[36:39], v[132:135]\n"
"ds_read_b128 a[28:31], v19 offset:6144\n"
"v_cmp_eq_u32 vcc, 3, v21\n"
"v_mfma_f32_16x16x32_bf16 v[136:139], a[88:91], a[40:43], v[136:139]\n"
"ds_read_b64_tr_b16 a[74:75], v16 offset:46720\n"
"s_nop 0\n"
"v_mfma_f32_16x16x32_bf16 v[136:139], a[92:95], a[56:59], v[136:139]\n"
"v_cndmask_b32 v11, 0, v22, vcc\n"
"s_lshr_b32 s21, s34, 1\n"
"v_mfma_f32_16x16x32_bf16 v[140:143], a[92:95], a[60:63], v[140:143]\n"
"ds_read_b64_tr_b16 a[76:77], v16 offset:50304\n"
"s_and_b32 s22, s34, 1\n"
"v_mfma_f32_16x16x32_bf16 v[140:143], a[88:91], a[44:47], v[140:143]\n"
"ds_read_b64_tr_b16 a[78:79], v16 offset:53888\n"
"s_waitcnt lgkmcnt(10)\n"
"v_mfma_f32_16x16x32_bf16 v[156:159], a[96:99], a[44:47], v[156:159]\n"
"s_waitcnt lgkmcnt(3)\n"
"s_lshl1_add_u32 s84, s20, s21\n"
"v_mfma_f32_16x16x32_bf16 v[156:159], a[100:103], a[60:63], v[156:159]\n"
"s_mul_i32 s84, s84, 896\n"
"s_lshl4_add_u32 s84, s22, s84\n"
"v_mfma_f32_16x16x32_bf16 v[152:155], a[100:103], a[56:59], v[152:155]\n"
"ds_read_b64_tr_b16 a[80:81], v16 offset:28832\n"
"s_lshl_b32 s84, s84, 11\n"
"v_mfma_f32_16x16x32_bf16 v[152:155], a[96:99], a[40:43], v[152:155]\n"
"ds_read_b64_tr_b16 a[82:83], v16 offset:32416\n"
"s_lshr_b32 s85, s75, 1\n"
"v_mfma_f32_16x16x32_bf16 v[148:151], a[96:99], a[36:39], v[148:151]\n"
"s_lshl_b32 s85, s85, 8\n"
"s_add_u32 s84, s84, s85\n"
"v_mfma_f32_16x16x32_bf16 v[148:151], a[100:103], a[52:55], v[148:151]\n"
"v_lshrrev_b32 v20, 4, v0\n"
"v_bfe_u32 v21, v0, 1, 3\n"
"v_mfma_f32_16x16x32_bf16 v[144:147], a[100:103], a[48:51], v[144:147]\n"
"ds_read_b64_tr_b16 a[84:85], v16 offset:36000\n"
"v_xor_b32 v21, v21, v20\n"
"v_mfma_f32_16x16x32_bf16 v[144:147], a[96:99], a[32:35], v[144:147]\n"
"ds_read_b64_tr_b16 a[86:87], v16 offset:39584\n"
"v_mfma_f32_16x16x32_bf16 v[160:163], a[64:67], a[32:35], v[160:163]\n"
"ds_read_b64_tr_b16 a[88:89], v16 offset:43168\n"
"v_lshlrev_b32 v20, 11, v20\n"
"v_mfma_f32_16x16x32_bf16 v[160:163], a[68:71], a[48:51], v[160:163]\n"
"v_add_u32 v20, s84, v20\n"
"v_and_b32 v22, 1, v0\n"
"v_mfma_f32_16x16x32_bf16 v[164:167], a[68:71], a[52:55], v[164:167]\n"
"v_lshl_add_u32 v20, v22, 4, v20\n"
"v_xor_b32 v22, 4, v21\n"
"v_mfma_f32_16x16x32_bf16 v[164:167], a[64:67], a[36:39], v[164:167]\n"
"ds_read_b64_tr_b16 a[90:91], v16 offset:46752\n"
"v_lshl_add_u32 v21, v21, 5, v20\n"
"v_mfma_f32_16x16x32_bf16 v[168:171], a[64:67], a[40:43], v[168:171]\n"
"ds_read_b64_tr_b16 a[92:93], v16 offset:50336\n"
"v_lshl_add_u32 v22, v22, 5, v20\n"
"v_mfma_f32_16x16x32_bf16 v[168:171], a[68:71], a[56:59], v[168:171]\n"
"v_add_u32 v4, 0x0, v21\n"
"v_add_u32 v5, 0x1c00, v22\n"
"v_mfma_f32_16x16x32_bf16 v[172:175], a[68:71], a[60:63], v[172:175]\n"
"ds_read_b64_tr_b16 a[94:95], v16 offset:53920\n"
"v_add_u32 v6, 0x3800, v21\n"
"v_mfma_f32_16x16x32_bf16 v[172:175], a[64:67], a[44:47], v[172:175]\n"
"v_add_u32 v7, 0x5400, v22\n"
"s_waitcnt lgkmcnt(8)\n"
"v_mfma_f32_16x16x32_bf16 v[188:191], a[72:75], a[44:47], v[188:191]\n"
"ds_read_b64_tr_b16 a[96:97], v16 offset:28864\n"
"s_mul_i32 s77, s21, 53248\n"
"v_mfma_f32_16x16x32_bf16 v[188:191], a[76:79], a[60:63], v[188:191]\n"
"s_lshl_b32 s84, s22, 12\n"
"s_add_u32 s77, s77, s84\n"
"v_mfma_f32_16x16x32_bf16 v[184:187], a[76:79], a[56:59], v[184:187]\n"
"ds_read_b64_tr_b16 a[98:99], v16 offset:32448\n"
"s_add_u32 s17, s22, 1\n"
"v_mfma_f32_16x16x32_bf16 v[184:187], a[72:75], a[40:43], v[184:187]\n"
"ds_read_b64_tr_b16 a[100:101], v16 offset:36032\n"
"s_mul_i32 s17, s17, 196608\n"
"v_mfma_f32_16x16x32_bf16 v[180:183], a[72:75], a[36:39], v[180:183]\n"
"s_bfe_u32 s81, s75, 0x20001\n"
"s_and_b32 s87, s81, 1\n"
"v_mfma_f32_16x16x32_bf16 v[180:183], a[76:79], a[52:55], v[180:183]\n"
"s_lshl_b32 s81, s81, 3\n"
"v_mfma_f32_16x16x32_bf16 v[176:179], a[76:79], a[48:51], v[176:179]\n"
"ds_read_b64_tr_b16 a[102:103], v16 offset:39616\n"
"v_mfma_f32_16x16x32_bf16 v[176:179], a[72:75], a[32:35], v[176:179]\n"
"ds_read_b64_tr_b16 a[64:65], v16 offset:43200\n"
"s_waitcnt lgkmcnt(9)\n"
"v_mfma_f32_16x16x32_bf16 v[192:195], a[80:83], a[32:35], v[192:195]\n"
"ds_read_b64_tr_b16 a[66:67], v16 offset:46784\n"
"v_mfma_f32_16x16x32_bf16 v[192:195], a[84:87], a[48:51], v[192:195]\n"
"s_mov_b32 s88, 0xe090400\n"
"s_mov_b32 s89, s88\n"
"s_lshr_b64 s[88:89], s[88:89], s81\n"
"s_mov_b32 s8, s88\n"
"v_mfma_f32_16x16x32_bf16 v[196:199], a[84:87], a[52:55], v[196:199]\n"
"s_mov_b32 s88, 0x201c1712\n"
"s_mov_b32 s89, s88\n"
"s_lshr_b64 s[88:89], s[88:89], s81\n"
"s_mov_b32 s9, s88\n"
"v_mfma_f32_16x16x32_bf16 v[196:199], a[80:83], a[36:39], v[196:199]\n"
"ds_read_b64_tr_b16 a[68:69], v16 offset:50368\n"
"v_mfma_f32_16x16x32_bf16 v[200:203], a[80:83], a[40:43], v[200:203]\n"
"ds_read_b64_tr_b16 a[70:71], v16 offset:53952\n"
"s_lshl2_add_u32 s25, s33, s34\n"
"v_mfma_f32_16x16x32_bf16 v[200:203], a[84:87], a[56:59], v[200:203]\n"
"s_lshr_b32 s24, s25, 8\n"
"v_mfma_f32_16x16x32_bf16 v[204:207], a[84:87], a[60:63], v[204:207]\n"
"s_and_b32 s25, s25, 255\n"
"v_mfma_f32_16x16x32_bf16 v[204:207], a[80:83], a[44:47], v[204:207]\n"
"s_mul_i32 s24, s24, 14336\n"
"s_waitcnt lgkmcnt(8)\n"
"v_mfma_f32_16x16x32_bf16 v[220:223], a[88:91], a[44:47], v[220:223]\n"
"v_add_u32 v16, 0xe000, v16\n"
"v_mfma_f32_16x16x32_bf16 v[220:223], a[92:95], a[60:63], v[220:223]\n"
"s_mul_i32 s25, s25, 56\n"
"v_mfma_f32_16x16x32_bf16 v[216:219], a[92:95], a[56:59], v[216:219]\n"
"s_add_u32 s24, s24, s25\n"
"v_mfma_f32_16x16x32_bf16 v[216:219], a[88:91], a[40:43], v[216:219]\n"
".Lfk_hop4022:\n"
"s_cbranch_execz .Lfk_hopret\n"
"s_add_u32 s24, s24, 0\n"
"v_mfma_f32_16x16x32_bf16 v[212:215], a[88:91], a[36:39], v[212:215]\n"
"s_lshl_b32 s24, s24, 7\n"
"v_mfma_f32_16x16x32_bf16 v[212:215], a[92:95], a[52:55], v[212:215]\n"
"s_mov_b32 s88, 0x332e2a25\n"
"s_mov_b32 s89, s88\n"
"s_lshr_b64 s[88:89], s[88:89], s81\n"
"s_mov_b32 s10, s88\n"
"v_mfma_f32_16x16x32_bf16 v[208:211], a[92:95], a[48:51], v[208:211]\n"
"v_min_u32 v17, 55, v0\n"
"v_mfma_f32_16x16x32_bf16 v[208:211], a[88:91], a[32:35], v[208:211]\n"
"s_waitcnt vmcnt(25) lgkmcnt(0)\n"
"s_barrier\n"
"s_cmp_lg_u32 s34, 0\n"
"s_cbranch_scc1 .Lfk_sig5\n"
"s_lshl_b32 s85, s11, 8\n"
"s_add_u32 s85, s85, 8192\n"
"v_mov_b32 v30, s85\n"
"v_mov_b32 v31, 1\n"
"s_mov_b64 exec, 1\n"
"global_atomic_add v30, v31, s[30:31]\n"
"s_mov_b64 exec, -1\n"
".Lfk_sig5:\n"
"v_mfma_f32_16x16x32_bf16 v[224:227], a[96:99], a[32:35], v[224:227]\n"
"ds_read_b64_tr_b16 a[72:73], v16 offset:0\n"
"v_lshlrev_b32 v17, 7, v17\n"
"v_mfma_f32_16x16x32_bf16 v[224:227], a[100:103], a[48:51], v[224:227]\n"
"buffer_load_dword a196, v17, s[48:51], s24 offen\n"
"v_mfma_f32_16x16x32_bf16 v[228:231], a[100:103], a[52:55], v[228:231]\n"
"ds_read_b64_tr_b16 a[74:75], v16 offset:3584\n"
"v_mfma_f32_16x16x32_bf16 v[228:231], a[96:99], a[36:39], v[228:231]\n"
"s_mov_b32 s88, 0x100b0702\n"
"s_mov_b32 s89, s88\n"
"s_lshr_b64 s[88:89], s[88:89], s81\n"
"s_mov_b32 s14, s88\n"
"v_mfma_f32_16x16x32_bf16 v[232:235], a[96:99], a[40:43], v[232:235]\n"
"ds_read_b64_tr_b16 a[76:77], v16 offset:7168\n"
"v_mfma_f32_16x16x32_bf16 v[232:235], a[100:103], a[56:59], v[232:235]\n"
"s_mov_b32 s88, 0x231e1915\n"
"s_mov_b32 s89, s88\n"
"s_lshr_b64 s[88:89], s[88:89], s81\n"
"s_mov_b32 s15, s88\n"
"v_mfma_f32_16x16x32_bf16 v[236:239], a[100:103], a[60:63], v[236:239]\n"
"ds_read_b64_tr_b16 a[78:79], v16 offset:10752\n"
"v_mfma_f32_16x16x32_bf16 v[236:239], a[96:99], a[44:47], v[236:239]\n"
"v_mfma_f32_16x16x32_bf16 v[252:255], a[64:67], a[44:47], v[252:255]\n"
"ds_read_b64_tr_b16 a[80:81], v16 offset:14336\n"
"v_mfma_f32_16x16x32_bf16 v[252:255], a[68:71], a[60:63], v[252:255]\n"
"s_mov_b32 s88, 0x35312c27\n"
"s_mov_b32 s89, s88\n"
"s_lshr_b64 s[88:89], s[88:89], s81\n"
"s_mov_b32 s27, s88\n"
"v_mfma_f32_16x16x32_bf16 v[248:251], a[68:71], a[56:59], v[248:251]\n"
"ds_read_b64_tr_b16 a[82:83], v16 offset:17920\n"
"s_lshr_b32 s96, s74, 1\n"
"v_mfma_f32_16x16x32_bf16 v[248:251], a[64:67], a[40:43], v[248:251]\n"
"s_lshl1_add_u32 s96, s96, s20\n"
"s_lshl_b32 s96, s96, 10\n"
"v_mfma_f32_16x16x32_bf16 v[244:247], a[64:67], a[36:39], v[244:247]\n"
"ds_read_b64_tr_b16 a[84:85], v16 offset:21504\n"
"s_add_u32 s96, s96, 32768\n"
"v_mfma_f32_16x16x32_bf16 v[244:247], a[68:71], a[52:55], v[244:247]\n"
"s_lshr_b32 s81, s75, 1\n"
"s_lshl_b32 s81, s81, 7\n"
"v_mfma_f32_16x16x32_bf16 v[240:243], a[68:71], a[48:51], v[240:243]\n"
"ds_read_b64_tr_b16 a[86:87], v16 offset:25088\n"
"s_add_u32 s96, s96, s81\n"
"v_mfma_f32_16x16x32_bf16 v[240:243], a[64:67], a[32:35], v[240:243]\n"
"s_lshl_b32 s97, s75, 8\n"
"s_add_u32 s97, s97, 4096\n"
"s_waitcnt lgkmcnt(4)\n"
"v_mfma_f32_16x16x32_bf16 v[32:35], a[72:75], a[0:3], v[32:35]\n"
"ds_read_b64_tr_b16 a[88:89], v16 offset:32\n"
"s_lshl_b32 s81, s76, 12\n"
"v_mfma_f32_16x16x32_bf16 v[32:35], a[76:79], a[16:19], v[32:35]\n"
"s_lshl_b32 s82, s34, 12\n"
"s_add_u32 s81, s81, s82\n"
"v_mfma_f32_16x16x32_bf16 v[36:39], a[76:79], a[20:23], v[36:39]\n"
"ds_read_b64_tr_b16 a[90:91], v16 offset:3616\n"
"s_add_u32 s16, s81, 0x100000\n"
"v_mfma_f32_16x16x32_bf16 v[36:39], a[72:75], a[4:7], v[36:39]\n"
"ds_read_b64_tr_b16 a[92:93], v16 offset:7200\n"
"s_lshr_b32 s26, s73, 4\n"
"v_mfma_f32_16x16x32_bf16 v[40:43], a[72:75], a[8:11], v[40:43]\n"
"s_lshl1_add_u32 s26, s72, s26\n"
"v_lshlrev_b32 v1, 4, v0\n"
"v_mfma_f32_16x16x32_bf16 v[40:43], a[76:79], a[24:27], v[40:43]\n"
"v_lshlrev_b32 v2, 3, v0\n"
"v_mfma_f32_16x16x32_bf16 v[44:47], a[76:79], a[28:31], v[44:47]\n"
"ds_read_b64_tr_b16 a[94:95], v16 offset:10784\n"
"v_mfma_f32_16x16x32_bf16 v[44:47], a[72:75], a[12:15], v[44:47]\n"
"ds_read_b64_tr_b16 a[96:97], v16 offset:14368\n"
"s_waitcnt lgkmcnt(5)\n"
"v_mfma_f32_16x16x32_bf16 v[60:63], a[80:83], a[12:15], v[60:63]\n"
"ds_read_b64_tr_b16 a[98:99], v16 offset:17952\n"
"v_mfma_f32_16x16x32_bf16 v[60:63], a[84:87], a[28:31], v[60:63]\n"
"v_mfma_f32_16x16x32_bf16 v[56:59], a[84:87], a[24:27], v[56:59]\n"
"v_mfma_f32_16x16x32_bf16 v[56:59], a[80:83], a[8:11], v[56:59]\n"
"ds_read_b64_tr_b16 a[100:101], v16 offset:21536\n"
"v_mfma_f32_16x16x32_bf16 v[52:55], a[80:83], a[4:7], v[52:55]\n"
"ds_read_b64_tr_b16 a[102:103], v16 offset:25120\n"
"s_waitcnt vmcnt(17)\n"
"v_mfma_f32_16x16x32_bf16 v[52:55], a[84:87], a[20:23], v[52:55]\n"
"v_mfma_f32_16x16x32_bf16 v[48:51], a[84:87], a[16:19], v[48:51]\n"
"ds_read_b64_tr_b16 a[64:65], v16 offset:64\n"
"v_mfma_f32_16x16x32_bf16 v[48:51], a[80:83], a[0:3], v[48:51]\n"
"ds_read_b128 a[32:35], v18 offset:8448\n"
"s_waitcnt lgkmcnt(6)\n"
"s_cmp_eq_u32 s33, 0\n"
"s_cselect_b32 s13, 1, 0\n"
"s_waitcnt vmcnt(11)\n"
"v_accvgpr_read_b32 v30, a201\n"
"v_cmp_ne_u32 vcc, s13, v30\n"
"s_nop 4\n"
"s_cbranch_vccz .Lfk_hd6\n"
"s_mov_b32 s35, 0x10000\n"
".Lfk_hs6:\n"
"v_lshl_add_u32 v17, v0, 2, s28\n"
"buffer_load_dword v30, v17, s[64:67], 0 offen offset:1024 sc1\n"
"s_waitcnt vmcnt(0)\n"
"buffer_store_dword v30, v17, s[64:67], 0 offen offset:2048 sc1\n"
"s_waitcnt vmcnt(0)\n"
"v_lshl_add_u32 v17, v0, 2, s28\n"
"buffer_load_dword v30, v17, s[64:67], 0 offen offset:3072 sc1\n"
"s_waitcnt vmcnt(0)\n"
"v_cmp_ne_u32 vcc, s13, v30\n"
"s_nop 4\n"
"s_cbranch_vccz .Lfk_hd6\n"
"s_sub_u32 s35, s35, 1\n"
"s_cmp_lg_u32 s35, 0\n"
"s_cbranch_scc1 .Lfk_hs6\n"
".Lfk_hd6:\n"
"v_mfma_f32_16x16x32_bf16 v[64:67], a[88:91], a[0:3], v[64:67]\n"
"ds_read_b64_tr_b16 a[66:67], v16 offset:3648\n"
"v_mfma_f32_16x16x32_bf16 v[64:67], a[92:95], a[16:19], v[64:67]\n"
"v_mfma_f32_16x16x32_bf16 v[68:71], a[92:95], a[20:23], v[68:71]\n"
"ds_read_b64_tr_b16 a[68:69], v16 offset:7232\n"
"v_mfma_f32_16x16x32_bf16 v[68:71], a[88:91], a[4:7], v[68:71]\n"
"ds_read_b64_tr_b16 a[70:71], v16 offset:10816\n"
"v_mfma_f32_16x16x32_bf16 v[72:75], a[88:91], a[8:11], v[72:75]\n"
"ds_read_b128 a[36:39], v18 offset:10496\n"
"v_mfma_f32_16x16x32_bf16 v[72:75], a[92:95], a[24:27], v[72:75]\n"
"v_mfma_f32_16x16x32_bf16 v[76:79], a[92:95], a[28:31], v[76:79]\n"
"ds_read_b64_tr_b16 a[72:73], v16 offset:14400\n"
"v_mfma_f32_16x16x32_bf16 v[76:79], a[88:91], a[12:15], v[76:79]\n"
"ds_read_b64_tr_b16 a[74:75], v16 offset:17984\n"
"s_waitcnt lgkmcnt(8)\n"
"v_mfma_f32_16x16x32_bf16 v[92:95], a[96:99], a[12:15], v[92:95]\n"
"ds_read_b64_tr_b16 a[76:77], v16 offset:21568\n"
"v_mfma_f32_16x16x32_bf16 v[92:95], a[100:103], a[28:31], v[92:95]\n"
"v_mfma_f32_16x16x32_bf16 v[88:91], a[100:103], a[24:27], v[88:91]\n"
"ds_read_b128 a[40:43], v18 offset:12544\n"
"v_mfma_f32_16x16x32_bf16 v[88:91], a[96:99], a[8:11], v[88:91]\n"
"ds_read_b64_tr_b16 a[78:79], v16 offset:25152\n"
"v_mfma_f32_16x16x32_bf16 v[84:87], a[96:99], a[4:7], v[84:87]\n"
"ds_read_b64_tr_b16 a[80:81], v16 offset:96\n"
"v_mfma_f32_16x16x32_bf16 v[84:87], a[100:103], a[20:23], v[84:87]\n"
"v_mfma_f32_16x16x32_bf16 v[80:83], a[100:103], a[16:19], v[80:83]\n"
"ds_read_b128 a[44:47], v18 offset:14592\n"
"v_mfma_f32_16x16x32_bf16 v[80:83], a[96:99], a[0:3], v[80:83]\n"
"ds_read_b64_tr_b16 a[82:83], v16 offset:3680\n"
"s_waitcnt lgkmcnt(9)\n"
"v_mfma_f32_16x16x32_bf16 v[96:99], a[64:67], a[0:3], v[96:99]\n"
"ds_read_b64_tr_b16 a[84:85], v16 offset:7264\n"
"v_mfma_f32_16x16x32_bf16 v[96:99], a[68:71], a[16:19], v[96:99]\n"
"v_mfma_f32_16x16x32_bf16 v[100:103], a[68:71], a[20:23], v[100:103]\n"
"ds_read_b64_tr_b16 a[86:87], v16 offset:10848\n"
"v_mfma_f32_16x16x32_bf16 v[100:103], a[64:67], a[4:7], v[100:103]\n"
"ds_read_b128 a[48:51], v19 offset:8448\n"
"v_mfma_f32_16x16x32_bf16 v[104:107], a[64:67], a[8:11], v[104:107]\n"
"ds_read_b64_tr_b16 a[88:89], v16 offset:14432\n"
"v_mfma_f32_16x16x32_bf16 v[104:107], a[68:71], a[24:27], v[104:107]\n"
"v_mfma_f32_16x16x32_bf16 v[108:111], a[68:71], a[28:31], v[108:111]\n"
"ds_read_b64_tr_b16 a[90:91], v16 offset:18016\n"
"v_mfma_f32_16x16x32_bf16 v[108:111], a[64:67], a[12:15], v[108:111]\n"
"ds_read_b64_tr_b16 a[92:93], v16 offset:21600\n"
"s_waitcnt lgkmcnt(9)\n"
"v_mfma_f32_16x16x32_bf16 v[124:127], a[72:75], a[12:15], v[124:127]\n"
"ds_read_b128 a[52:55], v19 offset:10496\n"
"v_mfma_f32_16x16x32_bf16 v[124:127], a[76:79], a[28:31], v[124:127]\n"
"v_mfma_f32_16x16x32_bf16 v[120:123], a[76:79], a[24:27], v[120:123]\n"
"ds_read_b64_tr_b16 a[94:95], v16 offset:25184\n"
"v_mfma_f32_16x16x32_bf16 v[120:123], a[72:75], a[8:11], v[120:123]\n"
"ds_read_b64_tr_b16 a[96:97], v16 offset:128\n"
"v_mfma_f32_16x16x32_bf16 v[116:119], a[72:75], a[4:7], v[116:119]\n"
"ds_read_b64_tr_b16 a[98:99], v16 offset:3712\n"
"v_mfma_f32_16x16x32_bf16 v[116:119], a[76:79], a[20:23], v[116:119]\n"
"v_mfma_f32_16x16x32_bf16 v[112:115], a[76:79], a[16:19], v[112:115]\n"
"ds_read_b128 a[56:59], v19 offset:12544\n"
"v_mfma_f32_16x16x32_bf16 v[112:115], a[72:75], a[0:3], v[112:115]\n"
"ds_read_b64_tr_b16 a[100:101], v16 offset:7296\n"
"s_waitcnt lgkmcnt(10)\n"
"v_mfma_f32_16x16x32_bf16 v[128:131], a[80:83], a[0:3], v[128:131]\n"
"ds_read_b64_tr_b16 a[102:103], v16 offset:10880\n"
"v_mfma_f32_16x16x32_bf16 v[128:131], a[84:87], a[16:19], v[128:131]\n"
"v_mfma_f32_16x16x32_bf16 v[132:135], a[84:87], a[20:23], v[132:135]\n"
"ds_read_b64_tr_b16 a[64:65], v16 offset:14464\n"
"v_mfma_f32_16x16x32_bf16 v[132:135], a[80:83], a[4:7], v[132:135]\n"
"ds_read_b128 a[60:63], v19 offset:14592\n"
"v_mfma_f32_16x16x32_bf16 v[136:139], a[80:83], a[8:11], v[136:139]\n"
"ds_read_b64_tr_b16 a[66:67], v16 offset:18048\n"
"v_mfma_f32_16x16x32_bf16 v[136:139], a[84:87], a[24:27], v[136:139]\n"
"v_mfma_f32_16x16x32_bf16 v[140:143], a[84:87], a[28:31], v[140:143]\n"
"ds_read_b64_tr_b16 a[68:69], v16 offset:21632\n"
"v_mfma_f32_16x16x32_bf16 v[140:143], a[80:83], a[12:15], v[140:143]\n"
"ds_read_b64_tr_b16 a[70:71], v16 offset:25216\n"
"s_waitcnt lgkmcnt(10)\n"
"v_mfma_f32_16x16x32_bf16 v[156:159], a[88:91], a[12:15], v[156:159]\n"
"s_waitcnt lgkmcnt(3)\n"
"v_mfma_f32_16x16x32_bf16 v[156:159], a[92:95], a[28:31], v[156:159]\n"
"s_add_u32 s81, s78, 32\n"
"v_add_u32 v13, s81, v12\n"
"v_mfma_f32_16x16x32_bf16 v[152:155], a[92:95], a[24:27], v[152:155]\n"
"ds_read_b64_tr_b16 a[72:73], v16 offset:160\n"
"s_waitcnt vmcnt(1)\n"
"v_mfma_f32_16x16x32_bf16 v[152:155], a[88:91], a[8:11], v[152:155]\n"
"ds_read_b64_tr_b16 a[74:75], v16 offset:3744\n"
"v_mfma_f32_16x16x32_bf16 v[148:151], a[88:91], a[4:7], v[148:151]\n"
"ds_write_b64 v13, a[204:205] offset:0\n"
"v_mfma_f32_16x16x32_bf16 v[148:151], a[92:95], a[20:23], v[148:151]\n"
"ds_write_b64 v13, a[206:207] offset:8\n"
"v_accvgpr_read_b32 v14, a204\n"
"v_mfma_f32_16x16x32_bf16 v[144:147], a[92:95], a[16:19], v[144:147]\n"
"ds_read_b64_tr_b16 a[76:77], v16 offset:7328\n"
"v_accvgpr_read_b32 v20, a205\n"
"v_mfma_f32_16x16x32_bf16 v[144:147], a[88:91], a[0:3], v[144:147]\n"
"ds_read_b64_tr_b16 a[78:79], v16 offset:10912\n"
"v_mfma_f32_16x16x32_bf16 v[160:163], a[96:99], a[0:3], v[160:163]\n"
"ds_read_b64_tr_b16 a[80:81], v16 offset:14496\n"
"v_accvgpr_read_b32 v21, a206\n"
"v_mfma_f32_16x16x32_bf16 v[160:163], a[100:103], a[16:19], v[160:163]\n"
"v_accvgpr_read_b32 v22, a207\n"
"v_accvgpr_read_b32 v23, a208\n"
"v_mfma_f32_16x16x32_bf16 v[164:167], a[100:103], a[20:23], v[164:167]\n"
"v_accvgpr_read_b32 v15, a209\n"
"v_mfma_f32_16x16x32_bf16 v[164:167], a[96:99], a[4:7], v[164:167]\n"
"ds_read_b64_tr_b16 a[82:83], v16 offset:18080\n"
"v_mfma_f32_16x16x32_bf16 v[168:171], a[96:99], a[8:11], v[168:171]\n"
"ds_read_b64_tr_b16 a[84:85], v16 offset:21664\n"
"v_mfma_f32_16x16x32_bf16 v[168:171], a[100:103], a[24:27], v[168:171]\n"
"ds_write_b64 v13, v[20:21] offset:8320\n"
"v_mfma_f32_16x16x32_bf16 v[172:175], a[100:103], a[28:31], v[172:175]\n"
"ds_read_b64_tr_b16 a[86:87], v16 offset:25248\n"
"v_mfma_f32_16x16x32_bf16 v[172:175], a[96:99], a[12:15], v[172:175]\n"
"ds_write_b64 v13, v[22:23] offset:8328\n"
"s_waitcnt lgkmcnt(12)\n"
"v_mfma_f32_16x16x32_bf16 v[188:191], a[64:67], a[12:15], v[188:191]\n"
"ds_read_b64_tr_b16 a[88:89], v16 offset:192\n"
"v_alignbit_b32 v24, v20, v14, 16\n"
"v_mfma_f32_16x16x32_bf16 v[188:191], a[68:71], a[28:31], v[188:191]\n"
"v_alignbit_b32 v25, v21, v20, 16\n"
"v_alignbit_b32 v26, v22, v21, 16\n"
"v_mfma_f32_16x16x32_bf16 v[184:187], a[68:71], a[24:27], v[184:187]\n"
"ds_read_b64_tr_b16 a[90:91], v16 offset:3776\n"
"v_alignbit_b32 v27, v23, v22, 16\n"
"v_mfma_f32_16x16x32_bf16 v[184:187], a[64:67], a[8:11], v[184:187]\n"
"ds_read_b64_tr_b16 a[92:93], v16 offset:7360\n"
"v_mfma_f32_16x16x32_bf16 v[180:183], a[64:67], a[4:7], v[180:183]\n"
"ds_write_b64 v13, v[24:25] offset:4160\n"
"v_mfma_f32_16x16x32_bf16 v[180:183], a[68:71], a[20:23], v[180:183]\n"
"ds_write_b64 v13, v[26:27] offset:4168\n"
"v_alignbit_b32 v24, v21, v20, 16\n"
"v_mfma_f32_16x16x32_bf16 v[176:179], a[68:71], a[16:19], v[176:179]\n"
"ds_read_b64_tr_b16 a[94:95], v16 offset:10944\n"
"v_alignbit_b32 v25, v22, v21, 16\n"
"v_mfma_f32_16x16x32_bf16 v[176:179], a[64:67], a[0:3], v[176:179]\n"
"ds_read_b64_tr_b16 a[96:97], v16 offset:14528\n"
"s_waitcnt lgkmcnt(13)\n"
"v_mfma_f32_16x16x32_bf16 v[192:195], a[72:75], a[0:3], v[192:195]\n"
"ds_read_b64_tr_b16 a[98:99], v16 offset:18112\n"
"v_alignbit_b32 v26, v23, v22, 16\n"
"v_mfma_f32_16x16x32_bf16 v[192:195], a[76:79], a[16:19], v[192:195]\n"
"v_alignbit_b32 v27, v15, v23, 16\n"
"v_mfma_f32_16x16x32_bf16 v[196:199], a[76:79], a[20:23], v[196:199]\n"
"ds_write_b64 v13, v[24:25] offset:12480\n"
"v_mfma_f32_16x16x32_bf16 v[196:199], a[72:75], a[4:7], v[196:199]\n"
"ds_read_b64_tr_b16 a[100:101], v16 offset:21696\n"
"v_mfma_f32_16x16x32_bf16 v[200:203], a[72:75], a[8:11], v[200:203]\n"
"ds_read_b64_tr_b16 a[102:103], v16 offset:25280\n"
"v_mfma_f32_16x16x32_bf16 v[200:203], a[76:79], a[24:27], v[200:203]\n"
"ds_write_b64 v13, v[26:27] offset:12488\n"
"v_mfma_f32_16x16x32_bf16 v[204:207], a[76:79], a[28:31], v[204:207]\n"
"ds_write_b64 v13, a[210:211] offset:1024\n"
"v_mfma_f32_16x16x32_bf16 v[204:207], a[72:75], a[12:15], v[204:207]\n"
"ds_write_b64 v13, a[212:213] offset:1032\n"
"s_waitcnt lgkmcnt(15)\n"
"v_mfma_f32_16x16x32_bf16 v[220:223], a[80:83], a[12:15], v[220:223]\n"
"v_subrev_u32 v16, 0xe000, v16\n"
"v_accvgpr_read_b32 v14, a210\n"
"v_mfma_f32_16x16x32_bf16 v[220:223], a[84:87], a[28:31], v[220:223]\n"
"v_accvgpr_read_b32 v20, a211\n"
"v_accvgpr_read_b32 v21, a212\n"
"v_mfma_f32_16x16x32_bf16 v[216:219], a[84:87], a[24:27], v[216:219]\n"
"v_accvgpr_read_b32 v22, a213\n"
"v_accvgpr_read_b32 v23, a214\n"
"v_mfma_f32_16x16x32_bf16 v[216:219], a[80:83], a[8:11], v[216:219]\n"
"v_accvgpr_read_b32 v15, a215\n"
"v_mfma_f32_16x16x32_bf16 v[212:215], a[80:83], a[4:7], v[212:215]\n"
"ds_write_b64 v13, v[20:21] offset:9344\n"
"v_mfma_f32_16x16x32_bf16 v[212:215], a[84:87], a[20:23], v[212:215]\n"
"ds_write_b64 v13, v[22:23] offset:9352\n"
"v_alignbit_b32 v24, v20, v14, 16\n"
"v_mfma_f32_16x16x32_bf16 v[208:211], a[84:87], a[16:19], v[208:211]\n"
"v_alignbit_b32 v25, v21, v20, 16\n"
"v_alignbit_b32 v26, v22, v21, 16\n"
"v_mfma_f32_16x16x32_bf16 v[208:211], a[80:83], a[0:3], v[208:211]\n"
"s_waitcnt lgkmcnt(0)\n"
"s_barrier\n"
"v_mfma_f32_16x16x32_bf16 v[224:227], a[88:91], a[0:3], v[224:227]\n"
"ds_read_b64_tr_b16 a[64:65], v16 offset:0\n"
"v_alignbit_b32 v27, v23, v22, 16\n"
"v_mfma_f32_16x16x32_bf16 v[224:227], a[92:95], a[16:19], v[224:227]\n"
"ds_write_b64 v13, v[24:25] offset:5184\n"
"v_mfma_f32_16x16x32_bf16 v[228:231], a[92:95], a[20:23], v[228:231]\n"
"ds_read_b64_tr_b16 a[66:67], v16 offset:3584\n"
"v_mfma_f32_16x16x32_bf16 v[228:231], a[88:91], a[4:7], v[228:231]\n"
"ds_write_b64 v13, v[26:27] offset:5192\n"
"v_alignbit_b32 v24, v21, v20, 16\n"
"v_mfma_f32_16x16x32_bf16 v[232:235], a[88:91], a[8:11], v[232:235]\n"
"ds_read_b64_tr_b16 a[68:69], v16 offset:7168\n"
"v_alignbit_b32 v25, v22, v21, 16\n"
"v_mfma_f32_16x16x32_bf16 v[232:235], a[92:95], a[24:27], v[232:235]\n"
"v_alignbit_b32 v26, v23, v22, 16\n"
"v_alignbit_b32 v27, v15, v23, 16\n"
"v_mfma_f32_16x16x32_bf16 v[236:239], a[92:95], a[28:31], v[236:239]\n"
"ds_read_b64_tr_b16 a[70:71], v16 offset:10752\n"
"v_mfma_f32_16x16x32_bf16 v[236:239], a[88:91], a[12:15], v[236:239]\n"
"ds_write_b64 v13, v[24:25] offset:13504\n"
"v_mfma_f32_16x16x32_bf16 v[252:255], a[96:99], a[12:15], v[252:255]\n"
"ds_read_b64_tr_b16 a[72:73], v16 offset:14336\n"
"v_mfma_f32_16x16x32_bf16 v[252:255], a[100:103], a[28:31], v[252:255]\n"
"ds_write_b64 v13, v[26:27] offset:13512\n"
"v_mfma_f32_16x16x32_bf16 v[248:251], a[100:103], a[24:27], v[248:251]\n"
"ds_read_b64_tr_b16 a[74:75], v16 offset:17920\n"
"v_mfma_f32_16x16x32_bf16 v[248:251], a[96:99], a[8:11], v[248:251]\n"
"ds_write_b64 v13, a[216:217] offset:2048\n"
"v_mfma_f32_16x16x32_bf16 v[244:247], a[96:99], a[4:7], v[244:247]\n"
"ds_read_b64_tr_b16 a[76:77], v16 offset:21504\n"
"v_mfma_f32_16x16x32_bf16 v[244:247], a[100:103], a[20:23], v[244:247]\n"
"ds_write_b64 v13, a[218:219] offset:2056\n"
"v_accvgpr_read_b32 v14, a216\n"
"v_mfma_f32_16x16x32_bf16 v[240:243], a[100:103], a[16:19], v[240:243]\n"
"ds_read_b64_tr_b16 a[78:79], v16 offset:25088\n"
"v_accvgpr_read_b32 v20, a217\n"
"v_mfma_f32_16x16x32_bf16 v[240:243], a[96:99], a[0:3], v[240:243]\n"
"v_accvgpr_read_b32 v21, a218\n"
"v_accvgpr_read_b32 v22, a219\n"
"s_waitcnt lgkmcnt(8)\n"
"v_mfma_f32_16x16x32_bf16 v[32:35], a[64:67], a[32:35], v[32:35]\n"
"ds_read_b64_tr_b16 a[80:81], v16 offset:32\n"
"v_accvgpr_read_b32 v23, a220\n"
"v_mfma_f32_16x16x32_bf16 v[32:35], a[68:71], a[48:51], v[32:35]\n"
"v_accvgpr_read_b32 v15, a221\n"
"v_mfma_f32_16x16x32_bf16 v[36:39], a[68:71], a[52:55], v[36:39]\n"
"ds_read_b64_tr_b16 a[82:83], v16 offset:3616\n"
"v_mfma_f32_16x16x32_bf16 v[36:39], a[64:67], a[36:39], v[36:39]\n"
"ds_read_b64_tr_b16 a[84:85], v16 offset:7200\n"
"v_mfma_f32_16x16x32_bf16 v[40:43], a[64:67], a[40:43], v[40:43]\n"
"ds_write_b64 v13, v[20:21] offset:10368\n"
"v_mfma_f32_16x16x32_bf16 v[40:43], a[68:71], a[56:59], v[40:43]\n"
"ds_write_b64 v13, v[22:23] offset:10376\n"
"v_alignbit_b32 v24, v20, v14, 16\n"
"v_mfma_f32_16x16x32_bf16 v[44:47], a[68:71], a[60:63], v[44:47]\n"
"ds_read_b64_tr_b16 a[86:87], v16 offset:10784\n"
"v_alignbit_b32 v25, v21, v20, 16\n"
"v_mfma_f32_16x16x32_bf16 v[44:47], a[64:67], a[44:47], v[44:47]\n"
"ds_read_b64_tr_b16 a[88:89], v16 offset:14368\n"
"s_waitcnt lgkmcnt(7)\n"
"v_mfma_f32_16x16x32_bf16 v[60:63], a[72:75], a[44:47], v[60:63]\n"
"ds_read_b64_tr_b16 a[90:91], v16 offset:17952\n"
"v_alignbit_b32 v26, v22, v21, 16\n"
"v_mfma_f32_16x16x32_bf16 v[60:63], a[76:79], a[60:63], v[60:63]\n"
"v_alignbit_b32 v27, v23, v22, 16\n"
"v_mfma_f32_16x16x32_bf16 v[56:59], a[76:79], a[56:59], v[56:59]\n"
"ds_write_b64 v13, v[24:25] offset:6208\n"
"v_mfma_f32_16x16x32_bf16 v[56:59], a[72:75], a[40:43], v[56:59]\n"
"ds_read_b64_tr_b16 a[92:93], v16 offset:21536\n"
"v_mfma_f32_16x16x32_bf16 v[52:55], a[72:75], a[36:39], v[52:55]\n"
"ds_read_b64_tr_b16 a[94:95], v16 offset:25120\n"
"v_mfma_f32_16x16x32_bf16 v[52:55], a[76:79], a[52:55], v[52:55]\n"
"ds_write_b64 v13, v[26:27] offset:6216\n"
"v_alignbit_b32 v24, v21, v20, 16\n"
"v_mfma_f32_16x16x32_bf16 v[48:51], a[76:79], a[48:51], v[48:51]\n"
"ds_read_b64_tr_b16 a[96:97], v16 offset:64\n"
"v_alignbit_b32 v25, v22, v21, 16\n"
"v_mfma_f32_16x16x32_bf16 v[48:51], a[72:75], a[32:35], v[48:51]\n"
"v_alignbit_b32 v26, v23, v22, 16\n"
"s_waitcnt lgkmcnt(7)\n"
"v_mfma_f32_16x16x32_bf16 v[64:67], a[80:83], a[32:35], v[64:67]\n"
"ds_read_b64_tr_b16 a[98:99], v16 offset:3648\n"
"v_alignbit_b32 v27, v15, v23, 16\n"
"v_mfma_f32_16x16x32_bf16 v[64:67], a[84:87], a[48:51], v[64:67]\n"
"ds_write_b64 v13, v[24:25] offset:14528\n"
"v_mfma_f32_16x16x32_bf16 v[68:71], a[84:87], a[52:55], v[68:71]\n"
"ds_read_b64_tr_b16 a[100:101], v16 offset:7232\n"
"v_mfma_f32_16x16x32_bf16 v[68:71], a[80:83], a[36:39], v[68:71]\n"
"ds_read_b64_tr_b16 a[102:103], v16 offset:10816\n"
"v_mfma_f32_16x16x32_bf16 v[72:75], a[80:83], a[40:43], v[72:75]\n"
"ds_write_b64 v13, v[26:27] offset:14536\n"
"v_mfma_f32_16x16x32_bf16 v[72:75], a[84:87], a[56:59], v[72:75]\n"
"ds_write_b64 v13, a[222:223] offset:3072\n"
"v_mfma_f32_16x16x32_bf16 v[76:79], a[84:87], a[60:63], v[76:79]\n"
"ds_read_b64_tr_b16 a[64:65], v16 offset:14400\n"
"v_mfma_f32_16x16x32_bf16 v[76:79], a[80:83], a[44:47], v[76:79]\n"
"ds_read_b64_tr_b16 a[66:67], v16 offset:17984\n"
"s_waitcnt lgkmcnt(10)\n"
"v_mfma_f32_16x16x32_bf16 v[92:95], a[88:91], a[44:47], v[92:95]\n"
"ds_read_b64_tr_b16 a[68:69], v16 offset:21568\n"
"v_mfma_f32_16x16x32_bf16 v[92:95], a[92:95], a[60:63], v[92:95]\n"
"ds_write_b64 v13, a[224:225] offset:3080\n"
"v_accvgpr_read_b32 v14, a222\n"
"v_mfma_f32_16x16x32_bf16 v[88:91], a[92:95], a[56:59], v[88:91]\n"
"v_accvgpr_read_b32 v20, a223\n"
"v_accvgpr_read_b32 v21, a224\n"
"v_mfma_f32_16x16x32_bf16 v[88:91], a[88:91], a[40:43], v[88:91]\n"
"ds_read_b64_tr_b16 a[70:71], v16 offset:25152\n"
"v_accvgpr_read_b32 v22, a225\n"
"v_mfma_f32_16x16x32_bf16 v[84:87], a[88:91], a[36:39], v[84:87]\n"
"ds_read_b64_tr_b16 a[72:73], v16 offset:96\n"
"v_accvgpr_read_b32 v23, a226\n"
"v_mfma_f32_16x16x32_bf16 v[84:87], a[92:95], a[52:55], v[84:87]\n"
"v_accvgpr_read_b32 v15, a227\n"
"v_mfma_f32_16x16x32_bf16 v[80:83], a[92:95], a[48:51], v[80:83]\n"
"ds_write_b64 v13, v[20:21] offset:11392\n"
"v_mfma_f32_16x16x32_bf16 v[80:83], a[88:91], a[32:35], v[80:83]\n"
"ds_read_b64_tr_b16 a[74:75], v16 offset:3680\n"
"s_waitcnt lgkmcnt(10)\n"
"v_mfma_f32_16x16x32_bf16 v[96:99], a[96:99], a[32:35], v[96:99]\n"
"ds_read_b64_tr_b16 a[76:77], v16 offset:7264\n"
"v_mfma_f32_16x16x32_bf16 v[96:99], a[100:103], a[48:51], v[96:99]\n"
"ds_write_b64 v13, v[22:23] offset:11400\n"
"v_alignbit_b32 v24, v20, v14, 16\n"
"v_mfma_f32_16x16x32_bf16 v[100:103], a[100:103], a[52:55], v[100:103]\n"
"ds_read_b64_tr_b16 a[78:79], v16 offset:10848\n"
"v_alignbit_b32 v25, v21, v20, 16\n"
"v_mfma_f32_16x16x32_bf16 v[100:103], a[96:99], a[36:39], v[100:103]\n"
"v_alignbit_b32 v26, v22, v21, 16\n"
"v_alignbit_b32 v27, v23, v22, 16\n"
"v_mfma_f32_16x16x32_bf16 v[104:107], a[96:99], a[40:43], v[104:107]\n"
"ds_read_b64_tr_b16 a[80:81], v16 offset:14432\n"
"v_mfma_f32_16x16x32_bf16 v[104:107], a[100:103], a[56:59], v[104:107]\n"
"ds_write_b64 v13, v[24:25] offset:7232\n"
"v_mfma_f32_16x16x32_bf16 v[108:111], a[100:103], a[60:63], v[108:111]\n"
"ds_read_b64_tr_b16 a[82:83], v16 offset:18016\n"
"v_mfma_f32_16x16x32_bf16 v[108:111], a[96:99], a[44:47], v[108:111]\n"
"ds_read_b64_tr_b16 a[84:85], v16 offset:21600\n"
"s_waitcnt lgkmcnt(10)\n"
"v_mfma_f32_16x16x32_bf16 v[124:127], a[64:67], a[44:47], v[124:127]\n"
"ds_write_b64 v13, v[26:27] offset:7240\n"
"v_alignbit_b32 v24, v21, v20, 16\n"
"v_mfma_f32_16x16x32_bf16 v[124:127], a[68:71], a[60:63], v[124:127]\n"
"v_alignbit_b32 v25, v22, v21, 16\n"
"v_alignbit_b32 v26, v23, v22, 16\n"
"v_mfma_f32_16x16x32_bf16 v[120:123], a[68:71], a[56:59], v[120:123]\n"
"ds_read_b64_tr_b16 a[86:87], v16 offset:25184\n"
"v_alignbit_b32 v27, v15, v23, 16\n"
"v_mfma_f32_16x16x32_bf16 v[120:123], a[64:67], a[40:43], v[120:123]\n"
"ds_read_b64_tr_b16 a[88:89], v16 offset:128\n"
"v_mfma_f32_16x16x32_bf16 v[116:119], a[64:67], a[36:39], v[116:119]\n"
"ds_read_b64_tr_b16 a[90:91], v16 offset:3712\n"
"v_mfma_f32_16x16x32_bf16 v[116:119], a[68:71], a[52:55], v[116:119]\n"
"ds_write_b64 v13, v[24:25] offset:15552\n"
"v_mfma_f32_16x16x32_bf16 v[112:115], a[68:71], a[48:51], v[112:115]\n"
"ds_write_b64 v13, v[26:27] offset:15560\n"
"s_lshl_b32 s81, s94, 16\n"
"v_mfma_f32_16x16x32_bf16 v[112:115], a[64:67], a[32:35], v[112:115]\n"
"ds_read_b64_tr_b16 a[92:93], v16 offset:7296\n"
"s_waitcnt lgkmcnt(11)\n"
"v_mfma_f32_16x16x32_bf16 v[128:131], a[72:75], a[32:35], v[128:131]\n"
"ds_read_b64_tr_b16 a[94:95], v16 offset:10880\n"
"s_lshr_b32 s82, s94, 16\n"
"v_mfma_f32_16x16x32_bf16 v[128:131], a[76:79], a[48:51], v[128:131]\n"
"s_lshl_b32 s83, s95, 16\n"
"s_or_b32 s82, s82, s83\n"
"v_mfma_f32_16x16x32_bf16 v[132:135], a[76:79], a[52:55], v[132:135]\n"
"ds_read_b64_tr_b16 a[96:97], v16 offset:14464\n"
"v_mov_b32 v20, 0\n"
"v_mfma_f32_16x16x32_bf16 v[132:135], a[72:75], a[36:39], v[132:135]\n"
"v_mov_b32 v21, 0\n"
"v_mov_b32 v22, 0\n"
"v_mfma_f32_16x16x32_bf16 v[136:139], a[72:75], a[40:43], v[136:139]\n"
"ds_read_b64_tr_b16 a[98:99], v16 offset:18048\n"
"v_mov_b32 v23, 0\n"
"v_mfma_f32_16x16x32_bf16 v[136:139], a[76:79], a[56:59], v[136:139]\n"
"v_writelane_b32 v23, s81, 3\n"
"v_writelane_b32 v23, s94, 5\n"
"v_mfma_f32_16x16x32_bf16 v[140:143], a[76:79], a[60:63], v[140:143]\n"
"ds_read_b64_tr_b16 a[100:101], v16 offset:21632\n"
"v_writelane_b32 v23, s82, 7\n"
"v_mfma_f32_16x16x32_bf16 v[140:143], a[72:75], a[44:47], v[140:143]\n"
"ds_read_b64_tr_b16 a[102:103], v16 offset:25216\n"
"s_waitcnt lgkmcnt(10)\n"
"v_mfma_f32_16x16x32_bf16 v[156:159], a[80:83], a[44:47], v[156:159]\n"
"v_writelane_b32 v22, s81, 7\n"
"v_lshrrev_b32 v14, 1, v0\n"
"v_mfma_f32_16x16x32_bf16 v[156:159], a[84:87], a[60:63], v[156:159]\n"
"v_mul_u32_u24 v14, 4160, v14\n"
"v_and_b32 v15, 1, v0\n"
"v_mfma_f32_16x16x32_bf16 v[152:155], a[84:87], a[56:59], v[152:155]\n"
"ds_read_b64_tr_b16 a[64:65], v16 offset:160\n"
"v_lshl_add_u32 v14, v15, 4, v14\n"
"v_mfma_f32_16x16x32_bf16 v[152:155], a[80:83], a[40:43], v[152:155]\n"
"ds_read_b64_tr_b16 a[66:67], v16 offset:3744\n"
"v_add_u32 v14, s78, v14\n"
"v_mfma_f32_16x16x32_bf16 v[148:151], a[80:83], a[36:39], v[148:151]\n"
"s_mov_b64 exec, 0xff\n"
"ds_write_b128 v14, v[20:23]\n"
"s_mov_b64 exec, -1\n"
"v_mfma_f32_16x16x32_bf16 v[148:151], a[84:87], a[52:55], v[148:151]\n"
"s_lshl1_add_u32 s81, s20, s21\n"
"s_mul_i32 s81, s81, 3670016\n"
"v_mfma_f32_16x16x32_bf16 v[144:147], a[84:87], a[48:51], v[144:147]\n"
"ds_read_b64_tr_b16 a[68:69], v16 offset:7328\n"
"s_lshl1_add_u32 s82, s74, s22\n"
"v_mfma_f32_16x16x32_bf16 v[144:147], a[80:83], a[32:35], v[144:147]\n"
"ds_read_b64_tr_b16 a[70:71], v16 offset:10912\n"
"s_waitcnt lgkmcnt(9)\n"
"v_mfma_f32_16x16x32_bf16 v[160:163], a[88:91], a[32:35], v[160:163]\n"
"ds_read_b64_tr_b16 a[72:73], v16 offset:14496\n"
"s_lshl_b32 s82, s82, 12\n"
"v_mfma_f32_16x16x32_bf16 v[160:163], a[92:95], a[48:51], v[160:163]\n"
"s_add_u32 s81, s81, s82\n"
"s_xor_b32 s82, s21, 1\n"
"v_mfma_f32_16x16x32_bf16 v[164:167], a[92:95], a[52:55], v[164:167]\n"
"s_lshl_b32 s82, s82, 11\n"
"s_add_u32 s28, s82, s81\n"
"v_mfma_f32_16x16x32_bf16 v[164:167], a[88:91], a[36:39], v[164:167]\n"
"ds_read_b64_tr_b16 a[74:75], v16 offset:18080\n"
"s_xor_b32 s82, s21, 0\n"
"v_mfma_f32_16x16x32_bf16 v[168:171], a[88:91], a[40:43], v[168:171]\n"
"ds_read_b64_tr_b16 a[76:77], v16 offset:21664\n"
"s_lshl_b32 s82, s82, 11\n"
"v_mfma_f32_16x16x32_bf16 v[168:171], a[92:95], a[56:59], v[168:171]\n"
"s_add_u32 s29, s82, s81\n"
"s_lshr_b32 s98, s76, 6\n"
"v_mfma_f32_16x16x32_bf16 v[172:175], a[92:95], a[60:63], v[172:175]\n"
"ds_read_b64_tr_b16 a[78:79], v16 offset:25248\n"
"s_lshl_b32 s98, s98, 8\n"
"v_mfma_f32_16x16x32_bf16 v[172:175], a[88:91], a[44:47], v[172:175]\n"
"s_add_u32 s98, s98, 8192\n"
"s_waitcnt lgkmcnt(9)\n"
"v_mfma_f32_16x16x32_bf16 v[188:191], a[96:99], a[44:47], v[188:191]\n"
"ds_read_b64_tr_b16 a[80:81], v16 offset:192\n"
"v_mfma_f32_16x16x32_bf16 v[188:191], a[100:103], a[60:63], v[188:191]\n"
"buffer_load_dword v30, off, s[64:67], s98 sc1\n"
"v_mfma_f32_16x16x32_bf16 v[184:187], a[100:103], a[56:59], v[184:187]\n"
"ds_read_b64_tr_b16 a[82:83], v16 offset:3776\n"
"v_mfma_f32_16x16x32_bf16 v[184:187], a[96:99], a[40:43], v[184:187]\n"
"ds_read_b64_tr_b16 a[84:85], v16 offset:7360\n"
"v_mfma_f32_16x16x32_bf16 v[180:183], a[96:99], a[36:39], v[180:183]\n"
"v_mfma_f32_16x16x32_bf16 v[180:183], a[100:103], a[52:55], v[180:183]\n"
"v_mfma_f32_16x16x32_bf16 v[176:179], a[100:103], a[48:51], v[176:179]\n"
"ds_read_b64_tr_b16 a[86:87], v16 offset:10944\n"
"v_mfma_f32_16x16x32_bf16 v[176:179], a[96:99], a[32:35], v[176:179]\n"
"ds_read_b64_tr_b16 a[88:89], v16 offset:14528\n"
"s_waitcnt lgkmcnt(9)\n"
"v_mfma_f32_16x16x32_bf16 v[192:195], a[64:67], a[32:35], v[192:195]\n"
"ds_read_b64_tr_b16 a[90:91], v16 offset:18112\n"
"v_mfma_f32_16x16x32_bf16 v[192:195], a[68:71], a[48:51], v[192:195]\n"
"v_mfma_f32_16x16x32_bf16 v[196:199], a[68:71], a[52:55], v[196:199]\n"
"v_mfma_f32_16x16x32_bf16 v[196:199], a[64:67], a[36:39], v[196:199]\n"
"ds_read_b64_tr_b16 a[92:93], v16 offset:21696\n"
"v_mfma_f32_16x16x32_bf16 v[200:203], a[64:67], a[40:43], v[200:203]\n"
"ds_read_b64_tr_b16 a[94:95], v16 offset:25280\n"
"v_mfma_f32_16x16x32_bf16 v[200:203], a[68:71], a[56:59], v[200:203]\n"
"v_mfma_f32_16x16x32_bf16 v[204:207], a[68:71], a[60:63], v[204:207]\n"
"v_mfma_f32_16x16x32_bf16 v[204:207], a[64:67], a[44:47], v[204:207]\n"
"s_waitcnt lgkmcnt(8)\n"
".Lfk_hopw2:\n"
".Lfk_hop4590:\n"
"s_cbranch_execz .Lfk_hop5236\n"
"v_mfma_f32_16x16x32_bf16 v[220:223], a[72:75], a[44:47], v[220:223]\n"
"v_mfma_f32_16x16x32_bf16 v[220:223], a[76:79], a[60:63], v[220:223]\n"
"v_mfma_f32_16x16x32_bf16 v[216:219], a[76:79], a[56:59], v[216:219]\n"
"v_mfma_f32_16x16x32_bf16 v[216:219], a[72:75], a[40:43], v[216:219]\n"
"v_mfma_f32_16x16x32_bf16 v[212:215], a[72:75], a[36:39], v[212:215]\n"
"v_mfma_f32_16x16x32_bf16 v[212:215], a[76:79], a[52:55], v[212:215]\n"
"v_mfma_f32_16x16x32_bf16 v[208:211], a[76:79], a[48:51], v[208:211]\n"
"v_mfma_f32_16x16x32_bf16 v[208:211], a[72:75], a[32:35], v[208:211]\n"
"s_waitcnt lgkmcnt(4)\n"
"v_mfma_f32_16x16x32_bf16 v[224:227], a[80:83], a[32:35], v[224:227]\n"
"v_mfma_f32_16x16x32_bf16 v[224:227], a[84:87], a[48:51], v[224:227]\n"
"v_mfma_f32_16x16x32_bf16 v[228:231], a[84:87], a[52:55], v[228:231]\n"
"v_mfma_f32_16x16x32_bf16 v[228:231], a[80:83], a[36:39], v[228:231]\n"
"v_mfma_f32_16x16x32_bf16 v[232:235], a[80:83], a[40:43], v[232:235]\n"
"v_mfma_f32_16x16x32_bf16 v[232:235], a[84:87], a[56:59], v[232:235]\n"
"v_mfma_f32_16x16x32_bf16 v[236:239], a[84:87], a[60:63], v[236:239]\n"
"v_mfma_f32_16x16x32_bf16 v[236:239], a[80:83], a[44:47], v[236:239]\n"
"s_waitcnt lgkmcnt(0)\n"
"v_mfma_f32_16x16x32_bf16 v[252:255], a[88:91], a[44:47], v[252:255]\n"
"v_mfma_f32_16x16x32_bf16 v[252:255], a[92:95], a[60:63], v[252:255]\n"
"v_mfma_f32_16x16x32_bf16 v[248:251], a[92:95], a[56:59], v[248:251]\n"
"v_mfma_f32_16x16x32_bf16 v[248:251], a[88:91], a[40:43], v[248:251]\n"
"v_mfma_f32_16x16x32_bf16 v[244:247], a[88:91], a[36:39], v[244:247]\n"
"v_mfma_f32_16x16x32_bf16 v[244:247], a[92:95], a[52:55], v[244:247]\n"
"v_mfma_f32_16x16x32_bf16 v[240:243], a[92:95], a[48:51], v[240:243]\n"
"v_mfma_f32_16x16x32_bf16 v[240:243], a[88:91], a[32:35], v[240:243]\n"
"s_waitcnt lgkmcnt(0)\n"
"s_lshr_b32 s81, s73, 1\n"
"s_mul_i32 s81, s81, 7\n"
"s_and_b32 s82, s73, 1\n"
"s_lshl2_add_u32 s83, s72, s34\n"
"s_lshl_b32 s83, s83, 12\n"
"s_add_u32 s84, s81, 6\n"
"s_lshl_b32 s84, s84, 17\n"
"s_add_u32 s84, s84, s83\n"
"s_lshl_b32 s85, s82, 9\n"
"s_add_u32 s84, s84, s85\n"
"s_mul_i32 s82, s82, 3\n"
"s_add_u32 s81, s81, s82\n"
"s_lshl_b32 s81, s81, 17\n"
"s_add_u32 s81, s81, s83\n"
"s_mov_b32 s86, 0xbfb8aa3b\n"
"v_pk_mul_f32 v[20:21], v[32:33], s[86:87] op_sel_hi:[1,0]\n"
"v_pk_mul_f32 v[24:25], v[64:65], s[86:87] op_sel_hi:[1,0]\n"
"v_pk_mul_f32 v[22:23], v[34:35], s[86:87] op_sel_hi:[1,0]\n"
"v_pk_mul_f32 v[26:27], v[66:67], s[86:87] op_sel_hi:[1,0]\n"
"v_exp_f32 v20, v20\n"
"v_exp_f32 v24, v24\n"
"v_exp_f32 v21, v21\n"
"v_exp_f32 v25, v25\n"
"v_exp_f32 v22, v22\n"
"v_exp_f32 v26, v26\n"
"v_exp_f32 v23, v23\n"
"v_exp_f32 v27, v27\n"
"v_pk_mul_f32 v[48:49], v[32:33], v[48:49]\n"
"v_pk_mul_f32 v[80:81], v[64:65], v[80:81]\n"
"v_pk_add_f32 v[20:21], v[20:21], 1.0 op_sel_hi:[1,0]\n"
"v_pk_add_f32 v[24:25], v[24:25], 1.0 op_sel_hi:[1,0]\n"
"v_pk_add_f32 v[22:23], v[22:23], 1.0 op_sel_hi:[1,0]\n"
"v_pk_add_f32 v[26:27], v[26:27], 1.0 op_sel_hi:[1,0]\n"
"v_pk_mul_f32 v[50:51], v[34:35], v[50:51]\n"
"v_pk_mul_f32 v[82:83], v[66:67], v[82:83]\n"
"v_rcp_f32 v20, v20\n"
"v_rcp_f32 v24, v24\n"
"v_rcp_f32 v21, v21\n"
"v_rcp_f32 v25, v25\n"
"v_rcp_f32 v22, v22\n"
"v_rcp_f32 v26, v26\n"
"v_rcp_f32 v23, v23\n"
"v_rcp_f32 v27, v27\n"
"v_pk_mul_f32 v[48:49], v[48:49], v[20:21]\n"
"v_pk_mul_f32 v[80:81], v[80:81], v[24:25]\n"
"v_pk_mul_f32 v[50:51], v[50:51], v[22:23]\n"
"v_pk_mul_f32 v[82:83], v[82:83], v[26:27]\n"
"v_cvt_pk_bf16_f32 v32, v48, v49\n"
"v_cvt_pk_bf16_f32 v34, v80, v81\n"
"v_cvt_pk_bf16_f32 v33, v50, v51\n"
"v_cvt_pk_bf16_f32 v35, v82, v83\n"
"buffer_store_dwordx4 v[32:35], v1, s[44:47], s81 offen offset:0 sc1\n"
"s_barrier\n"
"s_waitcnt vmcnt(1)\n"
"v_readfirstlane_b32 s85, v30\n"
"s_nop 3\n"
"s_cmp_ge_u32 s85, 16\n"
"s_cbranch_scc1 .Lfk_dn7\n"
"s_mov_b32 s35, 0x10000\n"
".Lfk_sp7:\n"
"s_sleep 1\n"
"buffer_load_dword v30, off, s[64:67], s98 sc1\n"
"s_waitcnt vmcnt(0)\n"
"v_readfirstlane_b32 s85, v30\n"
"s_nop 3\n"
"s_cmp_ge_u32 s85, 16\n"
"s_cbranch_scc1 .Lfk_dn7\n"
"s_sub_u32 s35, s35, 1\n"
"s_cmp_lg_u32 s35, 0\n"
"s_cbranch_scc1 .Lfk_sp7\n"
".Lfk_dn7:\n"
"s_add_u32 s85, s19, 0x100000\n"
"buffer_load_dwordx4 a[176:179], v1, s[64:67], s85 offen offset:0 sc1\n"
"buffer_load_dwordx4 a[180:183], v1, s[64:67], s85 offen offset:1024 sc1\n"
"buffer_load_dwordx4 a[184:187], v1, s[64:67], s85 offen offset:2048 sc1\n"
"buffer_load_dwordx4 a[188:191], v1, s[64:67], s85 offen offset:3072 sc1\n"
"s_mul_i32 s85, s34, 5120\n"
"v_add_u32 v16, s85, v1\n"
"v_mov_b32 v48, 0\n"
"v_mov_b32 v49, 0\n"
"v_mov_b32 v50, 0\n"
"v_mov_b32 v51, 0\n"
"ds_write_b128 v16, v[48:51] offset:36864\n"
"v_pk_mul_f32 v[20:21], v[36:37], s[86:87] op_sel_hi:[1,0]\n"
"v_pk_mul_f32 v[24:25], v[68:69], s[86:87] op_sel_hi:[1,0]\n"
"v_pk_mul_f32 v[22:23], v[38:39], s[86:87] op_sel_hi:[1,0]\n"
"v_pk_mul_f32 v[26:27], v[70:71], s[86:87] op_sel_hi:[1,0]\n"
"v_exp_f32 v20, v20\n"
"v_exp_f32 v24, v24\n"
"v_exp_f32 v21, v21\n"
"v_exp_f32 v25, v25\n"
"v_exp_f32 v22, v22\n"
"v_exp_f32 v26, v26\n"
"v_exp_f32 v23, v23\n"
"v_exp_f32 v27, v27\n"
"v_pk_mul_f32 v[52:53], v[36:37], v[52:53]\n"
"v_pk_mul_f32 v[84:85], v[68:69], v[84:85]\n"
"v_pk_add_f32 v[20:21], v[20:21], 1.0 op_sel_hi:[1,0]\n"
"v_pk_add_f32 v[24:25], v[24:25], 1.0 op_sel_hi:[1,0]\n"
"v_pk_add_f32 v[22:23], v[22:23], 1.0 op_sel_hi:[1,0]\n"
"v_pk_add_f32 v[26:27], v[26:27], 1.0 op_sel_hi:[1,0]\n"
"v_pk_mul_f32 v[54:55], v[38:39], v[54:55]\n"
"v_pk_mul_f32 v[86:87], v[70:71], v[86:87]\n"
"v_rcp_f32 v20, v20\n"
"v_rcp_f32 v24, v24\n"
"v_rcp_f32 v21, v21\n"
"v_rcp_f32 v25, v25\n"
"v_rcp_f32 v22, v22\n"
"v_rcp_f32 v26, v26\n"
"v_rcp_f32 v23, v23\n"
"v_rcp_f32 v27, v27\n"
"v_pk_mul_f32 v[52:53], v[52:53], v[20:21]\n"
"v_pk_mul_f32 v[84:85], v[84:85], v[24:25]\n"
"v_pk_mul_f32 v[54:55], v[54:55], v[22:23]\n"
"v_pk_mul_f32 v[86:87], v[86:87], v[26:27]\n"
"v_cvt_pk_bf16_f32 v36, v52, v53\n"
"v_cvt_pk_bf16_f32 v38, v84, v85\n"
"v_cvt_pk_bf16_f32 v37, v54, v55\n"
"v_cvt_pk_bf16_f32 v39, v86, v87\n"
"buffer_store_dwordx4 v[36:39], v1, s[44:47], s81 offen offset:1024 sc1\n"
"v_pk_mul_f32 v[20:21], v[40:41], s[86:87] op_sel_hi:[1,0]\n"
"v_pk_mul_f32 v[24:25], v[72:73], s[86:87] op_sel_hi:[1,0]\n"
"v_pk_mul_f32 v[22:23], v[42:43], s[86:87] op_sel_hi:[1,0]\n"
"v_pk_mul_f32 v[26:27], v[74:75], s[86:87] op_sel_hi:[1,0]\n"
"v_exp_f32 v20, v20\n"
"v_exp_f32 v24, v24\n"
"v_exp_f32 v21, v21\n"
"v_exp_f32 v25, v25\n"
"v_exp_f32 v22, v22\n"
"v_exp_f32 v26, v26\n"
"v_exp_f32 v23, v23\n"
"v_exp_f32 v27, v27\n"
"v_pk_mul_f32 v[56:57], v[40:41], v[56:57]\n"
"v_pk_mul_f32 v[88:89], v[72:73], v[88:89]\n"
"v_pk_add_f32 v[20:21], v[20:21], 1.0 op_sel_hi:[1,0]\n"
"v_pk_add_f32 v[24:25], v[24:25], 1.0 op_sel_hi:[1,0]\n"
"v_pk_add_f32 v[22:23], v[22:23], 1.0 op_sel_hi:[1,0]\n"
"v_pk_add_f32 v[26:27], v[26:27], 1.0 op_sel_hi:[1,0]\n"
"v_pk_mul_f32 v[58:59], v[42:43], v[58:59]\n"
"v_pk_mul_f32 v[90:91], v[74:75], v[90:91]\n"
"v_rcp_f32 v20, v20\n"
"v_rcp_f32 v24, v24\n"
"v_rcp_f32 v21, v21\n"
"v_rcp_f32 v25, v25\n"
"v_rcp_f32 v22, v22\n"
"v_rcp_f32 v26, v26\n"
"v_rcp_f32 v23, v23\n"
"v_rcp_f32 v27, v27\n"
"v_pk_mul_f32 v[56:57], v[56:57], v[20:21]\n"
"v_pk_mul_f32 v[88:89], v[88:89], v[24:25]\n"
"v_pk_mul_f32 v[58:59], v[58:59], v[22:23]\n"
"v_pk_mul_f32 v[90:91], v[90:91], v[26:27]\n"
"v_cvt_pk_bf16_f32 v40, v56, v57\n"
"v_cvt_pk_bf16_f32 v42, v88, v89\n"
"v_cvt_pk_bf16_f32 v41, v58, v59\n"
"v_cvt_pk_bf16_f32 v43, v90, v91\n"
"buffer_store_dwordx4 v[40:43], v1, s[44:47], s81 offen offset:2048 sc1\n"
"v_pk_mul_f32 v[20:21], v[44:45], s[86:87] op_sel_hi:[1,0]\n"
"v_pk_mul_f32 v[24:25], v[76:77], s[86:87] op_sel_hi:[1,0]\n"
"v_pk_mul_f32 v[22:23], v[46:47], s[86:87] op_sel_hi:[1,0]\n"
"v_pk_mul_f32 v[26:27], v[78:79], s[86:87] op_sel_hi:[1,0]\n"
"v_exp_f32 v20, v20\n"
"v_exp_f32 v24, v24\n"
"v_exp_f32 v21, v21\n"
"v_exp_f32 v25, v25\n"
"v_exp_f32 v22, v22\n"
"v_exp_f32 v26, v26\n"
"v_exp_f32 v23, v23\n"
"v_exp_f32 v27, v27\n"
"v_pk_mul_f32 v[60:61], v[44:45], v[60:61]\n"
"v_pk_mul_f32 v[92:93], v[76:77], v[92:93]\n"
"v_pk_add_f32 v[20:21], v[20:21], 1.0 op_sel_hi:[1,0]\n"
"v_pk_add_f32 v[24:25], v[24:25], 1.0 op_sel_hi:[1,0]\n"
"v_pk_add_f32 v[22:23], v[22:23], 1.0 op_sel_hi:[1,0]\n"
"v_pk_add_f32 v[26:27], v[26:27], 1.0 op_sel_hi:[1,0]\n"
"v_pk_mul_f32 v[62:63], v[46:47], v[62:63]\n"
"v_pk_mul_f32 v[94:95], v[78:79], v[94:95]\n"
"v_rcp_f32 v20, v20\n"
"v_rcp_f32 v24, v24\n"
"v_rcp_f32 v21, v21\n"
"v_rcp_f32 v25, v25\n"
"v_rcp_f32 v22, v22\n"
"v_rcp_f32 v26, v26\n"
"v_rcp_f32 v23, v23\n"
"v_rcp_f32 v27, v27\n"
"v_pk_mul_f32 v[60:61], v[60:61], v[20:21]\n"
"v_pk_mul_f32 v[92:93], v[92:93], v[24:25]\n"
"v_pk_mul_f32 v[62:63], v[62:63], v[22:23]\n"
"v_pk_mul_f32 v[94:95], v[94:95], v[26:27]\n"
"v_cvt_pk_bf16_f32 v44, v60, v61\n"
"v_cvt_pk_bf16_f32 v46, v92, v93\n"
"v_cvt_pk_bf16_f32 v45, v62, v63\n"
"v_cvt_pk_bf16_f32 v47, v94, v95\n"
"buffer_store_dwordx4 v[44:47], v1, s[44:47], s81 offen offset:3072 sc1\n"
"v_pk_mul_f32 v[20:21], v[96:97], s[86:87] op_sel_hi:[1,0]\n"
"v_pk_mul_f32 v[24:25], v[128:129], s[86:87] op_sel_hi:[1,0]\n"
"v_pk_mul_f32 v[22:23], v[98:99], s[86:87] op_sel_hi:[1,0]\n"
"v_pk_mul_f32 v[26:27], v[130:131], s[86:87] op_sel_hi:[1,0]\n"
"v_exp_f32 v20, v20\n"
"v_exp_f32 v24, v24\n"
"v_exp_f32 v21, v21\n"
"v_exp_f32 v25, v25\n"
"v_exp_f32 v22, v22\n"
"v_exp_f32 v26, v26\n"
"v_exp_f32 v23, v23\n"
"v_exp_f32 v27, v27\n"
"v_pk_mul_f32 v[112:113], v[96:97], v[112:113]\n"
"v_pk_mul_f32 v[144:145], v[128:129], v[144:145]\n"
"v_pk_add_f32 v[20:21], v[20:21], 1.0 op_sel_hi:[1,0]\n"
"v_pk_add_f32 v[24:25], v[24:25], 1.0 op_sel_hi:[1,0]\n"
"v_pk_add_f32 v[22:23], v[22:23], 1.0 op_sel_hi:[1,0]\n"
"v_pk_add_f32 v[26:27], v[26:27], 1.0 op_sel_hi:[1,0]\n"
"v_pk_mul_f32 v[114:115], v[98:99], v[114:115]\n"
"v_pk_mul_f32 v[146:147], v[130:131], v[146:147]\n"
"v_rcp_f32 v20, v20\n"
"v_rcp_f32 v24, v24\n"
"v_rcp_f32 v21, v21\n"
"v_rcp_f32 v25, v25\n"
"v_rcp_f32 v22, v22\n"
"v_rcp_f32 v26, v26\n"
"v_rcp_f32 v23, v23\n"
"v_rcp_f32 v27, v27\n"
"v_pk_mul_f32 v[112:113], v[112:113], v[20:21]\n"
"v_pk_mul_f32 v[144:145], v[144:145], v[24:25]\n"
"v_pk_mul_f32 v[114:115], v[114:115], v[22:23]\n"
"v_pk_mul_f32 v[146:147], v[146:147], v[26:27]\n"
"v_cvt_pk_bf16_f32 v96, v112, v113\n"
"v_cvt_pk_bf16_f32 v98, v144, v145\n"
"v_cvt_pk_bf16_f32 v97, v114, v115\n"
"v_cvt_pk_bf16_f32 v99, v146, v147\n"
"s_add_u32 s81, s81, 0x20000\n"
"buffer_store_dwordx4 v[96:99], v1, s[44:47], s81 offen offset:0 sc1\n"
"v_pk_mul_f32 v[20:21], v[100:101], s[86:87] op_sel_hi:[1,0]\n"
"v_pk_mul_f32 v[24:25], v[132:133], s[86:87] op_sel_hi:[1,0]\n"
"v_pk_mul_f32 v[22:23], v[102:103], s[86:87] op_sel_hi:[1,0]\n"
"v_pk_mul_f32 v[26:27], v[134:135], s[86:87] op_sel_hi:[1,0]\n"
"v_exp_f32 v20, v20\n"
"v_exp_f32 v24, v24\n"
"v_exp_f32 v21, v21\n"
"v_exp_f32 v25, v25\n"
"v_exp_f32 v22, v22\n"
"v_exp_f32 v26, v26\n"
"v_exp_f32 v23, v23\n"
"v_exp_f32 v27, v27\n"
"v_pk_mul_f32 v[116:117], v[100:101], v[116:117]\n"
"v_pk_mul_f32 v[148:149], v[132:133], v[148:149]\n"
"v_pk_add_f32 v[20:21], v[20:21], 1.0 op_sel_hi:[1,0]\n"
"v_pk_add_f32 v[24:25], v[24:25], 1.0 op_sel_hi:[1,0]\n"
"v_pk_add_f32 v[22:23], v[22:23], 1.0 op_sel_hi:[1,0]\n"
"v_pk_add_f32 v[26:27], v[26:27], 1.0 op_sel_hi:[1,0]\n"
"v_pk_mul_f32 v[118:119], v[102:103], v[118:119]\n"
"v_pk_mul_f32 v[150:151], v[134:135], v[150:151]\n"
"v_rcp_f32 v20, v20\n"
"v_rcp_f32 v24, v24\n"
"v_rcp_f32 v21, v21\n"
"v_rcp_f32 v25, v25\n"
"v_rcp_f32 v22, v22\n"
"v_rcp_f32 v26, v26\n"
"v_rcp_f32 v23, v23\n"
"v_rcp_f32 v27, v27\n"
"v_pk_mul_f32 v[116:117], v[116:117], v[20:21]\n"
"v_pk_mul_f32 v[148:149], v[148:149], v[24:25]\n"
"v_pk_mul_f32 v[118:119], v[118:119], v[22:23]\n"
"v_pk_mul_f32 v[150:151], v[150:151], v[26:27]\n"
"v_cvt_pk_bf16_f32 v100, v116, v117\n"
"v_cvt_pk_bf16_f32 v102, v148, v149\n"
"v_cvt_pk_bf16_f32 v101, v118, v119\n"
"v_cvt_pk_bf16_f32 v103, v150, v151\n"
"buffer_store_dwordx4 v[100:103], v1, s[44:47], s81 offen offset:1024 sc1\n"
"s_waitcnt vmcnt(5)\n"
"ds_write_b128 v16, a[176:179] offset:32768\n"
"ds_write_b128 v16, a[180:183] offset:33792\n"
"ds_write_b128 v16, a[184:187] offset:34816\n"
"ds_write_b128 v16, a[188:191] offset:35840\n"
"v_pk_mul_f32 v[20:21], v[104:105], s[86:87] op_sel_hi:[1,0]\n"
"v_pk_mul_f32 v[24:25], v[136:137], s[86:87] op_sel_hi:[1,0]\n"
"v_pk_mul_f32 v[22:23], v[106:107], s[86:87] op_sel_hi:[1,0]\n"
"v_pk_mul_f32 v[26:27], v[138:139], s[86:87] op_sel_hi:[1,0]\n"
"v_exp_f32 v20, v20\n"
"v_exp_f32 v24, v24\n"
"v_exp_f32 v21, v21\n"
"v_exp_f32 v25, v25\n"
"v_exp_f32 v22, v22\n"
"v_exp_f32 v26, v26\n"
"v_exp_f32 v23, v23\n"
"v_exp_f32 v27, v27\n"
"v_pk_mul_f32 v[120:121], v[104:105], v[120:121]\n"
"v_pk_mul_f32 v[152:153], v[136:137], v[152:153]\n"
"v_pk_add_f32 v[20:21], v[20:21], 1.0 op_sel_hi:[1,0]\n"
"v_pk_add_f32 v[24:25], v[24:25], 1.0 op_sel_hi:[1,0]\n"
"v_pk_add_f32 v[22:23], v[22:23], 1.0 op_sel_hi:[1,0]\n"
"v_pk_add_f32 v[26:27], v[26:27], 1.0 op_sel_hi:[1,0]\n"
"v_pk_mul_f32 v[122:123], v[106:107], v[122:123]\n"
"v_pk_mul_f32 v[154:155], v[138:139], v[154:155]\n"
"v_rcp_f32 v20, v20\n"
"v_rcp_f32 v24, v24\n"
"v_rcp_f32 v21, v21\n"
"v_rcp_f32 v25, v25\n"
"v_rcp_f32 v22, v22\n"
"v_rcp_f32 v26, v26\n"
"v_rcp_f32 v23, v23\n"
"v_rcp_f32 v27, v27\n"
"v_pk_mul_f32 v[120:121], v[120:121], v[20:21]\n"
"v_pk_mul_f32 v[152:153], v[152:153], v[24:25]\n"
"v_pk_mul_f32 v[122:123], v[122:123], v[22:23]\n"
"v_pk_mul_f32 v[154:155], v[154:155], v[26:27]\n"
"v_cvt_pk_bf16_f32 v104, v120, v121\n"
"v_cvt_pk_bf16_f32 v106, v152, v153\n"
"v_cvt_pk_bf16_f32 v105, v122, v123\n"
"v_cvt_pk_bf16_f32 v107, v154, v155\n"
"buffer_store_dwordx4 v[104:107], v1, s[44:47], s81 offen offset:2048 sc1\n"
"v_pk_mul_f32 v[20:21], v[108:109], s[86:87] op_sel_hi:[1,0]\n"
"v_pk_mul_f32 v[24:25], v[140:141], s[86:87] op_sel_hi:[1,0]\n"
"v_pk_mul_f32 v[22:23], v[110:111], s[86:87] op_sel_hi:[1,0]\n"
"v_pk_mul_f32 v[26:27], v[142:143], s[86:87] op_sel_hi:[1,0]\n"
"v_exp_f32 v20, v20\n"
"v_exp_f32 v24, v24\n"
"v_exp_f32 v21, v21\n"
"v_exp_f32 v25, v25\n"
"v_exp_f32 v22, v22\n"
"v_exp_f32 v26, v26\n"
"v_exp_f32 v23, v23\n"
"v_exp_f32 v27, v27\n"
"v_pk_mul_f32 v[124:125], v[108:109], v[124:125]\n"
"v_pk_mul_f32 v[156:157], v[140:141], v[156:157]\n"
"v_pk_add_f32 v[20:21], v[20:21], 1.0 op_sel_hi:[1,0]\n"
"v_pk_add_f32 v[24:25], v[24:25], 1.0 op_sel_hi:[1,0]\n"
"v_pk_add_f32 v[22:23], v[22:23], 1.0 op_sel_hi:[1,0]\n"
"v_pk_add_f32 v[26:27], v[26:27], 1.0 op_sel_hi:[1,0]\n"
"v_pk_mul_f32 v[126:127], v[110:111], v[126:127]\n"
"v_pk_mul_f32 v[158:159], v[142:143], v[158:159]\n"
"v_rcp_f32 v20, v20\n"
"v_rcp_f32 v24, v24\n"
"v_rcp_f32 v21, v21\n"
"v_rcp_f32 v25, v25\n"
"v_rcp_f32 v22, v22\n"
"v_rcp_f32 v26, v26\n"
"v_rcp_f32 v23, v23\n"
"v_rcp_f32 v27, v27\n"
"v_pk_mul_f32 v[124:125], v[124:125], v[20:21]\n"
"v_pk_mul_f32 v[156:157], v[156:157], v[24:25]\n"
"v_pk_mul_f32 v[126:127], v[126:127], v[22:23]\n"
"v_pk_mul_f32 v[158:159], v[158:159], v[26:27]\n"
"v_cvt_pk_bf16_f32 v108, v124, v125\n"
"v_cvt_pk_bf16_f32 v110, v156, v157\n"
"v_cvt_pk_bf16_f32 v109, v126, v127\n"
"v_cvt_pk_bf16_f32 v111, v158, v159\n"
"buffer_store_dwordx4 v[108:111], v1, s[44:47], s81 offen offset:3072 sc1\n"
"v_pk_mul_f32 v[20:21], v[160:161], s[86:87] op_sel_hi:[1,0]\n"
"v_pk_mul_f32 v[24:25], v[192:193], s[86:87] op_sel_hi:[1,0]\n"
"v_pk_mul_f32 v[22:23], v[162:163], s[86:87] op_sel_hi:[1,0]\n"
"v_pk_mul_f32 v[26:27], v[194:195], s[86:87] op_sel_hi:[1,0]\n"
"v_exp_f32 v20, v20\n"
"v_exp_f32 v24, v24\n"
"v_exp_f32 v21, v21\n"
"v_exp_f32 v25, v25\n"
"v_exp_f32 v22, v22\n"
"v_exp_f32 v26, v26\n"
"v_exp_f32 v23, v23\n"
"v_exp_f32 v27, v27\n"
"v_pk_mul_f32 v[176:177], v[160:161], v[176:177]\n"
"v_pk_mul_f32 v[208:209], v[192:193], v[208:209]\n"
"v_pk_add_f32 v[20:21], v[20:21], 1.0 op_sel_hi:[1,0]\n"
"v_pk_add_f32 v[24:25], v[24:25], 1.0 op_sel_hi:[1,0]\n"
"v_pk_add_f32 v[22:23], v[22:23], 1.0 op_sel_hi:[1,0]\n"
"v_pk_add_f32 v[26:27], v[26:27], 1.0 op_sel_hi:[1,0]\n"
"v_pk_mul_f32 v[178:179], v[162:163], v[178:179]\n"
"v_pk_mul_f32 v[210:211], v[194:195], v[210:211]\n"
"v_rcp_f32 v20, v20\n"
"v_rcp_f32 v24, v24\n"
"v_rcp_f32 v21, v21\n"
"v_rcp_f32 v25, v25\n"
"v_rcp_f32 v22, v22\n"
"v_rcp_f32 v26, v26\n"
"v_rcp_f32 v23, v23\n"
"v_rcp_f32 v27, v27\n"
"v_pk_mul_f32 v[176:177], v[176:177], v[20:21]\n"
"v_pk_mul_f32 v[208:209], v[208:209], v[24:25]\n"
"v_pk_mul_f32 v[178:179], v[178:179], v[22:23]\n"
"v_pk_mul_f32 v[210:211], v[210:211], v[26:27]\n"
"v_cvt_pk_bf16_f32 v160, v176, v177\n"
"v_cvt_pk_bf16_f32 v162, v208, v209\n"
"v_cvt_pk_bf16_f32 v161, v178, v179\n"
"v_cvt_pk_bf16_f32 v163, v210, v211\n"
"s_add_u32 s81, s81, 0x20000\n"
"buffer_store_dwordx4 v[160:163], v1, s[44:47], s81 offen offset:0 sc1\n"
"v_pk_mul_f32 v[20:21], v[164:165], s[86:87] op_sel_hi:[1,0]\n"
"v_pk_mul_f32 v[24:25], v[196:197], s[86:87] op_sel_hi:[1,0]\n"
"v_pk_mul_f32 v[22:23], v[166:167], s[86:87] op_sel_hi:[1,0]\n"
"v_pk_mul_f32 v[26:27], v[198:199], s[86:87] op_sel_hi:[1,0]\n"
"v_exp_f32 v20, v20\n"
"v_exp_f32 v24, v24\n"
"v_exp_f32 v21, v21\n"
"v_exp_f32 v25, v25\n"
"v_exp_f32 v22, v22\n"
"v_exp_f32 v26, v26\n"
"v_exp_f32 v23, v23\n"
"v_exp_f32 v27, v27\n"
"v_pk_mul_f32 v[180:181], v[164:165], v[180:181]\n"
"v_pk_mul_f32 v[212:213], v[196:197], v[212:213]\n"
"v_pk_add_f32 v[20:21], v[20:21], 1.0 op_sel_hi:[1,0]\n"
"v_pk_add_f32 v[24:25], v[24:25], 1.0 op_sel_hi:[1,0]\n"
"v_pk_add_f32 v[22:23], v[22:23], 1.0 op_sel_hi:[1,0]\n"
"v_pk_add_f32 v[26:27], v[26:27], 1.0 op_sel_hi:[1,0]\n"
"v_pk_mul_f32 v[182:183], v[166:167], v[182:183]\n"
"v_pk_mul_f32 v[214:215], v[198:199], v[214:215]\n"
"v_rcp_f32 v20, v20\n"
"v_rcp_f32 v24, v24\n"
"v_rcp_f32 v21, v21\n"
"v_rcp_f32 v25, v25\n"
"v_rcp_f32 v22, v22\n"
"v_rcp_f32 v26, v26\n"
"v_rcp_f32 v23, v23\n"
"v_rcp_f32 v27, v27\n"
"v_pk_mul_f32 v[180:181], v[180:181], v[20:21]\n"
"v_pk_mul_f32 v[212:213], v[212:213], v[24:25]\n"
"v_pk_mul_f32 v[182:183], v[182:183], v[22:23]\n"
"v_pk_mul_f32 v[214:215], v[214:215], v[26:27]\n"
"v_cvt_pk_bf16_f32 v164, v180, v181\n"
"v_cvt_pk_bf16_f32 v166, v212, v213\n"
"v_cvt_pk_bf16_f32 v165, v182, v183\n"
"v_cvt_pk_bf16_f32 v167, v214, v215\n"
"buffer_store_dwordx4 v[164:167], v1, s[44:47], s81 offen offset:1024 sc1\n"
"v_pk_mul_f32 v[20:21], v[168:169], s[86:87] op_sel_hi:[1,0]\n"
"v_pk_mul_f32 v[24:25], v[200:201], s[86:87] op_sel_hi:[1,0]\n"
"v_pk_mul_f32 v[22:23], v[170:171], s[86:87] op_sel_hi:[1,0]\n"
"v_pk_mul_f32 v[26:27], v[202:203], s[86:87] op_sel_hi:[1,0]\n"
"ds_read_b128 a[96:99], v28 offset:3584\n"
"ds_read_b64 a[64:65], v3 offset:0\n"
"v_exp_f32 v20, v20\n"
"v_exp_f32 v24, v24\n"
"v_exp_f32 v21, v21\n"
"v_exp_f32 v25, v25\n"
"v_exp_f32 v22, v22\n"
"v_exp_f32 v26, v26\n"
"v_exp_f32 v23, v23\n"
"v_exp_f32 v27, v27\n"
"v_pk_mul_f32 v[184:185], v[168:169], v[184:185]\n"
"v_pk_mul_f32 v[216:217], v[200:201], v[216:217]\n"
"v_pk_add_f32 v[20:21], v[20:21], 1.0 op_sel_hi:[1,0]\n"
"v_pk_add_f32 v[24:25], v[24:25], 1.0 op_sel_hi:[1,0]\n"
"ds_read_b64 a[66:67], v3 offset:8\n"
"ds_read_b128 a[100:103], v28 offset:3072\n"
"v_pk_add_f32 v[22:23], v[22:23], 1.0 op_sel_hi:[1,0]\n"
"v_pk_add_f32 v[26:27], v[26:27], 1.0 op_sel_hi:[1,0]\n"
"v_pk_mul_f32 v[186:187], v[170:171], v[186:187]\n"
"v_pk_mul_f32 v[218:219], v[202:203], v[218:219]\n"
"v_rcp_f32 v20, v20\n"
"v_rcp_f32 v24, v24\n"
"v_rcp_f32 v21, v21\n"
"v_rcp_f32 v25, v25\n"
"v_rcp_f32 v22, v22\n"
"v_rcp_f32 v26, v26\n"
"v_rcp_f32 v23, v23\n"
"v_rcp_f32 v27, v27\n"
"ds_read_b64 a[68:69], v3 offset:512\n"
"ds_read_b64 a[70:71], v3 offset:520\n"
"v_pk_mul_f32 v[184:185], v[184:185], v[20:21]\n"
"v_pk_mul_f32 v[216:217], v[216:217], v[24:25]\n"
"v_pk_mul_f32 v[186:187], v[186:187], v[22:23]\n"
"v_pk_mul_f32 v[218:219], v[218:219], v[26:27]\n"
"v_cvt_pk_bf16_f32 v168, v184, v185\n"
"v_cvt_pk_bf16_f32 v170, v216, v217\n"
"v_cvt_pk_bf16_f32 v169, v186, v187\n"
"v_cvt_pk_bf16_f32 v171, v218, v219\n"
"buffer_store_dwordx4 v[168:171], v1, s[44:47], s81 offen offset:2048 sc1\n"
"v_pk_mul_f32 v[20:21], v[172:173], s[86:87] op_sel_hi:[1,0]\n"
"v_pk_mul_f32 v[24:25], v[204:205], s[86:87] op_sel_hi:[1,0]\n"
"v_pk_mul_f32 v[22:23], v[174:175], s[86:87] op_sel_hi:[1,0]\n"
"v_pk_mul_f32 v[26:27], v[206:207], s[86:87] op_sel_hi:[1,0]\n"
"ds_read_b128 a[104:107], v28 offset:2560\n"
"ds_read_b64 a[72:73], v3 offset:1024\n"
"v_exp_f32 v20, v20\n"
"v_exp_f32 v24, v24\n"
"v_exp_f32 v21, v21\n"
"v_exp_f32 v25, v25\n"
"v_exp_f32 v22, v22\n"
"v_exp_f32 v26, v26\n"
"v_exp_f32 v23, v23\n"
"v_exp_f32 v27, v27\n"
"v_pk_mul_f32 v[188:189], v[172:173], v[188:189]\n"
"v_pk_mul_f32 v[220:221], v[204:205], v[220:221]\n"
"v_pk_add_f32 v[20:21], v[20:21], 1.0 op_sel_hi:[1,0]\n"
"v_pk_add_f32 v[24:25], v[24:25], 1.0 op_sel_hi:[1,0]\n"
"ds_read_b64 a[74:75], v3 offset:1032\n"
"ds_read_b128 a[108:111], v28 offset:2048\n"
"v_pk_add_f32 v[22:23], v[22:23], 1.0 op_sel_hi:[1,0]\n"
"v_pk_add_f32 v[26:27], v[26:27], 1.0 op_sel_hi:[1,0]\n"
"v_pk_mul_f32 v[190:191], v[174:175], v[190:191]\n"
"v_pk_mul_f32 v[222:223], v[206:207], v[222:223]\n"
"v_rcp_f32 v20, v20\n"
"v_rcp_f32 v24, v24\n"
"v_rcp_f32 v21, v21\n"
"v_rcp_f32 v25, v25\n"
"v_rcp_f32 v22, v22\n"
"v_rcp_f32 v26, v26\n"
"v_rcp_f32 v23, v23\n"
"v_rcp_f32 v27, v27\n"
"ds_read_b64 a[76:77], v3 offset:1536\n"
"ds_read_b64 a[78:79], v3 offset:1544\n"
"v_pk_mul_f32 v[188:189], v[188:189], v[20:21]\n"
"v_pk_mul_f32 v[220:221], v[220:221], v[24:25]\n"
"v_pk_mul_f32 v[190:191], v[190:191], v[22:23]\n"
"v_pk_mul_f32 v[222:223], v[222:223], v[26:27]\n"
"v_cvt_pk_bf16_f32 v172, v188, v189\n"
"v_cvt_pk_bf16_f32 v174, v220, v221\n"
"v_cvt_pk_bf16_f32 v173, v190, v191\n"
"v_cvt_pk_bf16_f32 v175, v222, v223\n"
"buffer_store_dwordx4 v[172:175], v1, s[44:47], s81 offen offset:3072 sc1\n"
"v_pk_mul_f32 v[20:21], v[224:225], s[86:87] op_sel_hi:[1,0]\n"
"v_pk_mul_f32 v[24:25], v[228:229], s[86:87] op_sel_hi:[1,0]\n"
"v_pk_mul_f32 v[22:23], v[226:227], s[86:87] op_sel_hi:[1,0]\n"
"v_pk_mul_f32 v[26:27], v[230:231], s[86:87] op_sel_hi:[1,0]\n"
"ds_read_b128 a[112:115], v28 offset:1536\n"
"ds_read_b64 a[80:81], v3 offset:2048\n"
"v_exp_f32 v20, v20\n"
"v_exp_f32 v24, v24\n"
"v_exp_f32 v21, v21\n"
"v_exp_f32 v25, v25\n"
"v_exp_f32 v22, v22\n"
"v_exp_f32 v26, v26\n"
"v_exp_f32 v23, v23\n"
"v_exp_f32 v27, v27\n"
"v_pk_mul_f32 v[240:241], v[224:225], v[240:241]\n"
"v_pk_mul_f32 v[244:245], v[228:229], v[244:245]\n"
"v_pk_add_f32 v[20:21], v[20:21], 1.0 op_sel_hi:[1,0]\n"
"v_pk_add_f32 v[24:25], v[24:25], 1.0 op_sel_hi:[1,0]\n"
"ds_read_b64 a[82:83], v3 offset:2056\n"
"ds_read_b128 a[116:119], v28 offset:1024\n"
"v_pk_add_f32 v[22:23], v[22:23], 1.0 op_sel_hi:[1,0]\n"
"v_pk_add_f32 v[26:27], v[26:27], 1.0 op_sel_hi:[1,0]\n"
"v_pk_mul_f32 v[242:243], v[226:227], v[242:243]\n"
"v_pk_mul_f32 v[246:247], v[230:231], v[246:247]\n"
"v_rcp_f32 v20, v20\n"
"v_rcp_f32 v24, v24\n"
"v_rcp_f32 v21, v21\n"
"v_rcp_f32 v25, v25\n"
"v_rcp_f32 v22, v22\n"
"v_rcp_f32 v26, v26\n"
"v_rcp_f32 v23, v23\n"
"v_rcp_f32 v27, v27\n"
"ds_read_b64 a[84:85], v3 offset:2560\n"
"ds_read_b64 a[86:87], v3 offset:2568\n"
"v_pk_mul_f32 v[240:241], v[240:241], v[20:21]\n"
"v_pk_mul_f32 v[244:245], v[244:245], v[24:25]\n"
"v_pk_mul_f32 v[242:243], v[242:243], v[22:23]\n"
"v_pk_mul_f32 v[246:247], v[246:247], v[26:27]\n"
"v_cvt_pk_bf16_f32 v224, v240, v241\n"
"v_cvt_pk_bf16_f32 v228, v244, v245\n"
"v_cvt_pk_bf16_f32 v225, v242, v243\n"
"v_cvt_pk_bf16_f32 v229, v246, v247\n"
"buffer_store_dwordx2 v[224:225], v2, s[44:47], s84 offen offset:0 sc1\n"
"buffer_store_dwordx2 v[228:229], v2, s[44:47], s84 offen offset:1024 sc1\n"
"v_pk_mul_f32 v[20:21], v[232:233], s[86:87] op_sel_hi:[1,0]\n"
"v_pk_mul_f32 v[24:25], v[236:237], s[86:87] op_sel_hi:[1,0]\n"
"v_pk_mul_f32 v[22:23], v[234:235], s[86:87] op_sel_hi:[1,0]\n"
"v_pk_mul_f32 v[26:27], v[238:239], s[86:87] op_sel_hi:[1,0]\n"
"ds_read_b128 a[120:123], v28 offset:512\n"
"ds_read_b64 a[88:89], v3 offset:3072\n"
"v_exp_f32 v20, v20\n"
"v_exp_f32 v24, v24\n"
"v_exp_f32 v21, v21\n"
"v_exp_f32 v25, v25\n"
"v_exp_f32 v22, v22\n"
"v_exp_f32 v26, v26\n"
"v_exp_f32 v23, v23\n"
"v_exp_f32 v27, v27\n"
"v_pk_mul_f32 v[248:249], v[232:233], v[248:249]\n"
"v_pk_mul_f32 v[252:253], v[236:237], v[252:253]\n"
"v_pk_add_f32 v[20:21], v[20:21], 1.0 op_sel_hi:[1,0]\n"
"v_pk_add_f32 v[24:25], v[24:25], 1.0 op_sel_hi:[1,0]\n"
"ds_read_b64 a[90:91], v3 offset:3080\n"
"ds_read_b128 a[124:127], v28 offset:0\n"
"v_pk_add_f32 v[22:23], v[22:23], 1.0 op_sel_hi:[1,0]\n"
"v_pk_add_f32 v[26:27], v[26:27], 1.0 op_sel_hi:[1,0]\n"
"v_pk_mul_f32 v[250:251], v[234:235], v[250:251]\n"
"v_pk_mul_f32 v[254:255], v[238:239], v[254:255]\n"
"v_rcp_f32 v20, v20\n"
"v_rcp_f32 v24, v24\n"
"v_rcp_f32 v21, v21\n"
"v_rcp_f32 v25, v25\n"
"v_rcp_f32 v22, v22\n"
"v_rcp_f32 v26, v26\n"
"v_rcp_f32 v23, v23\n"
"v_rcp_f32 v27, v27\n"
"ds_read_b64 a[92:93], v3 offset:3584\n"
"ds_read_b64 a[94:95], v3 offset:3592\n"
"v_pk_mul_f32 v[248:249], v[248:249], v[20:21]\n"
"v_pk_mul_f32 v[252:253], v[252:253], v[24:25]\n"
"v_pk_mul_f32 v[250:251], v[250:251], v[22:23]\n"
"v_pk_mul_f32 v[254:255], v[254:255], v[26:27]\n"
"v_cvt_pk_bf16_f32 v232, v248, v249\n"
"v_cvt_pk_bf16_f32 v236, v252, v253\n"
"v_cvt_pk_bf16_f32 v233, v250, v251\n"
"v_cvt_pk_bf16_f32 v237, v254, v255\n"
"buffer_store_dwordx2 v[232:233], v2, s[44:47], s84 offen offset:2048 sc1\n"
"buffer_store_dwordx2 v[236:237], v2, s[44:47], s84 offen offset:3072 sc1\n"
"v_mfma_f32_16x16x32_bf16 v[84:87], a[248:251], a[232:235], 0\n"
"s_add_u32 m0, s77, 0x0\n"
"s_bfe_u32 s79, s8, 0x60000\n"
"v_mfma_f32_16x16x32_bf16 v[84:87], a[252:255], a[236:239], v[84:87]\n"
"s_lshl_b32 s79, s79, 15\n"
"v_lshl_add_u32 v16, v0, 4, s28\n"
"v_mfma_f32_16x16x32_bf16 v[88:91], a[248:251], a[240:243], 0\n"
"buffer_load_dwordx4 v4, s[48:51], s79 offen offset:0 lds\n"
"v_mfma_f32_16x16x32_bf16 v[88:91], a[252:255], a[244:247], v[88:91]\n"
"v_lshl_add_u32 v18, v0, 3, s28\n"
"s_waitcnt lgkmcnt(15)\n"
"v_mfma_f32_16x16x32_bf16 v[32:35], a[64:67], a[96:99], 0\n"
"v_lshl_add_u32 v17, v0, 4, s29\n"
"v_lshl_add_u32 v19, v0, 3, s29\n"
"v_mfma_f32_16x16x32_bf16 v[32:35], v[8:11], a[96:99], v[32:35]\n"
"s_mul_i32 s81, s34, 4112\n"
"v_mfma_f32_16x16x32_bf16 v[36:39], a[68:71], a[96:99], 0\n"
"buffer_load_dwordx4 v5, s[48:51], s79 offen offset:1024 lds\n"
"v_mfma_f32_16x16x32_bf16 v[36:39], a[64:67], a[100:103], v[36:39]\n"
"s_add_u32 s81, s81, 139264\n"
"v_lshl_add_u32 v20, v0, 3, s81\n"
"v_mfma_f32_16x16x32_bf16 v[36:39], v[8:11], a[100:103], v[36:39]\n"
".Lfk_hop5236:\n"
"s_cbranch_execz .Lfk_hop5810\n"
"ds_read_b128 a[32:35], v28 offset:3648\n"
"v_mfma_f32_16x16x32_bf16 v[40:43], a[64:67], a[104:107], 0\n"
"buffer_load_dwordx4 v6, s[48:51], s79 offen offset:2048 lds\n"
"v_mfma_f32_16x16x32_bf16 v[40:43], a[68:71], a[100:103], v[40:43]\n"
"ds_read_b64 a[0:1], v3 offset:64\n"
"v_bfe_u32 v21, v0, 2, 2\n"
"v_mfma_f32_16x16x32_bf16 v[40:43], a[72:75], a[96:99], v[40:43]\n"
"ds_read_b64 a[2:3], v3 offset:72\n"
"v_mul_u32_u24 v21, 4112, v21\n"
"v_mfma_f32_16x16x32_bf16 v[40:43], v[8:11], a[104:107], v[40:43]\n"
"ds_read_b128 a[36:39], v28 offset:3136\n"
"s_waitcnt lgkmcnt(15)\n"
"v_mfma_f32_16x16x32_bf16 v[44:47], a[76:79], a[96:99], 0\n"
"buffer_load_dwordx4 v7, s[48:51], s79 offen offset:3072 lds\n"
"v_mfma_f32_16x16x32_bf16 v[44:47], a[72:75], a[100:103], v[44:47]\n"
"ds_read_b64 a[4:5], v3 offset:576\n"
"s_add_u32 m0, s77, 0x2000\n"
"v_mfma_f32_16x16x32_bf16 v[44:47], a[68:71], a[104:107], v[44:47]\n"
"s_bfe_u32 s79, s14, 0x60000\n"
"s_lshl_b32 s79, s79, 15\n"
"v_mfma_f32_16x16x32_bf16 v[44:47], a[64:67], a[108:111], v[44:47]\n"
"ds_read_b64 a[6:7], v3 offset:584\n"
"v_and_b32 v22, 3, v0\n"
"v_mfma_f32_16x16x32_bf16 v[44:47], v[8:11], a[108:111], v[44:47]\n"
"v_lshl_add_u32 v21, v22, 7, v21\n"
"s_waitcnt lgkmcnt(15)\n"
"v_mfma_f32_16x16x32_bf16 v[48:51], a[64:67], a[112:115], 0\n"
"ds_read_b128 a[40:43], v28 offset:2624\n"
"v_lshrrev_b32 v22, 4, v0\n"
"v_mfma_f32_16x16x32_bf16 v[48:51], a[68:71], a[108:111], v[48:51]\n"
"ds_read_b64 a[8:9], v3 offset:1088\n"
"v_lshl_add_u32 v21, v22, 3, v21\n"
"v_mfma_f32_16x16x32_bf16 v[48:51], a[72:75], a[104:107], v[48:51]\n"
"buffer_load_dwordx4 v4, s[48:51], s79 offen offset:0 lds\n"
"v_mfma_f32_16x16x32_bf16 v[48:51], a[76:79], a[100:103], v[48:51]\n"
"s_lshl_b32 s81, s34, 10\n"
"s_add_u32 s81, s81, 139264\n"
"v_mfma_f32_16x16x32_bf16 v[48:51], a[80:83], a[96:99], v[48:51]\n"
"ds_read_b64 a[10:11], v3 offset:1096\n"
"v_add_u32 v21, s81, v21\n"
"v_mfma_f32_16x16x32_bf16 v[48:51], v[8:11], a[112:115], v[48:51]\n"
"ds_read_b128 a[44:47], v28 offset:2112\n"
"s_waitcnt lgkmcnt(15)\n"
"v_mfma_f32_16x16x32_bf16 v[52:55], a[84:87], a[96:99], 0\n"
"ds_read_b64 a[12:13], v3 offset:1600\n"
"s_mov_b32 vcc_lo, 0x00100001\n"
"v_mfma_f32_16x16x32_bf16 v[52:55], a[80:83], a[100:103], v[52:55]\n"
"buffer_load_dwordx4 v5, s[48:51], s79 offen offset:1024 lds\n"
"v_mfma_f32_16x16x32_bf16 v[52:55], a[76:79], a[104:107], v[52:55]\n"
"ds_read_b64 a[14:15], v3 offset:1608\n"
"s_mov_b32 vcc_hi, 0x10000100\n"
"v_mfma_f32_16x16x32_bf16 v[52:55], a[72:75], a[108:111], v[52:55]\n"
"ds_read_b128 a[48:51], v28 offset:1600\n"
"v_cndmask_b32 v64, 0, v84, vcc\n"
"v_mfma_f32_16x16x32_bf16 v[52:55], a[68:71], a[112:115], v[52:55]\n"
"buffer_load_dwordx4 v6, s[48:51], s79 offen offset:2048 lds\n"
"v_mfma_f32_16x16x32_bf16 v[52:55], a[64:67], a[116:119], v[52:55]\n"
"v_cndmask_b32 v65, 0, v88, vcc\n"
"s_lshl_b64 vcc, vcc, 1\n"
"v_mfma_f32_16x16x32_bf16 v[52:55], v[8:11], a[116:119], v[52:55]\n"
"ds_read_b64 a[16:17], v3 offset:2112\n"
"s_waitcnt lgkmcnt(15)\n"
"v_mfma_f32_16x16x32_bf16 v[56:59], a[64:67], a[120:123], 0\n"
"ds_read_b64 a[18:19], v3 offset:2120\n"
"v_cndmask_b32 v64, v64, v85, vcc\n"
"v_mfma_f32_16x16x32_bf16 v[56:59], a[68:71], a[116:119], v[56:59]\n"
"ds_read_b128 a[52:55], v28 offset:1088\n"
"v_cndmask_b32 v65, v65, v89, vcc\n"
"v_mfma_f32_16x16x32_bf16 v[56:59], a[72:75], a[112:115], v[56:59]\n"
"buffer_load_dwordx4 v7, s[48:51], s79 offen offset:3072 lds\n"
"v_mfma_f32_16x16x32_bf16 v[56:59], a[76:79], a[108:111], v[56:59]\n"
"ds_read_b64 a[20:21], v3 offset:2624\n"
"s_lshl_b64 vcc, vcc, 1\n"
"v_mfma_f32_16x16x32_bf16 v[56:59], a[80:83], a[104:107], v[56:59]\n"
"ds_read_b64 a[22:23], v3 offset:2632\n"
"v_cndmask_b32 v64, v64, v86, vcc\n"
"v_mfma_f32_16x16x32_bf16 v[56:59], a[84:87], a[100:103], v[56:59]\n"
"v_cndmask_b32 v65, v65, v90, vcc\n"
"s_lshl_b64 vcc, vcc, 1\n"
"v_mfma_f32_16x16x32_bf16 v[56:59], a[88:91], a[96:99], v[56:59]\n"
"v_cndmask_b32 v64, v64, v87, vcc\n"
"v_cndmask_b32 v65, v65, v91, vcc\n"
"v_mfma_f32_16x16x32_bf16 v[56:59], v[8:11], a[120:123], v[56:59]\n"
"ds_read_b128 a[56:59], v28 offset:576\n"
"s_waitcnt lgkmcnt(15)\n"
"v_mfma_f32_16x16x32_bf16 v[60:63], a[92:95], a[96:99], 0\n"
"ds_read_b64 a[24:25], v3 offset:3136\n"
"s_lshl_b32 s19, s72, 5\n"
"v_mfma_f32_16x16x32_bf16 v[60:63], a[88:91], a[100:103], v[60:63]\n"
"ds_read_b64 a[26:27], v3 offset:3144\n"
"s_add_u32 s19, s19, s73\n"
"v_mfma_f32_16x16x32_bf16 v[60:63], a[84:87], a[104:107], v[60:63]\n"
"s_lshl2_add_u32 s19, s19, s34\n"
"s_lshl_b32 s19, s19, 3\n"
"v_mfma_f32_16x16x32_bf16 v[60:63], a[80:83], a[108:111], v[60:63]\n"
"ds_read_b128 a[60:63], v28 offset:64\n"
"s_add_u32 s19, s19, 0x10000\n"
"v_mfma_f32_16x16x32_bf16 v[60:63], a[76:79], a[112:115], v[60:63]\n"
"ds_read_b64 a[28:29], v3 offset:3648\n"
"v_add_f32_dpp v64, v64, v64 row_shr:1 row_mask:0xf bank_mask:0xf bound_ctrl:1\n"
"v_mfma_f32_16x16x32_bf16 v[60:63], a[72:75], a[116:119], v[60:63]\n"
"v_add_f32_dpp v65, v65, v65 row_shr:1 row_mask:0xf bank_mask:0xf bound_ctrl:1\n"
"v_add_f32_dpp v64, v64, v64 row_shr:2 row_mask:0xf bank_mask:0xf bound_ctrl:1\n"
"v_mfma_f32_16x16x32_bf16 v[60:63], a[68:71], a[120:123], v[60:63]\n"
"v_add_f32_dpp v65, v65, v65 row_shr:2 row_mask:0xf bank_mask:0xf bound_ctrl:1\n"
"v_add_f32_dpp v64, v64, v64 row_shr:4 row_mask:0xf bank_mask:0xf bound_ctrl:1\n"
"v_mfma_f32_16x16x32_bf16 v[60:63], a[64:67], a[124:127], v[60:63]\n"
"ds_read_b64 a[30:31], v3 offset:3656\n"
"v_add_f32_dpp v65, v65, v65 row_shr:4 row_mask:0xf bank_mask:0xf bound_ctrl:1\n"
"v_mfma_f32_16x16x32_bf16 v[60:63], v[8:11], a[124:127], v[60:63]\n"
"v_add_f32_dpp v64, v64, v64 row_shr:8 row_mask:0xf bank_mask:0xf bound_ctrl:1\n"
"s_waitcnt vmcnt(8)\n"
"s_barrier\n"
"s_cmp_lg_u32 s34, 0\n"
"s_cbranch_scc1 .Lfk_hsig\n"
"s_lshl_b32 s85, s26, 10\n"
"s_add_u32 s85, s85, 32768\n"
"v_lshl_add_u32 v30, v0, 7, s85\n"
"v_mov_b32 v31, 1\n"
"s_mov_b64 exec, 0xff\n"
"global_atomic_add v30, v31, s[30:31]\n"
"s_mov_b64 exec, -1\n"
".Lfk_hsig:\n"
"s_waitcnt lgkmcnt(6)\n"
"v_mfma_f32_16x16x32_bf16 v[32:35], a[0:3], a[32:35], v[32:35]\n"
"v_mov_b32 v80, -1\n"
"v_mov_b32 v81, -1\n"
"v_mfma_f32_16x16x32_bf16 v[36:39], a[4:7], a[32:35], v[36:39]\n"
"v_mov_b32 v82, -1\n"
"v_mov_b32 v83, -1\n"
"v_mfma_f32_16x16x32_bf16 v[36:39], a[0:3], a[36:39], v[36:39]\n"
"ds_read_b128 a[96:99], v28 offset:3712\n"
"s_mul_i32 s81, s21, 53248\n"
"v_mfma_f32_16x16x32_bf16 v[40:43], a[0:3], a[40:43], v[40:43]\n"
"ds_read_b64 a[64:65], v3 offset:128\n"
"s_xor_b32 s82, s20, 1\n"
"v_mfma_f32_16x16x32_bf16 v[40:43], a[4:7], a[36:39], v[40:43]\n"
"ds_read_b64 a[66:67], v3 offset:136\n"
"s_lshl_b32 s82, s82, 2\n"
"v_mfma_f32_16x16x32_bf16 v[40:43], a[8:11], a[32:35], v[40:43]\n"
"buffer_store_dwordx4 v[80:83], v1, s[56:59], s18 offen offset:0 sc1\n"
"v_mfma_f32_16x16x32_bf16 v[44:47], a[12:15], a[32:35], v[44:47]\n"
"ds_read_b128 a[100:103], v28 offset:3200\n"
"v_lshrrev_b32 v74, 2, v0\n"
"v_mfma_f32_16x16x32_bf16 v[44:47], a[8:11], a[36:39], v[44:47]\n"
"ds_read_b64 a[68:69], v3 offset:640\n"
"v_lshlrev_b32 v75, 8, v74\n"
"v_mfma_f32_16x16x32_bf16 v[44:47], a[4:7], a[40:43], v[44:47]\n"
"ds_read_b64 a[70:71], v3 offset:648\n"
"v_and_b32 v76, 3, v0\n"
"v_mfma_f32_16x16x32_bf16 v[44:47], a[0:3], a[44:47], v[44:47]\n"
"buffer_store_dwordx4 v[80:83], v1, s[56:59], s18 offen offset:1024 sc1\n"
"v_mfma_f32_16x16x32_bf16 v[48:51], a[0:3], a[48:51], v[48:51]\n"
"ds_read_b128 a[104:107], v28 offset:2688\n"
"s_lshr_b32 s24, s33, 3\n"
"v_mfma_f32_16x16x32_bf16 v[48:51], a[4:7], a[44:47], v[48:51]\n"
"ds_read_b64 a[72:73], v3 offset:1152\n"
"s_lshl2_add_u32 s24, s24, s34\n"
"v_mfma_f32_16x16x32_bf16 v[48:51], a[8:11], a[40:43], v[48:51]\n"
"buffer_store_dwordx4 v[80:83], v1, s[56:59], s18 offen offset:2048 sc1\n"
"v_mfma_f32_16x16x32_bf16 v[48:51], a[12:15], a[36:39], v[48:51]\n"
"s_and_b32 s25, s24, 63\n"
"s_mul_i32 s25, s25, 7\n"
"v_mfma_f32_16x16x32_bf16 v[48:51], a[16:19], a[32:35], v[48:51]\n"
"ds_read_b64 a[74:75], v3 offset:1160\n"
"s_waitcnt lgkmcnt(9)\n"
"v_mfma_f32_16x16x32_bf16 v[52:55], a[20:23], a[32:35], v[52:55]\n"
"ds_read_b128 a[108:111], v28 offset:2176\n"
"s_lshr_b32 s24, s24, 6\n"
"v_mfma_f32_16x16x32_bf16 v[52:55], a[16:19], a[36:39], v[52:55]\n"
"ds_read_b64 a[76:77], v3 offset:1664\n"
"s_lshl1_add_u32 s24, s20, s24\n"
"v_mfma_f32_16x16x32_bf16 v[52:55], a[12:15], a[40:43], v[52:55]\n"
"buffer_store_dwordx4 v[80:83], v1, s[56:59], s18 offen offset:3072 sc1\n"
"v_mfma_f32_16x16x32_bf16 v[52:55], a[8:11], a[44:47], v[52:55]\n"
"ds_read_b64 a[78:79], v3 offset:1672\n"
"s_mul_i32 s24, s24, 896\n"
"v_mfma_f32_16x16x32_bf16 v[52:55], a[4:7], a[48:51], v[52:55]\n"
"ds_read_b128 a[112:115], v28 offset:1664\n"
"s_add_u32 s24, s24, s25\n"
"v_mfma_f32_16x16x32_bf16 v[52:55], a[0:3], a[52:55], v[52:55]\n"
"ds_read_b64 a[80:81], v3 offset:2176\n"
"s_add_u32 s24, s24, 0\n"
"v_mfma_f32_16x16x32_bf16 v[56:59], a[0:3], a[56:59], v[56:59]\n"
"s_lshr_b32 s25, s75, 3\n"
"s_lshl1_add_u32 s24, s24, s25\n"
"v_mfma_f32_16x16x32_bf16 v[56:59], a[4:7], a[52:55], v[56:59]\n"
"ds_read_b64 a[82:83], v3 offset:2184\n"
"s_lshl_b32 s24, s24, 10\n"
"v_mfma_f32_16x16x32_bf16 v[56:59], a[8:11], a[48:51], v[56:59]\n"
"ds_read_b128 a[116:119], v28 offset:1152\n"
"v_lshl_add_u32 v75, v76, 3, v75\n"
"v_mfma_f32_16x16x32_bf16 v[56:59], a[12:15], a[44:47], v[56:59]\n"
"ds_read_b64 a[84:85], v3 offset:2688\n"
"v_min_u32 v23, 55, v0\n"
"v_mfma_f32_16x16x32_bf16 v[56:59], a[16:19], a[40:43], v[56:59]\n"
"v_lshlrev_b32 v23, 7, v23\n"
"v_add_u32 v75, s81, v75\n"
"v_mfma_f32_16x16x32_bf16 v[56:59], a[20:23], a[36:39], v[56:59]\n"
"ds_read_b64 a[86:87], v3 offset:2696\n"
"v_and_b32 v73, 0xfc00, v23\n"
"v_mfma_f32_16x16x32_bf16 v[56:59], a[24:27], a[32:35], v[56:59]\n"
"ds_read_b128 a[120:123], v28 offset:640\n"
"v_add_u32 v23, v73, v23\n"
"v_mfma_f32_16x16x32_bf16 v[60:63], a[28:31], a[32:35], v[60:63]\n"
"ds_read_b64 a[88:89], v3 offset:3200\n"
"v_and_b32 v74, 7, v74\n"
"v_mfma_f32_16x16x32_bf16 v[60:63], a[24:27], a[36:39], v[60:63]\n"
"buffer_load_dword v72, v23, s[48:51], s24 offen\n"
"v_mfma_f32_16x16x32_bf16 v[60:63], a[20:23], a[40:43], v[60:63]\n"
"ds_read_b64 a[90:91], v3 offset:3208\n"
"v_xor_b32 v74, s82, v74\n"
"v_mfma_f32_16x16x32_bf16 v[60:63], a[16:19], a[44:47], v[60:63]\n"
"ds_read_b128 a[124:127], v28 offset:128\n"
"v_xor_b32 v76, 0, v74\n"
"v_mfma_f32_16x16x32_bf16 v[60:63], a[12:15], a[48:51], v[60:63]\n"
"ds_read_b64 a[92:93], v3 offset:3712\n"
"v_lshl_add_u32 v8, v76, 5, v75\n"
"v_mfma_f32_16x16x32_bf16 v[60:63], a[8:11], a[52:55], v[60:63]\n"
"v_xor_b32 v76, 1, v74\n"
"v_lshl_add_u32 v9, v76, 5, v75\n"
"v_mfma_f32_16x16x32_bf16 v[60:63], a[4:7], a[56:59], v[60:63]\n"
"ds_read_b64 a[94:95], v3 offset:3720\n"
"v_xor_b32 v76, 2, v74\n"
"v_mfma_f32_16x16x32_bf16 v[60:63], a[0:3], a[60:63], v[60:63]\n"
"v_lshl_add_u32 v10, v76, 5, v75\n"
"v_xor_b32 v76, 3, v74\n"
"s_waitcnt lgkmcnt(6)\n"
"v_mfma_f32_16x16x32_bf16 v[32:35], a[64:67], a[96:99], v[32:35]\n"
"v_lshl_add_u32 v11, v76, 5, v75\n"
"v_xor_b32 v76, 4, v74\n"
"v_mfma_f32_16x16x32_bf16 v[36:39], a[68:71], a[96:99], v[36:39]\n"
"s_cmp_eq_u32 s34, 0\n"
"s_cselect_b32 s85, s96, 0x7ff00000\n"
"v_mov_b32 v30, s85\n"
"buffer_load_dword v30, v30, s[64:67], 0 offen sc1\n"
"v_mfma_f32_16x16x32_bf16 v[36:39], a[64:67], a[100:103], v[36:39]\n"
"ds_read_b128 a[32:35], v28 offset:3776\n"
"v_lshl_add_u32 v12, v76, 5, v75\n"
"v_mfma_f32_16x16x32_bf16 v[40:43], a[64:67], a[104:107], v[40:43]\n"
"ds_read_b64 a[0:1], v3 offset:192\n"
"s_add_u32 m0, s77, 0x4000\n"
"v_mfma_f32_16x16x32_bf16 v[40:43], a[68:71], a[100:103], v[40:43]\n"
"ds_read_b64 a[2:3], v3 offset:200\n"
"s_bfe_u32 s79, s8, 0x60008\n"
"v_mfma_f32_16x16x32_bf16 v[40:43], a[72:75], a[96:99], v[40:43]\n"
"s_lshl_b32 s79, s79, 15\n"
"v_xor_b32 v76, 5, v74\n"
"v_mfma_f32_16x16x32_bf16 v[44:47], a[76:79], a[96:99], v[44:47]\n"
"ds_read_b128 a[36:39], v28 offset:3264\n"
"v_lshl_add_u32 v13, v76, 5, v75\n"
"v_mfma_f32_16x16x32_bf16 v[44:47], a[72:75], a[100:103], v[44:47]\n"
"ds_read_b64 a[4:5], v3 offset:704\n"
"v_xor_b32 v76, 6, v74\n"
"v_mfma_f32_16x16x32_bf16 v[44:47], a[68:71], a[104:107], v[44:47]\n"
"ds_read_b64 a[6:7], v3 offset:712\n"
"v_lshl_add_u32 v14, v76, 5, v75\n"
"v_mfma_f32_16x16x32_bf16 v[44:47], a[64:67], a[108:111], v[44:47]\n"
"buffer_load_dwordx4 v4, s[48:51], s79 offen offset:0 lds\n"
"v_mfma_f32_16x16x32_bf16 v[48:51], a[64:67], a[112:115], v[48:51]\n"
"ds_read_b128 a[40:43], v28 offset:2752\n"
"v_xor_b32 v76, 7, v74\n"
"v_mfma_f32_16x16x32_bf16 v[48:51], a[68:71], a[108:111], v[48:51]\n"
"ds_read_b64 a[8:9], v3 offset:1216\n"
"v_lshl_add_u32 v15, v76, 5, v75\n"
"v_mfma_f32_16x16x32_bf16 v[48:51], a[72:75], a[104:107], v[48:51]\n"
"buffer_load_dwordx4 v5, s[48:51], s79 offen offset:1024 lds\n"
"v_mfma_f32_16x16x32_bf16 v[48:51], a[76:79], a[100:103], v[48:51]\n"
"v_add_f32_dpp v65, v65, v65 row_shr:8 row_mask:0xf bank_mask:0xf bound_ctrl:1\n"
"v_add_f32_dpp v64, v64, v64 row_bcast:15 row_mask:0xa bank_mask:0xf\n"
"v_mfma_f32_16x16x32_bf16 v[48:51], a[80:83], a[96:99], v[48:51]\n"
"ds_read_b64 a[10:11], v3 offset:1224\n"
"s_waitcnt lgkmcnt(9)\n"
"v_mfma_f32_16x16x32_bf16 v[52:55], a[84:87], a[96:99], v[52:55]\n"
"ds_read_b128 a[44:47], v28 offset:2240\n"
"v_add_f32_dpp v65, v65, v65 row_bcast:15 row_mask:0xa bank_mask:0xf\n"
"v_mfma_f32_16x16x32_bf16 v[52:55], a[80:83], a[100:103], v[52:55]\n"
"ds_read_b64 a[12:13], v3 offset:1728\n"
"v_add_f32_dpp v64, v64, v64 row_bcast:31 row_mask:0xc bank_mask:0xf\n"
"v_mfma_f32_16x16x32_bf16 v[52:55], a[76:79], a[104:107], v[52:55]\n"
"buffer_load_dwordx4 v6, s[48:51], s79 offen offset:2048 lds\n"
"v_mfma_f32_16x16x32_bf16 v[52:55], a[72:75], a[108:111], v[52:55]\n"
"ds_read_b64 a[14:15], v3 offset:1736\n"
"v_add_f32_dpp v65, v65, v65 row_bcast:31 row_mask:0xc bank_mask:0xf\n"
"v_mfma_f32_16x16x32_bf16 v[52:55], a[68:71], a[112:115], v[52:55]\n"
"ds_read_b128 a[48:51], v28 offset:1728\n"
"v_cmp_ne_u32 vcc, 63, v0\n"
"v_mfma_f32_16x16x32_bf16 v[52:55], a[64:67], a[116:119], v[52:55]\n"
"ds_read_b64 a[16:17], v3 offset:2240\n"
"v_mov_b32 v66, 0x7ffffff0\n"
"v_mfma_f32_16x16x32_bf16 v[56:59], a[64:67], a[120:123], v[56:59]\n"
"buffer_load_dwordx4 v7, s[48:51], s79 offen offset:3072 lds\n"
"v_mfma_f32_16x16x32_bf16 v[56:59], a[68:71], a[116:119], v[56:59]\n"
"ds_read_b64 a[18:19], v3 offset:2248\n"
"s_add_u32 m0, s77, 0x6000\n"
"v_mfma_f32_16x16x32_bf16 v[56:59], a[72:75], a[112:115], v[56:59]\n"
"ds_read_b128 a[52:55], v28 offset:1216\n"
"s_bfe_u32 s79, s14, 0x60008\n"
"v_mfma_f32_16x16x32_bf16 v[56:59], a[76:79], a[108:111], v[56:59]\n"
"ds_read_b64 a[20:21], v3 offset:2752\n"
"s_lshl_b32 s79, s79, 15\n"
"v_mfma_f32_16x16x32_bf16 v[56:59], a[80:83], a[104:107], v[56:59]\n"
"buffer_load_dwordx4 v4, s[48:51], s79 offen offset:0 lds\n"
"v_mfma_f32_16x16x32_bf16 v[56:59], a[84:87], a[100:103], v[56:59]\n"
"ds_read_b64 a[22:23], v3 offset:2760\n"
"s_lshr_b32 s24, s33, 3\n"
"v_mfma_f32_16x16x32_bf16 v[56:59], a[88:91], a[96:99], v[56:59]\n"
"ds_read_b128 a[56:59], v28 offset:704\n"
"s_lshl2_add_u32 s24, s24, s34\n"
"v_mfma_f32_16x16x32_bf16 v[60:63], a[92:95], a[96:99], v[60:63]\n"
"ds_read_b64 a[24:25], v3 offset:3264\n"
"s_lshl_b32 s24, s24, 7\n"
"v_mfma_f32_16x16x32_bf16 v[60:63], a[88:91], a[100:103], v[60:63]\n"
"buffer_load_dwordx4 v5, s[48:51], s79 offen offset:1024 lds\n"
"v_mfma_f32_16x16x32_bf16 v[60:63], a[84:87], a[104:107], v[60:63]\n"
"ds_read_b64 a[26:27], v3 offset:3272\n"
"v_mov_b32 v23, s24\n"
"v_mfma_f32_16x16x32_bf16 v[60:63], a[80:83], a[108:111], v[60:63]\n"
"ds_read_b128 a[60:63], v28 offset:192\n"
"v_mul_f32 v64, 0xbfb8aa3b, v64\n"
"v_mfma_f32_16x16x32_bf16 v[60:63], a[76:79], a[112:115], v[60:63]\n"
"ds_read_b64 a[28:29], v3 offset:3776\n"
"v_mul_f32 v65, 0xbfb8aa3b, v65\n"
"v_mfma_f32_16x16x32_bf16 v[60:63], a[72:75], a[116:119], v[60:63]\n"
"buffer_load_dwordx4 v6, s[48:51], s79 offen offset:2048 lds\n"
"v_mfma_f32_16x16x32_bf16 v[60:63], a[68:71], a[120:123], v[60:63]\n"
"ds_read_b64 a[30:31], v3 offset:3784\n"
"v_mfma_f32_16x16x32_bf16 v[60:63], a[64:67], a[124:127], v[60:63]\n"
"s_getpc_b64 s[24:25]\n"
".Lfk_ct8:\n"
"s_add_u32 s24, s24, 0xbb8\n"
"s_addc_u32 s25, s25, 0\n"
"global_load_dword v72, v23, s[24:25]\n"
"s_waitcnt lgkmcnt(6)\n"
"s_waitcnt vmcnt(8)\n"
"s_cmp_lg_u32 s34, 0\n"
"s_cbranch_scc1 .Lfk_hchk\n"
"v_readfirstlane_b32 s85, v30\n"
"s_nop 3\n"
"s_cmp_ge_u32 s85, 16\n"
"s_cbranch_scc1 .Lfk_dn9\n"
"s_mov_b32 s35, 0x10000\n"
".Lfk_sp9:\n"
"s_sleep 1\n"
"buffer_load_dword v30, off, s[64:67], s96 sc1\n"
"s_waitcnt vmcnt(0)\n"
"v_readfirstlane_b32 s85, v30\n"
"s_nop 3\n"
"s_cmp_ge_u32 s85, 16\n"
"s_cbranch_scc1 .Lfk_dn9\n"
"s_sub_u32 s35, s35, 1\n"
"s_cmp_lg_u32 s35, 0\n"
"s_cbranch_scc1 .Lfk_sp9\n"
".Lfk_dn9:\n"
".Lfk_hchk:\n"
"s_barrier\n"
"v_mfma_f32_16x16x32_bf16 v[32:35], a[0:3], a[32:35], v[32:35]\n"
"buffer_load_dwordx4 v7, s[48:51], s79 offen offset:3072 lds\n"
"v_mfma_f32_16x16x32_bf16 v[36:39], a[4:7], a[32:35], v[36:39]\n"
"s_bfe_u32 s80, s8, 0x50001\n"
"s_lshl_b32 s80, s80, 17\n"
"v_mfma_f32_16x16x32_bf16 v[36:39], a[0:3], a[36:39], v[36:39]\n"
"ds_read_b128 a[96:99], v28 offset:3840\n"
"v_mfma_f32_16x16x32_bf16 v[40:43], a[0:3], a[40:43], v[40:43]\n"
"ds_read_b64 a[64:65], v3 offset:256\n"
"v_mfma_f32_16x16x32_bf16 v[40:43], a[4:7], a[36:39], v[40:43]\n"
"ds_read_b64 a[66:67], v3 offset:264\n"
"v_mfma_f32_16x16x32_bf16 v[40:43], a[8:11], a[32:35], v[40:43]\n"
"buffer_load_dwordx4 v[96:99], v16, s[44:47], s80 offen offset:0 sc1\n"
"v_mfma_f32_16x16x32_bf16 v[44:47], a[12:15], a[32:35], v[44:47]\n"
"ds_read_b128 a[100:103], v28 offset:3328\n"
"v_mfma_f32_16x16x32_bf16 v[44:47], a[8:11], a[36:39], v[44:47]\n"
"ds_read_b64 a[68:69], v3 offset:768\n"
"v_mfma_f32_16x16x32_bf16 v[44:47], a[4:7], a[40:43], v[44:47]\n"
"ds_read_b64 a[70:71], v3 offset:776\n"
"v_mfma_f32_16x16x32_bf16 v[44:47], a[0:3], a[44:47], v[44:47]\n"
"buffer_load_dwordx4 v[100:103], v16, s[44:47], s80 offen offset:1024 sc1\n"
"v_mfma_f32_16x16x32_bf16 v[48:51], a[0:3], a[48:51], v[48:51]\n"
"ds_read_b128 a[104:107], v28 offset:2816\n"
"v_mfma_f32_16x16x32_bf16 v[48:51], a[4:7], a[44:47], v[48:51]\n"
"ds_read_b64 a[72:73], v3 offset:1280\n"
"v_mfma_f32_16x16x32_bf16 v[48:51], a[8:11], a[40:43], v[48:51]\n"
"buffer_load_dwordx4 v[104:107], v17, s[44:47], s80 offen offset:0 sc1\n"
"v_mfma_f32_16x16x32_bf16 v[48:51], a[12:15], a[36:39], v[48:51]\n"
"v_exp_f32 v64, v64\n"
"v_mfma_f32_16x16x32_bf16 v[48:51], a[16:19], a[32:35], v[48:51]\n"
"ds_read_b64 a[74:75], v3 offset:1288\n"
"s_waitcnt lgkmcnt(9)\n"
"v_mfma_f32_16x16x32_bf16 v[52:55], a[20:23], a[32:35], v[52:55]\n"
"ds_read_b128 a[108:111], v28 offset:2304\n"
"v_mfma_f32_16x16x32_bf16 v[52:55], a[16:19], a[36:39], v[52:55]\n"
"ds_read_b64 a[76:77], v3 offset:1792\n"
"v_mfma_f32_16x16x32_bf16 v[52:55], a[12:15], a[40:43], v[52:55]\n"
"buffer_load_dwordx4 v[108:111], v17, s[44:47], s80 offen offset:1024 sc1\n"
"v_mfma_f32_16x16x32_bf16 v[52:55], a[8:11], a[44:47], v[52:55]\n"
"ds_read_b64 a[78:79], v3 offset:1800\n"
"s_add_u32 s80, s80, 0x20000\n"
"v_mfma_f32_16x16x32_bf16 v[52:55], a[4:7], a[48:51], v[52:55]\n"
"ds_read_b128 a[112:115], v28 offset:1792\n"
"v_mfma_f32_16x16x32_bf16 v[52:55], a[0:3], a[52:55], v[52:55]\n"
"ds_read_b64 a[80:81], v3 offset:2304\n"
"v_mfma_f32_16x16x32_bf16 v[56:59], a[0:3], a[56:59], v[56:59]\n"
"buffer_load_dwordx4 v[112:115], v16, s[44:47], s80 offen offset:0 sc1\n"
"v_mfma_f32_16x16x32_bf16 v[56:59], a[4:7], a[52:55], v[56:59]\n"
"ds_read_b64 a[82:83], v3 offset:2312\n"
"v_mfma_f32_16x16x32_bf16 v[56:59], a[8:11], a[48:51], v[56:59]\n"
"ds_read_b128 a[116:119], v28 offset:1280\n"
"v_mfma_f32_16x16x32_bf16 v[56:59], a[12:15], a[44:47], v[56:59]\n"
"ds_read_b64 a[84:85], v3 offset:2816\n"
"v_mfma_f32_16x16x32_bf16 v[56:59], a[16:19], a[40:43], v[56:59]\n"
"buffer_load_dwordx4 v[116:119], v16, s[44:47], s80 offen offset:1024 sc1\n"
"v_mfma_f32_16x16x32_bf16 v[56:59], a[20:23], a[36:39], v[56:59]\n"
"ds_read_b64 a[86:87], v3 offset:2824\n"
"v_mfma_f32_16x16x32_bf16 v[56:59], a[24:27], a[32:35], v[56:59]\n"
"ds_read_b128 a[120:123], v28 offset:768\n"
"v_mfma_f32_16x16x32_bf16 v[60:63], a[28:31], a[32:35], v[60:63]\n"
"ds_read_b64 a[88:89], v3 offset:3328\n"
"v_mfma_f32_16x16x32_bf16 v[60:63], a[24:27], a[36:39], v[60:63]\n"
"buffer_load_dwordx4 v[120:123], v17, s[44:47], s80 offen offset:0 sc1\n"
"v_mfma_f32_16x16x32_bf16 v[60:63], a[20:23], a[40:43], v[60:63]\n"
"ds_read_b64 a[90:91], v3 offset:3336\n"
"v_mfma_f32_16x16x32_bf16 v[60:63], a[16:19], a[44:47], v[60:63]\n"
"ds_read_b128 a[124:127], v28 offset:256\n"
"v_mfma_f32_16x16x32_bf16 v[60:63], a[12:15], a[48:51], v[60:63]\n"
"ds_read_b64 a[92:93], v3 offset:3840\n"
"v_mfma_f32_16x16x32_bf16 v[60:63], a[8:11], a[52:55], v[60:63]\n"
"buffer_load_dwordx4 v[124:127], v17, s[44:47], s80 offen offset:1024 sc1\n"
"v_mfma_f32_16x16x32_bf16 v[60:63], a[4:7], a[56:59], v[60:63]\n"
"ds_read_b64 a[94:95], v3 offset:3848\n"
"s_bfe_u32 s80, s8, 0x50009\n"
"v_mfma_f32_16x16x32_bf16 v[60:63], a[0:3], a[60:63], v[60:63]\n"
"s_lshl_b32 s80, s80, 17\n"
"s_waitcnt lgkmcnt(6)\n"
"v_mfma_f32_16x16x32_bf16 v[32:35], a[64:67], a[96:99], v[32:35]\n"
"buffer_load_dwordx4 v[128:131], v16, s[44:47], s80 offen offset:0 sc1\n"
"v_mfma_f32_16x16x32_bf16 v[36:39], a[68:71], a[96:99], v[36:39]\n"
"v_exp_f32 v65, v65\n"
"v_mfma_f32_16x16x32_bf16 v[36:39], a[64:67], a[100:103], v[36:39]\n"
"ds_read_b128 a[32:35], v28 offset:3904\n"
"v_cndmask_b32 v66, 0, v66, vcc\n"
"v_mfma_f32_16x16x32_bf16 v[40:43], a[64:67], a[104:107], v[40:43]\n"
"ds_read_b64 a[0:1], v3 offset:320\n"
"v_add_f32 v64, 1.0, v64\n"
"v_mfma_f32_16x16x32_bf16 v[40:43], a[68:71], a[100:103], v[40:43]\n"
"ds_read_b64 a[2:3], v3 offset:328\n"
"v_add_f32 v65, 1.0, v65\n"
"v_mfma_f32_16x16x32_bf16 v[40:43], a[72:75], a[96:99], v[40:43]\n"
"buffer_load_dwordx4 v[132:135], v16, s[44:47], s80 offen offset:1024 sc1\n"
"v_mfma_f32_16x16x32_bf16 v[44:47], a[76:79], a[96:99], v[44:47]\n"
"ds_read_b128 a[36:39], v28 offset:3392\n"
"v_mfma_f32_16x16x32_bf16 v[44:47], a[72:75], a[100:103], v[44:47]\n"
"ds_read_b64 a[4:5], v3 offset:832\n"
"v_mfma_f32_16x16x32_bf16 v[44:47], a[68:71], a[104:107], v[44:47]\n"
"ds_read_b64 a[6:7], v3 offset:840\n"
"v_mfma_f32_16x16x32_bf16 v[44:47], a[64:67], a[108:111], v[44:47]\n"
"buffer_load_dwordx4 v[136:139], v17, s[44:47], s80 offen offset:0 sc1\n"
"v_mfma_f32_16x16x32_bf16 v[48:51], a[64:67], a[112:115], v[48:51]\n"
"ds_read_b128 a[40:43], v28 offset:2880\n"
"v_mfma_f32_16x16x32_bf16 v[48:51], a[68:71], a[108:111], v[48:51]\n"
"ds_read_b64 a[8:9], v3 offset:1344\n"
"v_mfma_f32_16x16x32_bf16 v[48:51], a[72:75], a[104:107], v[48:51]\n"
"buffer_load_dwordx4 v[140:143], v17, s[44:47], s80 offen offset:1024 sc1\n"
"v_mfma_f32_16x16x32_bf16 v[48:51], a[76:79], a[100:103], v[48:51]\n"
"s_add_u32 s80, s80, 0x20000\n"
"v_mfma_f32_16x16x32_bf16 v[48:51], a[80:83], a[96:99], v[48:51]\n"
"ds_read_b64 a[10:11], v3 offset:1352\n"
"s_waitcnt lgkmcnt(9)\n"
"v_mfma_f32_16x16x32_bf16 v[52:55], a[84:87], a[96:99], v[52:55]\n"
"ds_read_b128 a[44:47], v28 offset:2368\n"
"v_mfma_f32_16x16x32_bf16 v[52:55], a[80:83], a[100:103], v[52:55]\n"
"ds_read_b64 a[12:13], v3 offset:1856\n"
"v_mfma_f32_16x16x32_bf16 v[52:55], a[76:79], a[104:107], v[52:55]\n"
"buffer_load_dwordx4 v[144:147], v16, s[44:47], s80 offen offset:0 sc1\n"
"v_mfma_f32_16x16x32_bf16 v[52:55], a[72:75], a[108:111], v[52:55]\n"
"ds_read_b64 a[14:15], v3 offset:1864\n"
"v_mfma_f32_16x16x32_bf16 v[52:55], a[68:71], a[112:115], v[52:55]\n"
"ds_read_b128 a[48:51], v28 offset:1856\n"
"v_mfma_f32_16x16x32_bf16 v[52:55], a[64:67], a[116:119], v[52:55]\n"
"ds_read_b64 a[16:17], v3 offset:2368\n"
"v_mfma_f32_16x16x32_bf16 v[56:59], a[64:67], a[120:123], v[56:59]\n"
"buffer_load_dwordx4 v[148:151], v16, s[44:47], s80 offen offset:1024 sc1\n"
"v_mfma_f32_16x16x32_bf16 v[56:59], a[68:71], a[116:119], v[56:59]\n"
"ds_read_b64 a[18:19], v3 offset:2376\n"
"v_mfma_f32_16x16x32_bf16 v[56:59], a[72:75], a[112:115], v[56:59]\n"
"ds_read_b128 a[52:55], v28 offset:1344\n"
"v_mfma_f32_16x16x32_bf16 v[56:59], a[76:79], a[108:111], v[56:59]\n"
"ds_read_b64 a[20:21], v3 offset:2880\n"
"v_mfma_f32_16x16x32_bf16 v[56:59], a[80:83], a[104:107], v[56:59]\n"
"buffer_load_dwordx4 v[152:155], v17, s[44:47], s80 offen offset:0 sc1\n"
"v_mfma_f32_16x16x32_bf16 v[56:59], a[84:87], a[100:103], v[56:59]\n"
"ds_read_b64 a[22:23], v3 offset:2888\n"
"v_mfma_f32_16x16x32_bf16 v[56:59], a[88:91], a[96:99], v[56:59]\n"
"ds_read_b128 a[56:59], v28 offset:832\n"
"v_mfma_f32_16x16x32_bf16 v[60:63], a[92:95], a[96:99], v[60:63]\n"
"ds_read_b64 a[24:25], v3 offset:3392\n"
"v_mfma_f32_16x16x32_bf16 v[60:63], a[88:91], a[100:103], v[60:63]\n"
"buffer_load_dwordx4 v[156:159], v17, s[44:47], s80 offen offset:1024 sc1\n"
"v_mfma_f32_16x16x32_bf16 v[60:63], a[84:87], a[104:107], v[60:63]\n"
"ds_read_b64 a[26:27], v3 offset:3400\n"
"s_bfe_u32 s80, s8, 0x50011\n"
"v_mfma_f32_16x16x32_bf16 v[60:63], a[80:83], a[108:111], v[60:63]\n"
"ds_read_b128 a[60:63], v28 offset:320\n"
"s_lshl_b32 s80, s80, 17\n"
"v_mfma_f32_16x16x32_bf16 v[60:63], a[76:79], a[112:115], v[60:63]\n"
"ds_read_b64 a[28:29], v3 offset:3904\n"
"v_mfma_f32_16x16x32_bf16 v[60:63], a[72:75], a[116:119], v[60:63]\n"
"buffer_load_dwordx4 v[160:163], v16, s[44:47], s80 offen offset:0 sc1\n"
"v_mfma_f32_16x16x32_bf16 v[60:63], a[68:71], a[120:123], v[60:63]\n"
"ds_read_b64 a[30:31], v3 offset:3912\n"
"v_mfma_f32_16x16x32_bf16 v[60:63], a[64:67], a[124:127], v[60:63]\n"
"v_rcp_f32 v64, v64\n"
"s_waitcnt lgkmcnt(6)\n"
"v_mfma_f32_16x16x32_bf16 v[32:35], a[0:3], a[32:35], v[32:35]\n"
"buffer_load_dwordx4 v[164:167], v16, s[44:47], s80 offen offset:1024 sc1\n"
"v_mfma_f32_16x16x32_bf16 v[36:39], a[4:7], a[32:35], v[36:39]\n"
"v_rcp_f32 v65, v65\n"
"v_mfma_f32_16x16x32_bf16 v[36:39], a[0:3], a[36:39], v[36:39]\n"
"ds_read_b128 a[96:99], v28 offset:3968\n"
"v_mfma_f32_16x16x32_bf16 v[40:43], a[0:3], a[40:43], v[40:43]\n"
"ds_read_b64 a[64:65], v3 offset:384\n"
"v_mfma_f32_16x16x32_bf16 v[40:43], a[4:7], a[36:39], v[40:43]\n"
"ds_read_b64 a[66:67], v3 offset:392\n"
"v_mfma_f32_16x16x32_bf16 v[40:43], a[8:11], a[32:35], v[40:43]\n"
"buffer_load_dwordx4 v[168:171], v17, s[44:47], s80 offen offset:0 sc1\n"
"v_mfma_f32_16x16x32_bf16 v[44:47], a[12:15], a[32:35], v[44:47]\n"
"ds_read_b128 a[100:103], v28 offset:3456\n"
"v_mfma_f32_16x16x32_bf16 v[44:47], a[8:11], a[36:39], v[44:47]\n"
"ds_read_b64 a[68:69], v3 offset:896\n"
"v_mfma_f32_16x16x32_bf16 v[44:47], a[4:7], a[40:43], v[44:47]\n"
"ds_read_b64 a[70:71], v3 offset:904\n"
"v_mfma_f32_16x16x32_bf16 v[44:47], a[0:3], a[44:47], v[44:47]\n"
"buffer_load_dwordx4 v[172:175], v17, s[44:47], s80 offen offset:1024 sc1\n"
"v_mfma_f32_16x16x32_bf16 v[48:51], a[0:3], a[48:51], v[48:51]\n"
"ds_read_b128 a[104:107], v28 offset:2944\n"
"s_add_u32 s80, s80, 0x20000\n"
"v_mfma_f32_16x16x32_bf16 v[48:51], a[4:7], a[44:47], v[48:51]\n"
"ds_read_b64 a[72:73], v3 offset:1408\n"
"v_mfma_f32_16x16x32_bf16 v[48:51], a[8:11], a[40:43], v[48:51]\n"
"buffer_load_dwordx4 v[176:179], v16, s[44:47], s80 offen offset:0 sc1\n"
"v_mfma_f32_16x16x32_bf16 v[48:51], a[12:15], a[36:39], v[48:51]\n"
"buffer_store_dwordx2 v[64:65], v66, s[64:67], s19 offen sc1\n"
"v_mfma_f32_16x16x32_bf16 v[48:51], a[16:19], a[32:35], v[48:51]\n"
"ds_read_b64 a[74:75], v3 offset:1416\n"
"s_waitcnt lgkmcnt(9)\n"
"v_mfma_f32_16x16x32_bf16 v[52:55], a[20:23], a[32:35], v[52:55]\n"
"ds_read_b128 a[108:111], v28 offset:2432\n"
"v_mfma_f32_16x16x32_bf16 v[52:55], a[16:19], a[36:39], v[52:55]\n"
"ds_read_b64 a[76:77], v3 offset:1920\n"
"v_mfma_f32_16x16x32_bf16 v[52:55], a[12:15], a[40:43], v[52:55]\n"
"buffer_load_dwordx4 v[180:183], v16, s[44:47], s80 offen offset:1024 sc1\n"
"v_mfma_f32_16x16x32_bf16 v[52:55], a[8:11], a[44:47], v[52:55]\n"
"ds_read_b64 a[78:79], v3 offset:1928\n"
"v_mfma_f32_16x16x32_bf16 v[52:55], a[4:7], a[48:51], v[52:55]\n"
"ds_read_b128 a[112:115], v28 offset:1920\n"
"v_mfma_f32_16x16x32_bf16 v[52:55], a[0:3], a[52:55], v[52:55]\n"
"ds_read_b64 a[80:81], v3 offset:2432\n"
"v_mfma_f32_16x16x32_bf16 v[56:59], a[0:3], a[56:59], v[56:59]\n"
"buffer_load_dwordx4 v[184:187], v17, s[44:47], s80 offen offset:0 sc1\n"
"v_mfma_f32_16x16x32_bf16 v[56:59], a[4:7], a[52:55], v[56:59]\n"
"ds_read_b64 a[82:83], v3 offset:2440\n"
"v_mfma_f32_16x16x32_bf16 v[56:59], a[8:11], a[48:51], v[56:59]\n"
"ds_read_b128 a[116:119], v28 offset:1408\n"
"v_mfma_f32_16x16x32_bf16 v[56:59], a[12:15], a[44:47], v[56:59]\n"
"ds_read_b64 a[84:85], v3 offset:2944\n"
"v_mfma_f32_16x16x32_bf16 v[56:59], a[16:19], a[40:43], v[56:59]\n"
"buffer_load_dwordx4 v[188:191], v17, s[44:47], s80 offen offset:1024 sc1\n"
"v_mfma_f32_16x16x32_bf16 v[56:59], a[20:23], a[36:39], v[56:59]\n"
"ds_read_b64 a[86:87], v3 offset:2952\n"
"s_bfe_u32 s80, s8, 0x50019\n"
"v_mfma_f32_16x16x32_bf16 v[56:59], a[24:27], a[32:35], v[56:59]\n"
"ds_read_b128 a[120:123], v28 offset:896\n"
"s_lshl_b32 s80, s80, 17\n"
"v_mfma_f32_16x16x32_bf16 v[60:63], a[28:31], a[32:35], v[60:63]\n"
"ds_read_b64 a[88:89], v3 offset:3456\n"
".Lfk_hop5810:\n"
"s_cbranch_execz .Lfk_hopret\n"
"v_mfma_f32_16x16x32_bf16 v[60:63], a[24:27], a[36:39], v[60:63]\n"
"buffer_load_dwordx4 v[192:195], v16, s[44:47], s80 offen offset:0 sc1\n"
"v_mfma_f32_16x16x32_bf16 v[60:63], a[20:23], a[40:43], v[60:63]\n"
"ds_read_b64 a[90:91], v3 offset:3464\n"
"v_mfma_f32_16x16x32_bf16 v[60:63], a[16:19], a[44:47], v[60:63]\n"
"ds_read_b128 a[124:127], v28 offset:384\n"
"v_mfma_f32_16x16x32_bf16 v[60:63], a[12:15], a[48:51], v[60:63]\n"
"ds_read_b64 a[92:93], v3 offset:3968\n"
"v_mfma_f32_16x16x32_bf16 v[60:63], a[8:11], a[52:55], v[60:63]\n"
"buffer_load_dwordx4 v[196:199], v16, s[44:47], s80 offen offset:1024 sc1\n"
"v_mfma_f32_16x16x32_bf16 v[60:63], a[4:7], a[56:59], v[60:63]\n"
"ds_read_b64 a[94:95], v3 offset:3976\n"
"v_mfma_f32_16x16x32_bf16 v[60:63], a[0:3], a[60:63], v[60:63]\n"
"s_waitcnt lgkmcnt(6)\n"
"v_mfma_f32_16x16x32_bf16 v[32:35], a[64:67], a[96:99], v[32:35]\n"
"buffer_load_dwordx4 v[200:203], v17, s[44:47], s80 offen offset:0 sc1\n"
"v_mfma_f32_16x16x32_bf16 v[36:39], a[68:71], a[96:99], v[36:39]\n"
"v_mfma_f32_16x16x32_bf16 v[36:39], a[64:67], a[100:103], v[36:39]\n"
"ds_read_b128 a[32:35], v28 offset:4032\n"
"v_mfma_f32_16x16x32_bf16 v[40:43], a[64:67], a[104:107], v[40:43]\n"
"ds_read_b64 a[0:1], v3 offset:448\n"
"v_mfma_f32_16x16x32_bf16 v[40:43], a[68:71], a[100:103], v[40:43]\n"
"ds_read_b64 a[2:3], v3 offset:456\n"
"v_mfma_f32_16x16x32_bf16 v[40:43], a[72:75], a[96:99], v[40:43]\n"
"buffer_load_dwordx4 v[204:207], v17, s[44:47], s80 offen offset:1024 sc1\n"
"v_mfma_f32_16x16x32_bf16 v[44:47], a[76:79], a[96:99], v[44:47]\n"
"ds_read_b128 a[36:39], v28 offset:3520\n"
"s_add_u32 s80, s80, 0x20000\n"
"v_mfma_f32_16x16x32_bf16 v[44:47], a[72:75], a[100:103], v[44:47]\n"
"ds_read_b64 a[4:5], v3 offset:960\n"
"v_mfma_f32_16x16x32_bf16 v[44:47], a[68:71], a[104:107], v[44:47]\n"
"ds_read_b64 a[6:7], v3 offset:968\n"
"v_mfma_f32_16x16x32_bf16 v[44:47], a[64:67], a[108:111], v[44:47]\n"
"buffer_load_dwordx4 v[208:211], v16, s[44:47], s80 offen offset:0 sc1\n"
"v_mfma_f32_16x16x32_bf16 v[48:51], a[64:67], a[112:115], v[48:51]\n"
"ds_read_b128 a[40:43], v28 offset:3008\n"
"v_mfma_f32_16x16x32_bf16 v[48:51], a[68:71], a[108:111], v[48:51]\n"
"ds_read_b64 a[8:9], v3 offset:1472\n"
"v_mfma_f32_16x16x32_bf16 v[48:51], a[72:75], a[104:107], v[48:51]\n"
"buffer_load_dwordx4 v[212:215], v16, s[44:47], s80 offen offset:1024 sc1\n"
"v_mfma_f32_16x16x32_bf16 v[48:51], a[76:79], a[100:103], v[48:51]\n"
"v_mfma_f32_16x16x32_bf16 v[48:51], a[80:83], a[96:99], v[48:51]\n"
"ds_read_b64 a[10:11], v3 offset:1480\n"
"s_waitcnt lgkmcnt(9)\n"
"v_mfma_f32_16x16x32_bf16 v[52:55], a[84:87], a[96:99], v[52:55]\n"
"ds_read_b128 a[44:47], v28 offset:2496\n"
"v_mfma_f32_16x16x32_bf16 v[52:55], a[80:83], a[100:103], v[52:55]\n"
"ds_read_b64 a[12:13], v3 offset:1984\n"
"v_mfma_f32_16x16x32_bf16 v[52:55], a[76:79], a[104:107], v[52:55]\n"
"buffer_load_dwordx4 v[216:219], v17, s[44:47], s80 offen offset:0 sc1\n"
"v_mfma_f32_16x16x32_bf16 v[52:55], a[72:75], a[108:111], v[52:55]\n"
"ds_read_b64 a[14:15], v3 offset:1992\n"
"v_mfma_f32_16x16x32_bf16 v[52:55], a[68:71], a[112:115], v[52:55]\n"
"ds_read_b128 a[48:51], v28 offset:1984\n"
"v_mfma_f32_16x16x32_bf16 v[52:55], a[64:67], a[116:119], v[52:55]\n"
"ds_read_b64 a[16:17], v3 offset:2496\n"
"v_mfma_f32_16x16x32_bf16 v[56:59], a[64:67], a[120:123], v[56:59]\n"
"buffer_load_dwordx4 v[220:223], v17, s[44:47], s80 offen offset:1024 sc1\n"
"v_mfma_f32_16x16x32_bf16 v[56:59], a[68:71], a[116:119], v[56:59]\n"
"ds_read_b64 a[18:19], v3 offset:2504\n"
"s_bfe_u32 s80, s9, 0x50001\n"
"v_mfma_f32_16x16x32_bf16 v[56:59], a[72:75], a[112:115], v[56:59]\n"
"ds_read_b128 a[52:55], v28 offset:1472\n"
"s_lshl_b32 s80, s80, 17\n"
"v_mfma_f32_16x16x32_bf16 v[56:59], a[76:79], a[108:111], v[56:59]\n"
"ds_read_b64 a[20:21], v3 offset:3008\n"
"v_mfma_f32_16x16x32_bf16 v[56:59], a[80:83], a[104:107], v[56:59]\n"
"buffer_load_dwordx4 v[224:227], v16, s[44:47], s80 offen offset:0 sc1\n"
"v_mfma_f32_16x16x32_bf16 v[56:59], a[84:87], a[100:103], v[56:59]\n"
"ds_read_b64 a[22:23], v3 offset:3016\n"
"v_mfma_f32_16x16x32_bf16 v[56:59], a[88:91], a[96:99], v[56:59]\n"
"ds_read_b128 a[56:59], v28 offset:960\n"
"v_mfma_f32_16x16x32_bf16 v[60:63], a[92:95], a[96:99], v[60:63]\n"
"ds_read_b64 a[24:25], v3 offset:3520\n"
"v_mfma_f32_16x16x32_bf16 v[60:63], a[88:91], a[100:103], v[60:63]\n"
"buffer_load_dwordx4 v[228:231], v16, s[44:47], s80 offen offset:1024 sc1\n"
"v_mfma_f32_16x16x32_bf16 v[60:63], a[84:87], a[104:107], v[60:63]\n"
"ds_read_b64 a[26:27], v3 offset:3528\n"
"v_mfma_f32_16x16x32_bf16 v[60:63], a[80:83], a[108:111], v[60:63]\n"
"ds_read_b128 a[60:63], v28 offset:448\n"
"v_mfma_f32_16x16x32_bf16 v[60:63], a[76:79], a[112:115], v[60:63]\n"
"ds_read_b64 a[28:29], v3 offset:4032\n"
"v_mfma_f32_16x16x32_bf16 v[60:63], a[72:75], a[116:119], v[60:63]\n"
"buffer_load_dwordx4 v[232:235], v17, s[44:47], s80 offen offset:0 sc1\n"
"v_mfma_f32_16x16x32_bf16 v[60:63], a[68:71], a[120:123], v[60:63]\n"
"ds_read_b64 a[30:31], v3 offset:4040\n"
"v_mfma_f32_16x16x32_bf16 v[60:63], a[64:67], a[124:127], v[60:63]\n"
"s_waitcnt lgkmcnt(6)\n"
"v_mfma_f32_16x16x32_bf16 v[32:35], a[0:3], a[32:35], v[32:35]\n"
"ds_read_b64_tr_b16 a[64:65], v8 offset:0\n"
"v_mfma_f32_16x16x32_bf16 v[36:39], a[4:7], a[32:35], v[36:39]\n"
"buffer_load_dwordx4 v[236:239], v17, s[44:47], s80 offen offset:1024 sc1\n"
"v_mfma_f32_16x16x32_bf16 v[36:39], a[0:3], a[36:39], v[36:39]\n"
"ds_read_b64_tr_b16 a[66:67], v8 offset:4096\n"
"s_add_u32 s80, s80, 0x20000\n"
"v_mfma_f32_16x16x32_bf16 v[40:43], a[0:3], a[40:43], v[40:43]\n"
"ds_read_b64_tr_b16 a[68:69], v8 offset:8192\n"
"v_mfma_f32_16x16x32_bf16 v[40:43], a[4:7], a[36:39], v[40:43]\n"
"ds_read_b64_tr_b16 a[70:71], v8 offset:12288\n"
"v_mfma_f32_16x16x32_bf16 v[40:43], a[8:11], a[32:35], v[40:43]\n"
"buffer_load_dwordx4 v[240:243], v16, s[44:47], s80 offen offset:0 sc1\n"
"v_mfma_f32_16x16x32_bf16 v[44:47], a[12:15], a[32:35], v[44:47]\n"
"ds_read_b64_tr_b16 a[72:73], v9 offset:0\n"
"v_cvt_pk_bf16_f32 v32, v32, v33\n"
"v_mfma_f32_16x16x32_bf16 v[44:47], a[8:11], a[36:39], v[44:47]\n"
"ds_read_b64_tr_b16 a[74:75], v9 offset:4096\n"
"v_cvt_pk_bf16_f32 v33, v34, v35\n"
"v_mfma_f32_16x16x32_bf16 v[44:47], a[4:7], a[40:43], v[44:47]\n"
"v_cvt_pk_bf16_f32 v36, v36, v37\n"
"v_cvt_pk_bf16_f32 v37, v38, v39\n"
"v_mfma_f32_16x16x32_bf16 v[44:47], a[0:3], a[44:47], v[44:47]\n"
"buffer_load_dwordx4 v[244:247], v16, s[44:47], s80 offen offset:1024 sc1\n"
"v_mfma_f32_16x16x32_bf16 v[48:51], a[0:3], a[48:51], v[48:51]\n"
"ds_read_b64_tr_b16 a[76:77], v9 offset:8192\n"
"v_cvt_pk_bf16_f32 v40, v40, v41\n"
"v_mfma_f32_16x16x32_bf16 v[48:51], a[4:7], a[44:47], v[48:51]\n"
"ds_read_b64_tr_b16 a[78:79], v9 offset:12288\n"
"v_cvt_pk_bf16_f32 v41, v42, v43\n"
"v_mfma_f32_16x16x32_bf16 v[48:51], a[8:11], a[40:43], v[48:51]\n"
"buffer_load_dwordx4 v[248:251], v17, s[44:47], s80 offen offset:0 sc1\n"
"v_mfma_f32_16x16x32_bf16 v[48:51], a[12:15], a[36:39], v[48:51]\n"
"v_mfma_f32_16x16x32_bf16 v[48:51], a[16:19], a[32:35], v[48:51]\n"
"v_cvt_pk_bf16_f32 v44, v44, v45\n"
"s_waitcnt lgkmcnt(8)\n"
"s_barrier\n"
"v_mfma_f32_16x16x32_bf16 v[52:55], a[20:23], a[32:35], v[52:55]\n"
"v_cvt_pk_bf16_f32 v45, v46, v47\n"
"v_mfma_f32_16x16x32_bf16 v[52:55], a[16:19], a[36:39], v[52:55]\n"
"buffer_load_dwordx4 v[252:255], v17, s[44:47], s80 offen offset:1024 sc1\n"
"v_mfma_f32_16x16x32_bf16 v[52:55], a[12:15], a[40:43], v[52:55]\n"
"s_bfe_u32 s80, s9, 0x50009\n"
"s_lshl_b32 s80, s80, 17\n"
"v_mfma_f32_16x16x32_bf16 v[52:55], a[8:11], a[44:47], v[52:55]\n"
"v_mfma_f32_16x16x32_bf16 v[52:55], a[4:7], a[48:51], v[52:55]\n"
"v_cvt_pk_bf16_f32 v48, v48, v49\n"
"v_cvt_pk_bf16_f32 v49, v50, v51\n"
"v_mfma_f32_16x16x32_bf16 v[52:55], a[0:3], a[52:55], v[52:55]\n"
"buffer_load_dwordx4 a[136:139], v16, s[44:47], s80 offen offset:0 sc1\n"
"v_mfma_f32_16x16x32_bf16 v[56:59], a[0:3], a[56:59], v[56:59]\n"
"v_mfma_f32_16x16x32_bf16 v[56:59], a[4:7], a[52:55], v[56:59]\n"
"ds_write_b64 v20, v[32:33] offset:0\n"
"v_mfma_f32_16x16x32_bf16 v[56:59], a[8:11], a[48:51], v[56:59]\n"
"ds_write_b64 v20, v[36:37] offset:512\n"
"v_mfma_f32_16x16x32_bf16 v[56:59], a[12:15], a[44:47], v[56:59]\n"
"ds_write_b64 v20, v[40:41] offset:1024\n"
"v_mfma_f32_16x16x32_bf16 v[56:59], a[16:19], a[40:43], v[56:59]\n"
"buffer_load_dwordx4 a[140:143], v16, s[44:47], s80 offen offset:1024 sc1\n"
"v_mfma_f32_16x16x32_bf16 v[56:59], a[20:23], a[36:39], v[56:59]\n"
"v_cvt_pk_bf16_f32 v52, v52, v53\n"
"v_cvt_pk_bf16_f32 v53, v54, v55\n"
"v_mfma_f32_16x16x32_bf16 v[56:59], a[24:27], a[32:35], v[56:59]\n"
"ds_write_b64 v20, v[44:45] offset:1536\n"
"v_mfma_f32_16x16x32_bf16 v[60:63], a[28:31], a[32:35], v[60:63]\n"
"ds_write_b64 v20, v[48:49] offset:2048\n"
"v_mfma_f32_16x16x32_bf16 v[60:63], a[24:27], a[36:39], v[60:63]\n"
"buffer_load_dwordx4 a[144:147], v17, s[44:47], s80 offen offset:0 sc1\n"
"v_mfma_f32_16x16x32_bf16 v[60:63], a[20:23], a[40:43], v[60:63]\n"
"ds_write_b64 v20, v[52:53] offset:2560\n"
"v_mfma_f32_16x16x32_bf16 v[60:63], a[16:19], a[44:47], v[60:63]\n"
"v_mfma_f32_16x16x32_bf16 v[60:63], a[12:15], a[48:51], v[60:63]\n"
"buffer_load_dwordx4 a[148:151], v17, s[44:47], s80 offen offset:1024 sc1\n"
"v_mfma_f32_16x16x32_bf16 v[60:63], a[8:11], a[52:55], v[60:63]\n"
"s_add_u32 s80, s80, 0x20000\n"
"v_mfma_f32_16x16x32_bf16 v[60:63], a[4:7], a[56:59], v[60:63]\n"
"v_mfma_f32_16x16x32_bf16 v[60:63], a[0:3], a[60:63], v[60:63]\n"
"buffer_load_dwordx4 a[152:155], v16, s[44:47], s80 offen offset:0 sc1\n"
"s_waitcnt vmcnt(38) lgkmcnt(10)\n"
"v_mfma_f32_16x16x32_bf16 a[0:3], a[64:67], v[96:99], 0\n"
"ds_read_b64_tr_b16 a[80:81], v10 offset:0\n"
"s_add_u32 m0, s77, 0x8000\n"
"v_mfma_f32_16x16x32_bf16 a[0:3], a[68:71], v[112:115], a[0:3]\n"
"s_bfe_u32 s79, s8, 0x60010\n"
"s_lshl_b32 s79, s79, 15\n"
"v_mfma_f32_16x16x32_bf16 a[32:35], a[68:71], v[116:119], 0\n"
"ds_read_b64_tr_b16 a[82:83], v10 offset:4096\n"
"v_cvt_pk_bf16_f32 v56, v56, v57\n"
"v_mfma_f32_16x16x32_bf16 a[32:35], a[64:67], v[100:103], a[32:35]\n"
"buffer_load_dwordx4 v4, s[48:51], s79 offen offset:0 lds\n"
"v_mfma_f32_16x16x32_bf16 v[64:67], a[64:67], v[104:107], 0\n"
"ds_read_b64_tr_b16 a[84:85], v10 offset:8192\n"
"v_cvt_pk_bf16_f32 v57, v58, v59\n"
"v_mfma_f32_16x16x32_bf16 v[64:67], a[68:71], v[120:123], v[64:67]\n"
"v_mfma_f32_16x16x32_bf16 v[80:83], a[68:71], v[124:127], 0\n"
"ds_read_b64_tr_b16 a[86:87], v10 offset:12288\n"
"v_mfma_f32_16x16x32_bf16 v[80:83], a[64:67], v[108:111], v[80:83]\n"
"ds_write_b64 v20, v[56:57] offset:3072\n"
"s_waitcnt lgkmcnt(11)\n"
"v_mfma_f32_16x16x32_bf16 v[84:87], a[72:75], v[108:111], 0\n"
"ds_read_b64_tr_b16 a[88:89], v11 offset:0\n"
"v_cvt_pk_bf16_f32 v60, v60, v61\n"
"v_mfma_f32_16x16x32_bf16 v[84:87], a[76:79], v[124:127], v[84:87]\n"
"buffer_load_dwordx4 v5, s[48:51], s79 offen offset:1024 lds\n"
"v_mfma_f32_16x16x32_bf16 v[68:71], a[76:79], v[120:123], 0\n"
"ds_read_b64_tr_b16 a[90:91], v11 offset:4096\n"
"v_cvt_pk_bf16_f32 v61, v62, v63\n"
"v_mfma_f32_16x16x32_bf16 v[68:71], a[72:75], v[104:107], v[68:71]\n"
"v_mfma_f32_16x16x32_bf16 a[36:39], a[72:75], v[100:103], 0\n"
"ds_read_b64_tr_b16 a[92:93], v11 offset:8192\n"
"v_mfma_f32_16x16x32_bf16 a[36:39], a[76:79], v[116:119], a[36:39]\n"
"buffer_load_dwordx4 v6, s[48:51], s79 offen offset:2048 lds\n"
"v_mfma_f32_16x16x32_bf16 a[4:7], a[76:79], v[112:115], 0\n"
"ds_read_b64_tr_b16 a[94:95], v11 offset:12288\n"
"v_mfma_f32_16x16x32_bf16 a[4:7], a[72:75], v[96:99], a[4:7]\n"
"ds_write_b64 v20, v[60:61] offset:3584\n"
"s_waitcnt lgkmcnt(6)\n"
"v_mfma_f32_16x16x32_bf16 a[8:11], a[80:83], v[96:99], 0\n"
"ds_read_b64_tr_b16 a[96:97], v12 offset:0\n"
"v_mfma_f32_16x16x32_bf16 a[8:11], a[84:87], v[112:115], a[8:11]\n"
"buffer_load_dwordx4 v7, s[48:51], s79 offen offset:3072 lds\n"
"v_mfma_f32_16x16x32_bf16 a[40:43], a[84:87], v[116:119], 0\n"
"ds_read_b64_tr_b16 a[98:99], v12 offset:4096\n"
"s_add_u32 m0, s77, 0xa000\n"
"v_mfma_f32_16x16x32_bf16 a[40:43], a[80:83], v[100:103], a[40:43]\n"
"ds_read_b64_tr_b16 a[100:101], v12 offset:8192\n"
"s_bfe_u32 s79, s14, 0x60010\n"
"v_mfma_f32_16x16x32_bf16 v[72:75], a[80:83], v[104:107], 0\n"
"ds_read_b64_tr_b16 a[102:103], v12 offset:12288\n"
"s_lshl_b32 s79, s79, 15\n"
"v_mfma_f32_16x16x32_bf16 v[72:75], a[84:87], v[120:123], v[72:75]\n"
"buffer_load_dwordx4 v4, s[48:51], s79 offen offset:0 lds\n"
"v_mfma_f32_16x16x32_bf16 v[88:91], a[84:87], v[124:127], 0\n"
"ds_read_b64_tr_b16 a[64:65], v13 offset:0\n"
"v_mfma_f32_16x16x32_bf16 v[88:91], a[80:83], v[108:111], v[88:91]\n"
"ds_read_b64_tr_b16 a[66:67], v13 offset:4096\n"
"s_waitcnt lgkmcnt(7)\n"
"v_mfma_f32_16x16x32_bf16 v[92:95], a[88:91], v[108:111], 0\n"
"ds_read_b64_tr_b16 a[68:69], v13 offset:8192\n"
"v_mfma_f32_16x16x32_bf16 v[92:95], a[92:95], v[124:127], v[92:95]\n"
"buffer_load_dwordx4 v5, s[48:51], s79 offen offset:1024 lds\n"
"v_mfma_f32_16x16x32_bf16 v[76:79], a[92:95], v[120:123], 0\n"
"ds_read_b64_tr_b16 a[70:71], v13 offset:12288\n"
"v_mfma_f32_16x16x32_bf16 v[76:79], a[88:91], v[104:107], v[76:79]\n"
"ds_read_b64_tr_b16 a[72:73], v14 offset:0\n"
"v_mfma_f32_16x16x32_bf16 a[44:47], a[88:91], v[100:103], 0\n"
"ds_read_b64_tr_b16 a[74:75], v14 offset:4096\n"
"v_mfma_f32_16x16x32_bf16 a[44:47], a[92:95], v[116:119], a[44:47]\n"
"buffer_load_dwordx4 v6, s[48:51], s79 offen offset:2048 lds\n"
"v_mfma_f32_16x16x32_bf16 a[12:15], a[92:95], v[112:115], 0\n"
"ds_read_b64_tr_b16 a[76:77], v14 offset:8192\n"
"v_mfma_f32_16x16x32_bf16 a[12:15], a[88:91], v[96:99], a[12:15]\n"
"ds_read_b64_tr_b16 a[78:79], v14 offset:12288\n"
"s_waitcnt lgkmcnt(8)\n"
"v_mfma_f32_16x16x32_bf16 a[16:19], a[96:99], v[96:99], 0\n"
"ds_read_b64_tr_b16 a[80:81], v15 offset:0\n"
"v_mfma_f32_16x16x32_bf16 a[16:19], a[100:103], v[112:115], a[16:19]\n"
"buffer_load_dwordx4 v7, s[48:51], s79 offen offset:3072 lds\n"
"v_mfma_f32_16x16x32_bf16 a[48:51], a[100:103], v[116:119], 0\n"
"ds_read_b64_tr_b16 a[82:83], v15 offset:4096\n"
"v_mfma_f32_16x16x32_bf16 a[48:51], a[96:99], v[100:103], a[48:51]\n"
"ds_read_b64_tr_b16 a[84:85], v15 offset:8192\n"
"v_mfma_f32_16x16x32_bf16 v[32:35], a[96:99], v[104:107], 0\n"
"ds_read_b64_tr_b16 a[86:87], v15 offset:12288\n"
"v_mfma_f32_16x16x32_bf16 v[32:35], a[100:103], v[120:123], v[32:35]\n"
"buffer_load_dwordx4 a[156:159], v16, s[44:47], s80 offen offset:1024 sc1\n"
"v_mfma_f32_16x16x32_bf16 v[48:51], a[100:103], v[124:127], 0\n"
"v_mfma_f32_16x16x32_bf16 v[48:51], a[96:99], v[108:111], v[48:51]\n"
"s_waitcnt lgkmcnt(8)\n"
"v_mfma_f32_16x16x32_bf16 v[52:55], a[64:67], v[108:111], 0\n"
"buffer_load_dwordx4 a[160:163], v17, s[44:47], s80 offen offset:0 sc1\n"
"v_mfma_f32_16x16x32_bf16 v[52:55], a[68:71], v[124:127], v[52:55]\n"
"v_mfma_f32_16x16x32_bf16 v[36:39], a[68:71], v[120:123], 0\n"
"v_mfma_f32_16x16x32_bf16 v[36:39], a[64:67], v[104:107], v[36:39]\n"
"buffer_load_dwordx4 a[164:167], v17, s[44:47], s80 offen offset:1024 sc1\n"
"v_mfma_f32_16x16x32_bf16 a[52:55], a[64:67], v[100:103], 0\n"
"v_mfma_f32_16x16x32_bf16 a[52:55], a[68:71], v[116:119], a[52:55]\n"
"v_mfma_f32_16x16x32_bf16 a[20:23], a[68:71], v[112:115], 0\n"
"s_bfe_u32 s80, s9, 0x50011\n"
"s_lshl_b32 s80, s80, 17\n"
"v_mfma_f32_16x16x32_bf16 a[20:23], a[64:67], v[96:99], a[20:23]\n"
"s_waitcnt lgkmcnt(0)\n"
"s_barrier\n"
"v_mfma_f32_16x16x32_bf16 a[24:27], a[72:75], v[96:99], 0\n"
"ds_read_b64_tr_b16 a[88:89], v8 offset:16384\n"
"v_mfma_f32_16x16x32_bf16 a[24:27], a[76:79], v[112:115], a[24:27]\n"
"buffer_load_dwordx4 a[168:171], v16, s[44:47], s80 offen offset:0 sc1\n"
"v_mfma_f32_16x16x32_bf16 a[56:59], a[76:79], v[116:119], 0\n"
"ds_read_b64_tr_b16 a[90:91], v8 offset:20480\n"
"v_mfma_f32_16x16x32_bf16 a[56:59], a[72:75], v[100:103], a[56:59]\n"
"ds_read_b64_tr_b16 a[104:105], v21 offset:0\n"
"v_mfma_f32_16x16x32_bf16 v[40:43], a[72:75], v[104:107], 0\n"
"ds_read_b64_tr_b16 a[92:93], v8 offset:24576\n"
"v_mfma_f32_16x16x32_bf16 v[40:43], a[76:79], v[120:123], v[40:43]\n"
"buffer_load_dwordx4 a[172:175], v16, s[44:47], s80 offen offset:1024 sc1\n"
"v_mfma_f32_16x16x32_bf16 v[56:59], a[76:79], v[124:127], 0\n"
"ds_read_b64_tr_b16 a[94:95], v8 offset:28672\n"
"v_mfma_f32_16x16x32_bf16 v[56:59], a[72:75], v[108:111], v[56:59]\n"
"ds_read_b64_tr_b16 a[106:107], v21 offset:32\n"
"v_mfma_f32_16x16x32_bf16 v[60:63], a[80:83], v[108:111], 0\n"
"ds_read_b64_tr_b16 a[96:97], v9 offset:16384\n"
"v_mfma_f32_16x16x32_bf16 v[60:63], a[84:87], v[124:127], v[60:63]\n"
"buffer_load_dwordx4 a[176:179], v17, s[44:47], s80 offen offset:0 sc1\n"
"v_mfma_f32_16x16x32_bf16 v[44:47], a[84:87], v[120:123], 0\n"
"ds_read_b64_tr_b16 a[98:99], v9 offset:20480\n"
"v_mfma_f32_16x16x32_bf16 v[44:47], a[80:83], v[104:107], v[44:47]\n"
"ds_read_b64_tr_b16 a[108:109], v21 offset:64\n"
"v_mfma_f32_16x16x32_bf16 a[60:63], a[80:83], v[100:103], 0\n"
"ds_read_b64_tr_b16 a[100:101], v9 offset:24576\n"
"v_mfma_f32_16x16x32_bf16 a[60:63], a[84:87], v[116:119], a[60:63]\n"
"buffer_load_dwordx4 a[180:183], v17, s[44:47], s80 offen offset:1024 sc1\n"
"v_mfma_f32_16x16x32_bf16 a[28:31], a[84:87], v[112:115], 0\n"
"ds_read_b64_tr_b16 a[102:103], v9 offset:28672\n"
"s_add_u32 s80, s80, 0x20000\n"
"v_mfma_f32_16x16x32_bf16 a[28:31], a[80:83], v[96:99], a[28:31]\n"
"ds_read_b64_tr_b16 a[110:111], v21 offset:96\n"
"s_waitcnt vmcnt(45) lgkmcnt(7)\n"
"v_mfma_f32_16x16x32_bf16 a[0:3], a[88:91], v[128:131], a[0:3]\n"
"ds_read_b64_tr_b16 a[64:65], v10 offset:16384\n"
"v_mfma_f32_16x16x32_bf16 a[0:3], a[92:95], v[144:147], a[0:3]\n"
"buffer_load_dwordx4 a[184:187], v16, s[44:47], s80 offen offset:0 sc1\n"
"v_mfma_f32_16x16x32_bf16 a[32:35], a[92:95], v[148:151], a[32:35]\n"
"ds_read_b64_tr_b16 a[66:67], v10 offset:20480\n"
"v_mfma_f32_16x16x32_bf16 a[32:35], a[88:91], v[132:135], a[32:35]\n"
"ds_read_b64_tr_b16 a[112:113], v21 offset:512\n"
"v_mfma_f32_16x16x32_bf16 v[64:67], a[88:91], v[136:139], v[64:67]\n"
"ds_read_b64_tr_b16 a[68:69], v10 offset:24576\n"
"v_mfma_f32_16x16x32_bf16 v[64:67], a[92:95], v[152:155], v[64:67]\n"
"buffer_load_dwordx4 a[188:191], v16, s[44:47], s80 offen offset:1024 sc1\n"
"v_mfma_f32_16x16x32_bf16 v[80:83], a[92:95], v[156:159], v[80:83]\n"
"ds_read_b64_tr_b16 a[70:71], v10 offset:28672\n"
"v_mfma_f32_16x16x32_bf16 v[80:83], a[88:91], v[140:143], v[80:83]\n"
"ds_read_b64_tr_b16 a[114:115], v21 offset:544\n"
"s_waitcnt lgkmcnt(7)\n"
"v_mfma_f32_16x16x32_bf16 v[84:87], a[96:99], v[140:143], v[84:87]\n"
"ds_read_b64_tr_b16 a[72:73], v11 offset:16384\n"
"v_mfma_f32_16x16x32_bf16 v[84:87], a[100:103], v[156:159], v[84:87]\n"
"buffer_load_dwordx4 a[192:195], v17, s[44:47], s80 offen offset:0 sc1\n"
"v_mfma_f32_16x16x32_bf16 v[68:71], a[100:103], v[152:155], v[68:71]\n"
"ds_read_b64_tr_b16 a[74:75], v11 offset:20480\n"
"v_mfma_f32_16x16x32_bf16 v[68:71], a[96:99], v[136:139], v[68:71]\n"
"ds_read_b64_tr_b16 a[116:117], v21 offset:576\n"
"v_mfma_f32_16x16x32_bf16 a[36:39], a[96:99], v[132:135], a[36:39]\n"
"ds_read_b64_tr_b16 a[76:77], v11 offset:24576\n"
"v_mfma_f32_16x16x32_bf16 a[36:39], a[100:103], v[148:151], a[36:39]\n"
"buffer_load_dwordx4 a[196:199], v17, s[44:47], s80 offen offset:1024 sc1\n"
"v_mfma_f32_16x16x32_bf16 a[4:7], a[100:103], v[144:147], a[4:7]\n"
"ds_read_b64_tr_b16 a[78:79], v11 offset:28672\n"
"v_mfma_f32_16x16x32_bf16 a[4:7], a[96:99], v[128:131], a[4:7]\n"
"ds_read_b64_tr_b16 a[80:81], v12 offset:16384\n"
"s_waitcnt lgkmcnt(7)\n"
"v_mfma_f32_16x16x32_bf16 a[8:11], a[64:67], v[128:131], a[8:11]\n"
"ds_read_b64_tr_b16 a[82:83], v12 offset:20480\n"
"s_add_u32 m0, s77, 0x0\n"
"v_mfma_f32_16x16x32_bf16 a[8:11], a[68:71], v[144:147], a[8:11]\n"
"s_bfe_u32 s79, s8, 0x60018\n"
"s_lshl_b32 s79, s79, 15\n"
"v_mfma_f32_16x16x32_bf16 a[40:43], a[68:71], v[148:151], a[40:43]\n"
"ds_read_b64_tr_b16 a[84:85], v12 offset:24576\n"
"v_mfma_f32_16x16x32_bf16 a[40:43], a[64:67], v[132:135], a[40:43]\n"
"ds_read_b64_tr_b16 a[86:87], v12 offset:28672\n"
"v_mfma_f32_16x16x32_bf16 v[72:75], a[64:67], v[136:139], v[72:75]\n"
"ds_read_b64_tr_b16 a[88:89], v13 offset:16384\n"
"v_mfma_f32_16x16x32_bf16 v[72:75], a[68:71], v[152:155], v[72:75]\n"
"buffer_load_dwordx4 v4, s[48:51], s79 offen offset:0 lds\n"
"v_mfma_f32_16x16x32_bf16 v[88:91], a[68:71], v[156:159], v[88:91]\n"
"ds_read_b64_tr_b16 a[90:91], v13 offset:20480\n"
"v_mfma_f32_16x16x32_bf16 v[88:91], a[64:67], v[140:143], v[88:91]\n"
"ds_read_b64_tr_b16 a[92:93], v13 offset:24576\n"
"s_waitcnt lgkmcnt(7)\n"
"v_mfma_f32_16x16x32_bf16 v[92:95], a[72:75], v[140:143], v[92:95]\n"
"ds_read_b64_tr_b16 a[94:95], v13 offset:28672\n"
"v_mfma_f32_16x16x32_bf16 v[92:95], a[76:79], v[156:159], v[92:95]\n"
"buffer_load_dwordx4 v5, s[48:51], s79 offen offset:1024 lds\n"
"v_mfma_f32_16x16x32_bf16 v[76:79], a[76:79], v[152:155], v[76:79]\n"
"ds_read_b64_tr_b16 a[96:97], v14 offset:16384\n"
"v_mfma_f32_16x16x32_bf16 v[76:79], a[72:75], v[136:139], v[76:79]\n"
"ds_read_b64_tr_b16 a[98:99], v14 offset:20480\n"
"v_mfma_f32_16x16x32_bf16 a[44:47], a[72:75], v[132:135], a[44:47]\n"
"ds_read_b64_tr_b16 a[100:101], v14 offset:24576\n"
"v_mfma_f32_16x16x32_bf16 a[44:47], a[76:79], v[148:151], a[44:47]\n"
"buffer_load_dwordx4 v6, s[48:51], s79 offen offset:2048 lds\n"
"v_mfma_f32_16x16x32_bf16 a[12:15], a[76:79], v[144:147], a[12:15]\n"
"ds_read_b64_tr_b16 a[102:103], v14 offset:28672\n"
"v_mfma_f32_16x16x32_bf16 a[12:15], a[72:75], v[128:131], a[12:15]\n"
"ds_read_b64_tr_b16 a[64:65], v15 offset:16384\n"
"s_waitcnt lgkmcnt(9)\n"
"v_mfma_f32_16x16x32_bf16 a[16:19], a[80:83], v[128:131], a[16:19]\n"
"ds_read_b64_tr_b16 a[66:67], v15 offset:20480\n"
"v_mfma_f32_16x16x32_bf16 a[16:19], a[84:87], v[144:147], a[16:19]\n"
"buffer_load_dwordx4 v7, s[48:51], s79 offen offset:3072 lds\n"
"v_mfma_f32_16x16x32_bf16 a[48:51], a[84:87], v[148:151], a[48:51]\n"
"ds_read_b64_tr_b16 a[68:69], v15 offset:24576\n"
"s_add_u32 m0, s77, 0x2000\n"
"v_mfma_f32_16x16x32_bf16 a[48:51], a[80:83], v[132:135], a[48:51]\n"
"ds_read_b64_tr_b16 a[70:71], v15 offset:28672\n"
"s_bfe_u32 s79, s14, 0x60018\n"
"v_mfma_f32_16x16x32_bf16 v[32:35], a[80:83], v[136:139], v[32:35]\n"
"ds_read_b64_tr_b16 a[118:119], v21 offset:608\n"
"s_lshl_b32 s79, s79, 15\n"
"v_mfma_f32_16x16x32_bf16 v[32:35], a[84:87], v[152:155], v[32:35]\n"
"buffer_load_dwordx4 v4, s[48:51], s79 offen offset:0 lds\n"
"v_mfma_f32_16x16x32_bf16 v[48:51], a[84:87], v[156:159], v[48:51]\n"
"v_mfma_f32_16x16x32_bf16 v[48:51], a[80:83], v[140:143], v[48:51]\n"
"s_waitcnt lgkmcnt(9)\n"
"v_mfma_f32_16x16x32_bf16 v[52:55], a[88:91], v[140:143], v[52:55]\n"
"buffer_load_dwordx4 v5, s[48:51], s79 offen offset:1024 lds\n"
"v_mfma_f32_16x16x32_bf16 v[52:55], a[92:95], v[156:159], v[52:55]\n"
"v_mfma_f32_16x16x32_bf16 v[36:39], a[92:95], v[152:155], v[36:39]\n"
"v_mfma_f32_16x16x32_bf16 v[36:39], a[88:91], v[136:139], v[36:39]\n"
"buffer_load_dwordx4 v6, s[48:51], s79 offen offset:2048 lds\n"
"v_mfma_f32_16x16x32_bf16 a[52:55], a[88:91], v[132:135], a[52:55]\n"
"v_mfma_f32_16x16x32_bf16 a[52:55], a[92:95], v[148:151], a[52:55]\n"
"v_mfma_f32_16x16x32_bf16 a[20:23], a[92:95], v[144:147], a[20:23]\n"
"buffer_load_dwordx4 v7, s[48:51], s79 offen offset:3072 lds\n"
"v_mfma_f32_16x16x32_bf16 a[20:23], a[88:91], v[128:131], a[20:23]\n"
"s_waitcnt vmcnt(19) lgkmcnt(0)\n"
"s_barrier\n"
"v_mfma_f32_16x16x32_bf16 a[24:27], a[96:99], v[128:131], a[24:27]\n"
"ds_read_b64_tr_b16 a[72:73], v8 offset:32768\n"
"v_mfma_f32_16x16x32_bf16 a[24:27], a[100:103], v[144:147], a[24:27]\n"
"s_add_u32 m0, s77, 0x4000\n"
"s_bfe_u32 s79, s9, 0x60000\n"
"v_mfma_f32_16x16x32_bf16 a[56:59], a[100:103], v[148:151], a[56:59]\n"
"ds_read_b64_tr_b16 a[74:75], v8 offset:36864\n"
"s_lshl_b32 s79, s79, 15\n"
"v_mfma_f32_16x16x32_bf16 a[56:59], a[96:99], v[132:135], a[56:59]\n"
"buffer_load_dwordx4 v4, s[48:51], s79 offen offset:0 lds\n"
"v_mfma_f32_16x16x32_bf16 v[40:43], a[96:99], v[136:139], v[40:43]\n"
"ds_read_b64_tr_b16 a[76:77], v8 offset:40960\n"
"v_mfma_f32_16x16x32_bf16 v[40:43], a[100:103], v[152:155], v[40:43]\n"
"v_mfma_f32_16x16x32_bf16 v[56:59], a[100:103], v[156:159], v[56:59]\n"
"ds_read_b64_tr_b16 a[78:79], v8 offset:45056\n"
"v_mfma_f32_16x16x32_bf16 v[56:59], a[96:99], v[140:143], v[56:59]\n"
"v_mfma_f32_16x16x32_bf16 v[60:63], a[64:67], v[140:143], v[60:63]\n"
"ds_read_b64_tr_b16 a[80:81], v9 offset:32768\n"
"v_mfma_f32_16x16x32_bf16 v[60:63], a[68:71], v[156:159], v[60:63]\n"
"buffer_load_dwordx4 v5, s[48:51], s79 offen offset:1024 lds\n"
"v_mfma_f32_16x16x32_bf16 v[44:47], a[68:71], v[152:155], v[44:47]\n"
"ds_read_b64_tr_b16 a[82:83], v9 offset:36864\n"
"v_mfma_f32_16x16x32_bf16 v[44:47], a[64:67], v[136:139], v[44:47]\n"
"v_mfma_f32_16x16x32_bf16 a[60:63], a[64:67], v[132:135], a[60:63]\n"
"ds_read_b64_tr_b16 a[84:85], v9 offset:40960\n"
"v_mfma_f32_16x16x32_bf16 a[60:63], a[68:71], v[148:151], a[60:63]\n"
"buffer_load_dwordx4 v6, s[48:51], s79 offen offset:2048 lds\n"
"v_mfma_f32_16x16x32_bf16 a[28:31], a[68:71], v[144:147], a[28:31]\n"
"ds_read_b64_tr_b16 a[86:87], v9 offset:45056\n"
"v_mfma_f32_16x16x32_bf16 a[28:31], a[64:67], v[128:131], a[28:31]\n"
"s_waitcnt lgkmcnt(4)\n"
"v_mfma_f32_16x16x32_bf16 a[0:3], a[72:75], v[160:163], a[0:3]\n"
"ds_read_b64_tr_b16 a[88:89], v10 offset:32768\n"
"v_mfma_f32_16x16x32_bf16 a[0:3], a[76:79], v[176:179], a[0:3]\n"
"buffer_load_dwordx4 v7, s[48:51], s79 offen offset:3072 lds\n"
"v_mfma_f32_16x16x32_bf16 a[32:35], a[76:79], v[180:183], a[32:35]\n"
"ds_read_b64_tr_b16 a[90:91], v10 offset:36864\n"
"s_add_u32 m0, s77, 0x6000\n"
"v_mfma_f32_16x16x32_bf16 a[32:35], a[72:75], v[164:167], a[32:35]\n"
"s_bfe_u32 s79, s15, 0x60000\n"
"s_lshl_b32 s79, s79, 15\n"
"v_mfma_f32_16x16x32_bf16 v[64:67], a[72:75], v[168:171], v[64:67]\n"
"ds_read_b64_tr_b16 a[92:93], v10 offset:40960\n"
"v_mfma_f32_16x16x32_bf16 v[64:67], a[76:79], v[184:187], v[64:67]\n"
"buffer_load_dwordx4 v4, s[48:51], s79 offen offset:0 lds\n"
"v_mfma_f32_16x16x32_bf16 v[80:83], a[76:79], v[188:191], v[80:83]\n"
"ds_read_b64_tr_b16 a[94:95], v10 offset:45056\n"
"v_mfma_f32_16x16x32_bf16 v[80:83], a[72:75], v[172:175], v[80:83]\n"
"s_waitcnt lgkmcnt(4)\n"
"v_mfma_f32_16x16x32_bf16 v[84:87], a[80:83], v[172:175], v[84:87]\n"
"ds_read_b64_tr_b16 a[96:97], v11 offset:32768\n"
"v_mfma_f32_16x16x32_bf16 v[84:87], a[84:87], v[188:191], v[84:87]\n"
"buffer_load_dwordx4 v5, s[48:51], s79 offen offset:1024 lds\n"
"v_mfma_f32_16x16x32_bf16 v[68:71], a[84:87], v[184:187], v[68:71]\n"
"ds_read_b64_tr_b16 a[98:99], v11 offset:36864\n"
"v_mfma_f32_16x16x32_bf16 v[68:71], a[80:83], v[168:171], v[68:71]\n"
"v_mfma_f32_16x16x32_bf16 a[36:39], a[80:83], v[164:167], a[36:39]\n"
"ds_read_b64_tr_b16 a[100:101], v11 offset:40960\n"
"v_mfma_f32_16x16x32_bf16 a[36:39], a[84:87], v[180:183], a[36:39]\n"
"buffer_load_dwordx4 v6, s[48:51], s79 offen offset:2048 lds\n"
"v_mfma_f32_16x16x32_bf16 a[4:7], a[84:87], v[176:179], a[4:7]\n"
"ds_read_b64_tr_b16 a[102:103], v11 offset:45056\n"
"v_mfma_f32_16x16x32_bf16 a[4:7], a[80:83], v[160:163], a[4:7]\n"
"ds_read_b64_tr_b16 a[64:65], v12 offset:32768\n"
"s_waitcnt lgkmcnt(5)\n"
"v_mfma_f32_16x16x32_bf16 a[8:11], a[88:91], v[160:163], a[8:11]\n"
"ds_read_b64_tr_b16 a[66:67], v12 offset:36864\n"
"v_mfma_f32_16x16x32_bf16 a[8:11], a[92:95], v[176:179], a[8:11]\n"
"buffer_load_dwordx4 v7, s[48:51], s79 offen offset:3072 lds\n"
"v_mfma_f32_16x16x32_bf16 a[40:43], a[92:95], v[180:183], a[40:43]\n"
"ds_read_b64_tr_b16 a[68:69], v12 offset:40960\n"
"v_mfma_f32_16x16x32_bf16 a[40:43], a[88:91], v[164:167], a[40:43]\n"
"ds_read_b64_tr_b16 a[70:71], v12 offset:45056\n"
"v_mfma_f32_16x16x32_bf16 v[72:75], a[88:91], v[168:171], v[72:75]\n"
"ds_read_b64_tr_b16 a[72:73], v13 offset:32768\n"
"v_mfma_f32_16x16x32_bf16 v[72:75], a[92:95], v[184:187], v[72:75]\n"
"buffer_store_dwordx2 a[104:105], v2, s[64:67], s16 offen offset:0 sc1\n"
"v_mfma_f32_16x16x32_bf16 v[88:91], a[92:95], v[188:191], v[88:91]\n"
"ds_read_b64_tr_b16 a[74:75], v13 offset:36864\n"
"v_mfma_f32_16x16x32_bf16 v[88:91], a[88:91], v[172:175], v[88:91]\n"
"ds_read_b64_tr_b16 a[76:77], v13 offset:40960\n"
"s_waitcnt lgkmcnt(7)\n"
"v_mfma_f32_16x16x32_bf16 v[92:95], a[96:99], v[172:175], v[92:95]\n"
"ds_read_b64_tr_b16 a[78:79], v13 offset:45056\n"
"v_mfma_f32_16x16x32_bf16 v[92:95], a[100:103], v[188:191], v[92:95]\n"
"buffer_store_dwordx2 a[106:107], v2, s[64:67], s16 offen offset:512 sc1\n"
"v_mfma_f32_16x16x32_bf16 v[76:79], a[100:103], v[184:187], v[76:79]\n"
"ds_read_b64_tr_b16 a[80:81], v14 offset:32768\n"
"v_mfma_f32_16x16x32_bf16 v[76:79], a[96:99], v[168:171], v[76:79]\n"
"ds_read_b64_tr_b16 a[82:83], v14 offset:36864\n"
"v_mfma_f32_16x16x32_bf16 a[44:47], a[96:99], v[164:167], a[44:47]\n"
"ds_read_b64_tr_b16 a[84:85], v14 offset:40960\n"
"v_mfma_f32_16x16x32_bf16 a[44:47], a[100:103], v[180:183], a[44:47]\n"
"buffer_store_dwordx2 a[108:109], v2, s[64:67], s16 offen offset:1024 sc1\n"
"v_mfma_f32_16x16x32_bf16 a[12:15], a[100:103], v[176:179], a[12:15]\n"
"ds_read_b64_tr_b16 a[86:87], v14 offset:45056\n"
"v_mfma_f32_16x16x32_bf16 a[12:15], a[96:99], v[160:163], a[12:15]\n"
"ds_read_b64_tr_b16 a[88:89], v15 offset:32768\n"
"s_waitcnt lgkmcnt(9)\n"
"v_mfma_f32_16x16x32_bf16 a[16:19], a[64:67], v[160:163], a[16:19]\n"
"ds_read_b64_tr_b16 a[90:91], v15 offset:36864\n"
"v_mfma_f32_16x16x32_bf16 a[16:19], a[68:71], v[176:179], a[16:19]\n"
"buffer_store_dwordx2 a[110:111], v2, s[64:67], s16 offen offset:1536 sc1\n"
"v_mfma_f32_16x16x32_bf16 a[48:51], a[68:71], v[180:183], a[48:51]\n"
"ds_read_b64_tr_b16 a[92:93], v15 offset:40960\n"
"v_mfma_f32_16x16x32_bf16 a[48:51], a[64:67], v[164:167], a[48:51]\n"
"ds_read_b64_tr_b16 a[94:95], v15 offset:45056\n"
"v_mfma_f32_16x16x32_bf16 v[32:35], a[64:67], v[168:171], v[32:35]\n"
"buffer_store_dwordx2 a[112:113], v2, s[64:67], s16 offen offset:2048 sc1\n"
"v_mfma_f32_16x16x32_bf16 v[32:35], a[68:71], v[184:187], v[32:35]\n"
"buffer_store_dwordx2 a[114:115], v2, s[64:67], s16 offen offset:2560 sc1\n"
"v_mfma_f32_16x16x32_bf16 v[48:51], a[68:71], v[188:191], v[48:51]\n"
"buffer_store_dwordx2 a[116:117], v2, s[64:67], s16 offen offset:3072 sc1\n"
"v_mfma_f32_16x16x32_bf16 v[48:51], a[64:67], v[172:175], v[48:51]\n"
"s_waitcnt lgkmcnt(8)\n"
"v_mfma_f32_16x16x32_bf16 v[52:55], a[72:75], v[172:175], v[52:55]\n"
"buffer_store_dwordx2 a[118:119], v2, s[64:67], s16 offen offset:3584 sc1\n"
"v_mfma_f32_16x16x32_bf16 v[52:55], a[76:79], v[188:191], v[52:55]\n"
"v_mfma_f32_16x16x32_bf16 v[36:39], a[76:79], v[184:187], v[36:39]\n"
"v_mfma_f32_16x16x32_bf16 v[36:39], a[72:75], v[168:171], v[36:39]\n"
"s_bfe_u32 s80, s9, 0x50019\n"
"s_lshl_b32 s80, s80, 17\n"
"v_mfma_f32_16x16x32_bf16 a[52:55], a[72:75], v[164:167], a[52:55]\n"
"buffer_load_dwordx4 a[200:203], v16, s[44:47], s80 offen offset:0 sc1\n"
"v_mfma_f32_16x16x32_bf16 a[52:55], a[76:79], v[180:183], a[52:55]\n"
".Lfk_hopw3:\n"
".Lfk_hop6341:\n"
"s_cbranch_execz .Lfk_hop6890\n"
"v_mfma_f32_16x16x32_bf16 a[20:23], a[76:79], v[176:179], a[20:23]\n"
"v_mfma_f32_16x16x32_bf16 a[20:23], a[72:75], v[160:163], a[20:23]\n"
"s_waitcnt vmcnt(17) lgkmcnt(0)\n"
"s_barrier\n"
"v_mfma_f32_16x16x32_bf16 a[24:27], a[80:83], v[160:163], a[24:27]\n"
"ds_read_b64_tr_b16 a[96:97], v8 offset:0\n"
"v_mfma_f32_16x16x32_bf16 a[24:27], a[84:87], v[176:179], a[24:27]\n"
"buffer_load_dwordx4 a[204:207], v16, s[44:47], s80 offen offset:1024 sc1\n"
"v_mfma_f32_16x16x32_bf16 a[56:59], a[84:87], v[180:183], a[56:59]\n"
"ds_read_b64_tr_b16 a[98:99], v8 offset:4096\n"
"v_mfma_f32_16x16x32_bf16 a[56:59], a[80:83], v[164:167], a[56:59]\n"
"v_mfma_f32_16x16x32_bf16 v[40:43], a[80:83], v[168:171], v[40:43]\n"
"ds_read_b64_tr_b16 a[100:101], v8 offset:8192\n"
"v_mfma_f32_16x16x32_bf16 v[40:43], a[84:87], v[184:187], v[40:43]\n"
"buffer_load_dwordx4 a[208:211], v17, s[44:47], s80 offen offset:0 sc1\n"
"v_mfma_f32_16x16x32_bf16 v[56:59], a[84:87], v[188:191], v[56:59]\n"
"ds_read_b64_tr_b16 a[102:103], v8 offset:12288\n"
"v_mfma_f32_16x16x32_bf16 v[56:59], a[80:83], v[172:175], v[56:59]\n"
"v_mfma_f32_16x16x32_bf16 v[60:63], a[88:91], v[172:175], v[60:63]\n"
"ds_read_b64_tr_b16 a[64:65], v9 offset:0\n"
"v_mfma_f32_16x16x32_bf16 v[60:63], a[92:95], v[188:191], v[60:63]\n"
"buffer_load_dwordx4 a[212:215], v17, s[44:47], s80 offen offset:1024 sc1\n"
"v_mfma_f32_16x16x32_bf16 v[44:47], a[92:95], v[184:187], v[44:47]\n"
"ds_read_b64_tr_b16 a[66:67], v9 offset:4096\n"
"s_add_u32 s80, s80, 0x20000\n"
"v_mfma_f32_16x16x32_bf16 v[44:47], a[88:91], v[168:171], v[44:47]\n"
"v_mfma_f32_16x16x32_bf16 a[60:63], a[88:91], v[164:167], a[60:63]\n"
"ds_read_b64_tr_b16 a[68:69], v9 offset:8192\n"
"v_mfma_f32_16x16x32_bf16 a[60:63], a[92:95], v[180:183], a[60:63]\n"
"buffer_load_dwordx4 a[216:219], v16, s[44:47], s80 offen offset:0 sc1\n"
"v_mfma_f32_16x16x32_bf16 a[28:31], a[92:95], v[176:179], a[28:31]\n"
"ds_read_b64_tr_b16 a[70:71], v9 offset:12288\n"
"v_mfma_f32_16x16x32_bf16 a[28:31], a[88:91], v[160:163], a[28:31]\n"
"s_waitcnt lgkmcnt(4)\n"
"v_mfma_f32_16x16x32_bf16 a[0:3], a[96:99], v[192:195], a[0:3]\n"
"ds_read_b64_tr_b16 a[72:73], v10 offset:0\n"
"v_mfma_f32_16x16x32_bf16 a[0:3], a[100:103], v[208:211], a[0:3]\n"
"buffer_load_dwordx4 a[220:223], v16, s[44:47], s80 offen offset:1024 sc1\n"
"v_mfma_f32_16x16x32_bf16 a[32:35], a[100:103], v[212:215], a[32:35]\n"
"ds_read_b64_tr_b16 a[74:75], v10 offset:4096\n"
"v_mfma_f32_16x16x32_bf16 a[32:35], a[96:99], v[196:199], a[32:35]\n"
"v_mfma_f32_16x16x32_bf16 v[64:67], a[96:99], v[200:203], v[64:67]\n"
"ds_read_b64_tr_b16 a[76:77], v10 offset:8192\n"
"v_mfma_f32_16x16x32_bf16 v[64:67], a[100:103], v[216:219], v[64:67]\n"
"buffer_load_dwordx4 a[224:227], v17, s[44:47], s80 offen offset:0 sc1\n"
"v_mfma_f32_16x16x32_bf16 v[80:83], a[100:103], v[220:223], v[80:83]\n"
"ds_read_b64_tr_b16 a[78:79], v10 offset:12288\n"
"v_mfma_f32_16x16x32_bf16 v[80:83], a[96:99], v[204:207], v[80:83]\n"
"s_waitcnt lgkmcnt(4)\n"
"v_mfma_f32_16x16x32_bf16 v[84:87], a[64:67], v[204:207], v[84:87]\n"
"ds_read_b64_tr_b16 a[80:81], v11 offset:0\n"
"v_mfma_f32_16x16x32_bf16 v[84:87], a[68:71], v[220:223], v[84:87]\n"
"buffer_load_dwordx4 a[228:231], v17, s[44:47], s80 offen offset:1024 sc1\n"
"v_mfma_f32_16x16x32_bf16 v[68:71], a[68:71], v[216:219], v[68:71]\n"
"ds_read_b64_tr_b16 a[82:83], v11 offset:4096\n"
"v_mfma_f32_16x16x32_bf16 v[68:71], a[64:67], v[200:203], v[68:71]\n"
"v_mfma_f32_16x16x32_bf16 a[36:39], a[64:67], v[196:199], a[36:39]\n"
"ds_read_b64_tr_b16 a[84:85], v11 offset:8192\n"
"s_add_u32 m0, s77, 0x8000\n"
"v_mfma_f32_16x16x32_bf16 a[36:39], a[68:71], v[212:215], a[36:39]\n"
"s_bfe_u32 s79, s9, 0x60008\n"
"s_lshl_b32 s79, s79, 15\n"
"v_mfma_f32_16x16x32_bf16 a[4:7], a[68:71], v[208:211], a[4:7]\n"
"ds_read_b64_tr_b16 a[86:87], v11 offset:12288\n"
"v_mfma_f32_16x16x32_bf16 a[4:7], a[64:67], v[192:195], a[4:7]\n"
"ds_read_b64_tr_b16 a[88:89], v12 offset:0\n"
"s_waitcnt lgkmcnt(5)\n"
"v_mfma_f32_16x16x32_bf16 a[8:11], a[72:75], v[192:195], a[8:11]\n"
"ds_read_b64_tr_b16 a[90:91], v12 offset:4096\n"
"v_mfma_f32_16x16x32_bf16 a[8:11], a[76:79], v[208:211], a[8:11]\n"
"buffer_load_dwordx4 v4, s[48:51], s79 offen offset:0 lds\n"
"v_mfma_f32_16x16x32_bf16 a[40:43], a[76:79], v[212:215], a[40:43]\n"
"ds_read_b64_tr_b16 a[92:93], v12 offset:8192\n"
"v_mfma_f32_16x16x32_bf16 a[40:43], a[72:75], v[196:199], a[40:43]\n"
"ds_read_b64_tr_b16 a[94:95], v12 offset:12288\n"
"v_mfma_f32_16x16x32_bf16 v[72:75], a[72:75], v[200:203], v[72:75]\n"
"ds_read_b64_tr_b16 a[96:97], v13 offset:0\n"
"v_mfma_f32_16x16x32_bf16 v[72:75], a[76:79], v[216:219], v[72:75]\n"
"buffer_load_dwordx4 v5, s[48:51], s79 offen offset:1024 lds\n"
"v_mfma_f32_16x16x32_bf16 v[88:91], a[76:79], v[220:223], v[88:91]\n"
"ds_read_b64_tr_b16 a[98:99], v13 offset:4096\n"
"v_mfma_f32_16x16x32_bf16 v[88:91], a[72:75], v[204:207], v[88:91]\n"
"ds_read_b64_tr_b16 a[100:101], v13 offset:8192\n"
"s_waitcnt lgkmcnt(7)\n"
"v_mfma_f32_16x16x32_bf16 v[92:95], a[80:83], v[204:207], v[92:95]\n"
"ds_read_b64_tr_b16 a[102:103], v13 offset:12288\n"
"v_mfma_f32_16x16x32_bf16 v[92:95], a[84:87], v[220:223], v[92:95]\n"
"buffer_load_dwordx4 v6, s[48:51], s79 offen offset:2048 lds\n"
"v_mfma_f32_16x16x32_bf16 v[76:79], a[84:87], v[216:219], v[76:79]\n"
"ds_read_b64_tr_b16 a[64:65], v14 offset:0\n"
"v_mfma_f32_16x16x32_bf16 v[76:79], a[80:83], v[200:203], v[76:79]\n"
"ds_read_b64_tr_b16 a[66:67], v14 offset:4096\n"
"v_mfma_f32_16x16x32_bf16 a[44:47], a[80:83], v[196:199], a[44:47]\n"
"ds_read_b64_tr_b16 a[68:69], v14 offset:8192\n"
"v_mfma_f32_16x16x32_bf16 a[44:47], a[84:87], v[212:215], a[44:47]\n"
"buffer_load_dwordx4 v7, s[48:51], s79 offen offset:3072 lds\n"
"v_mfma_f32_16x16x32_bf16 a[12:15], a[84:87], v[208:211], a[12:15]\n"
"ds_read_b64_tr_b16 a[70:71], v14 offset:12288\n"
"s_add_u32 m0, s77, 0xa000\n"
"v_mfma_f32_16x16x32_bf16 a[12:15], a[80:83], v[192:195], a[12:15]\n"
"ds_read_b64_tr_b16 a[72:73], v15 offset:0\n"
"s_waitcnt lgkmcnt(9)\n"
"v_mfma_f32_16x16x32_bf16 a[16:19], a[88:91], v[192:195], a[16:19]\n"
"ds_read_b64_tr_b16 a[74:75], v15 offset:4096\n"
"s_bfe_u32 s79, s15, 0x60008\n"
"v_mfma_f32_16x16x32_bf16 a[16:19], a[92:95], v[208:211], a[16:19]\n"
"s_lshl_b32 s79, s79, 15\n"
"v_mfma_f32_16x16x32_bf16 a[48:51], a[92:95], v[212:215], a[48:51]\n"
"ds_read_b64_tr_b16 a[76:77], v15 offset:8192\n"
"v_mfma_f32_16x16x32_bf16 a[48:51], a[88:91], v[196:199], a[48:51]\n"
"ds_read_b64_tr_b16 a[78:79], v15 offset:12288\n"
"v_mfma_f32_16x16x32_bf16 v[32:35], a[88:91], v[200:203], v[32:35]\n"
"buffer_load_dwordx4 v4, s[48:51], s79 offen offset:0 lds\n"
"v_mfma_f32_16x16x32_bf16 v[32:35], a[92:95], v[216:219], v[32:35]\n"
"v_mfma_f32_16x16x32_bf16 v[48:51], a[92:95], v[220:223], v[48:51]\n"
"v_mfma_f32_16x16x32_bf16 v[48:51], a[88:91], v[204:207], v[48:51]\n"
"s_waitcnt lgkmcnt(8)\n"
"v_mfma_f32_16x16x32_bf16 v[52:55], a[96:99], v[204:207], v[52:55]\n"
"buffer_load_dwordx4 v5, s[48:51], s79 offen offset:1024 lds\n"
"v_mfma_f32_16x16x32_bf16 v[52:55], a[100:103], v[220:223], v[52:55]\n"
"v_mfma_f32_16x16x32_bf16 v[36:39], a[100:103], v[216:219], v[36:39]\n"
"v_mfma_f32_16x16x32_bf16 v[36:39], a[96:99], v[200:203], v[36:39]\n"
"buffer_load_dwordx4 v6, s[48:51], s79 offen offset:2048 lds\n"
"v_mfma_f32_16x16x32_bf16 a[52:55], a[96:99], v[196:199], a[52:55]\n"
"v_mfma_f32_16x16x32_bf16 a[52:55], a[100:103], v[212:215], a[52:55]\n"
"v_mfma_f32_16x16x32_bf16 a[20:23], a[100:103], v[208:211], a[20:23]\n"
"buffer_load_dwordx4 v7, s[48:51], s79 offen offset:3072 lds\n"
"v_mfma_f32_16x16x32_bf16 a[20:23], a[96:99], v[192:195], a[20:23]\n"
"s_waitcnt vmcnt(24) lgkmcnt(0)\n"
"s_barrier\n"
"v_mfma_f32_16x16x32_bf16 a[24:27], a[64:67], v[192:195], a[24:27]\n"
"ds_read_b64_tr_b16 a[80:81], v8 offset:16384\n"
"v_mfma_f32_16x16x32_bf16 a[24:27], a[68:71], v[208:211], a[24:27]\n"
"s_add_u32 m0, s77, 0x0\n"
"s_bfe_u32 s79, s9, 0x60010\n"
"v_mfma_f32_16x16x32_bf16 a[56:59], a[68:71], v[212:215], a[56:59]\n"
"ds_read_b64_tr_b16 a[82:83], v8 offset:20480\n"
"s_lshl_b32 s79, s79, 15\n"
"v_mfma_f32_16x16x32_bf16 a[56:59], a[64:67], v[196:199], a[56:59]\n"
"buffer_load_dwordx4 v4, s[48:51], s79 offen offset:0 lds\n"
"v_mfma_f32_16x16x32_bf16 v[40:43], a[64:67], v[200:203], v[40:43]\n"
"ds_read_b64_tr_b16 a[84:85], v8 offset:24576\n"
"v_mfma_f32_16x16x32_bf16 v[40:43], a[68:71], v[216:219], v[40:43]\n"
"v_mfma_f32_16x16x32_bf16 v[56:59], a[68:71], v[220:223], v[56:59]\n"
"ds_read_b64_tr_b16 a[86:87], v8 offset:28672\n"
"v_mfma_f32_16x16x32_bf16 v[56:59], a[64:67], v[204:207], v[56:59]\n"
"v_mfma_f32_16x16x32_bf16 v[60:63], a[72:75], v[204:207], v[60:63]\n"
"ds_read_b64_tr_b16 a[88:89], v9 offset:16384\n"
"v_mfma_f32_16x16x32_bf16 v[60:63], a[76:79], v[220:223], v[60:63]\n"
"buffer_load_dwordx4 v5, s[48:51], s79 offen offset:1024 lds\n"
"v_mfma_f32_16x16x32_bf16 v[44:47], a[76:79], v[216:219], v[44:47]\n"
"ds_read_b64_tr_b16 a[90:91], v9 offset:20480\n"
"v_mfma_f32_16x16x32_bf16 v[44:47], a[72:75], v[200:203], v[44:47]\n"
"v_mfma_f32_16x16x32_bf16 a[60:63], a[72:75], v[196:199], a[60:63]\n"
"ds_read_b64_tr_b16 a[92:93], v9 offset:24576\n"
"v_mfma_f32_16x16x32_bf16 a[60:63], a[76:79], v[212:215], a[60:63]\n"
"buffer_load_dwordx4 v6, s[48:51], s79 offen offset:2048 lds\n"
"v_mfma_f32_16x16x32_bf16 a[28:31], a[76:79], v[208:211], a[28:31]\n"
"ds_read_b64_tr_b16 a[94:95], v9 offset:28672\n"
"v_mfma_f32_16x16x32_bf16 a[28:31], a[72:75], v[192:195], a[28:31]\n"
"s_waitcnt lgkmcnt(4)\n"
"v_mfma_f32_16x16x32_bf16 a[0:3], a[80:83], v[224:227], a[0:3]\n"
"ds_read_b64_tr_b16 a[96:97], v10 offset:16384\n"
"v_mfma_f32_16x16x32_bf16 a[0:3], a[84:87], v[240:243], a[0:3]\n"
"buffer_load_dwordx4 v7, s[48:51], s79 offen offset:3072 lds\n"
"v_mfma_f32_16x16x32_bf16 a[32:35], a[84:87], v[244:247], a[32:35]\n"
"ds_read_b64_tr_b16 a[98:99], v10 offset:20480\n"
"s_add_u32 m0, s77, 0x2000\n"
"v_mfma_f32_16x16x32_bf16 a[32:35], a[80:83], v[228:231], a[32:35]\n"
"s_bfe_u32 s79, s15, 0x60010\n"
"s_lshl_b32 s79, s79, 15\n"
"v_mfma_f32_16x16x32_bf16 v[64:67], a[80:83], v[232:235], v[64:67]\n"
"ds_read_b64_tr_b16 a[100:101], v10 offset:24576\n"
"v_mfma_f32_16x16x32_bf16 v[64:67], a[84:87], v[248:251], v[64:67]\n"
"buffer_load_dwordx4 v4, s[48:51], s79 offen offset:0 lds\n"
"v_mfma_f32_16x16x32_bf16 v[80:83], a[84:87], v[252:255], v[80:83]\n"
"ds_read_b64_tr_b16 a[102:103], v10 offset:28672\n"
"v_mfma_f32_16x16x32_bf16 v[80:83], a[80:83], v[236:239], v[80:83]\n"
"s_waitcnt lgkmcnt(4)\n"
"v_mfma_f32_16x16x32_bf16 v[84:87], a[88:91], v[236:239], v[84:87]\n"
"ds_read_b64_tr_b16 a[64:65], v11 offset:16384\n"
"v_mfma_f32_16x16x32_bf16 v[84:87], a[92:95], v[252:255], v[84:87]\n"
"buffer_load_dwordx4 v5, s[48:51], s79 offen offset:1024 lds\n"
"v_mfma_f32_16x16x32_bf16 v[68:71], a[92:95], v[248:251], v[68:71]\n"
"ds_read_b64_tr_b16 a[66:67], v11 offset:20480\n"
"v_mfma_f32_16x16x32_bf16 v[68:71], a[88:91], v[232:235], v[68:71]\n"
"v_mfma_f32_16x16x32_bf16 a[36:39], a[88:91], v[228:231], a[36:39]\n"
"ds_read_b64_tr_b16 a[68:69], v11 offset:24576\n"
"v_mfma_f32_16x16x32_bf16 a[36:39], a[92:95], v[244:247], a[36:39]\n"
"buffer_load_dwordx4 v6, s[48:51], s79 offen offset:2048 lds\n"
"v_mfma_f32_16x16x32_bf16 a[4:7], a[92:95], v[240:243], a[4:7]\n"
"ds_read_b64_tr_b16 a[70:71], v11 offset:28672\n"
"v_mfma_f32_16x16x32_bf16 a[4:7], a[88:91], v[224:227], a[4:7]\n"
"ds_read_b64_tr_b16 a[72:73], v12 offset:16384\n"
"s_waitcnt lgkmcnt(5)\n"
"v_mfma_f32_16x16x32_bf16 a[8:11], a[96:99], v[224:227], a[8:11]\n"
"ds_read_b64_tr_b16 a[74:75], v12 offset:20480\n"
"v_mfma_f32_16x16x32_bf16 a[8:11], a[100:103], v[240:243], a[8:11]\n"
"buffer_load_dwordx4 v7, s[48:51], s79 offen offset:3072 lds\n"
"v_mfma_f32_16x16x32_bf16 a[40:43], a[100:103], v[244:247], a[40:43]\n"
"ds_read_b64_tr_b16 a[76:77], v12 offset:24576\n"
"v_mfma_f32_16x16x32_bf16 a[40:43], a[96:99], v[228:231], a[40:43]\n"
"ds_read_b64_tr_b16 a[78:79], v12 offset:28672\n"
"v_mfma_f32_16x16x32_bf16 v[72:75], a[96:99], v[232:235], v[72:75]\n"
"ds_read_b64_tr_b16 a[80:81], v13 offset:16384\n"
"s_bfe_u32 s80, s10, 0x50001\n"
"v_mfma_f32_16x16x32_bf16 v[72:75], a[100:103], v[248:251], v[72:75]\n"
"s_lshl_b32 s80, s80, 17\n"
"v_mfma_f32_16x16x32_bf16 v[88:91], a[100:103], v[252:255], v[88:91]\n"
"ds_read_b64_tr_b16 a[82:83], v13 offset:20480\n"
"v_mfma_f32_16x16x32_bf16 v[88:91], a[96:99], v[236:239], v[88:91]\n"
"ds_read_b64_tr_b16 a[84:85], v13 offset:24576\n"
"s_waitcnt lgkmcnt(7)\n"
"v_mfma_f32_16x16x32_bf16 v[92:95], a[64:67], v[236:239], v[92:95]\n"
"ds_read_b64_tr_b16 a[86:87], v13 offset:28672\n"
"v_mfma_f32_16x16x32_bf16 v[92:95], a[68:71], v[252:255], v[92:95]\n"
"buffer_load_dwordx4 a[104:107], v16, s[44:47], s80 offen offset:0 sc1\n"
"v_mfma_f32_16x16x32_bf16 v[76:79], a[68:71], v[248:251], v[76:79]\n"
"ds_read_b64_tr_b16 a[88:89], v14 offset:16384\n"
"v_mfma_f32_16x16x32_bf16 v[76:79], a[64:67], v[232:235], v[76:79]\n"
"ds_read_b64_tr_b16 a[90:91], v14 offset:20480\n"
"v_mfma_f32_16x16x32_bf16 a[44:47], a[64:67], v[228:231], a[44:47]\n"
"ds_read_b64_tr_b16 a[92:93], v14 offset:24576\n"
"v_mfma_f32_16x16x32_bf16 a[44:47], a[68:71], v[244:247], a[44:47]\n"
"buffer_load_dwordx4 a[108:111], v16, s[44:47], s80 offen offset:1024 sc1\n"
"v_mfma_f32_16x16x32_bf16 a[12:15], a[68:71], v[240:243], a[12:15]\n"
"ds_read_b64_tr_b16 a[94:95], v14 offset:28672\n"
"v_mfma_f32_16x16x32_bf16 a[12:15], a[64:67], v[224:227], a[12:15]\n"
"ds_read_b64_tr_b16 a[96:97], v15 offset:16384\n"
"s_waitcnt lgkmcnt(9)\n"
"v_mfma_f32_16x16x32_bf16 a[16:19], a[72:75], v[224:227], a[16:19]\n"
"ds_read_b64_tr_b16 a[98:99], v15 offset:20480\n"
"v_mfma_f32_16x16x32_bf16 a[16:19], a[76:79], v[240:243], a[16:19]\n"
"buffer_load_dwordx4 a[112:115], v17, s[44:47], s80 offen offset:0 sc1\n"
"v_mfma_f32_16x16x32_bf16 a[48:51], a[76:79], v[244:247], a[48:51]\n"
"ds_read_b64_tr_b16 a[100:101], v15 offset:24576\n"
"v_mfma_f32_16x16x32_bf16 a[48:51], a[72:75], v[228:231], a[48:51]\n"
"ds_read_b64_tr_b16 a[102:103], v15 offset:28672\n"
"v_mfma_f32_16x16x32_bf16 v[32:35], a[72:75], v[232:235], v[32:35]\n"
"buffer_load_dwordx4 a[116:119], v17, s[44:47], s80 offen offset:1024 sc1\n"
"v_mfma_f32_16x16x32_bf16 v[32:35], a[76:79], v[248:251], v[32:35]\n"
"s_add_u32 s80, s80, 0x20000\n"
"v_mfma_f32_16x16x32_bf16 v[48:51], a[76:79], v[252:255], v[48:51]\n"
"v_mfma_f32_16x16x32_bf16 v[48:51], a[72:75], v[236:239], v[48:51]\n"
"s_waitcnt lgkmcnt(8)\n"
"v_mfma_f32_16x16x32_bf16 v[52:55], a[80:83], v[236:239], v[52:55]\n"
"buffer_load_dwordx4 a[120:123], v16, s[44:47], s80 offen offset:0 sc1\n"
"v_mfma_f32_16x16x32_bf16 v[52:55], a[84:87], v[252:255], v[52:55]\n"
"v_mfma_f32_16x16x32_bf16 v[36:39], a[84:87], v[248:251], v[36:39]\n"
"v_mfma_f32_16x16x32_bf16 v[36:39], a[80:83], v[232:235], v[36:39]\n"
"buffer_load_dwordx4 a[124:127], v16, s[44:47], s80 offen offset:1024 sc1\n"
"v_mfma_f32_16x16x32_bf16 a[52:55], a[80:83], v[228:231], a[52:55]\n"
"v_mfma_f32_16x16x32_bf16 a[52:55], a[84:87], v[244:247], a[52:55]\n"
"v_mfma_f32_16x16x32_bf16 a[20:23], a[84:87], v[240:243], a[20:23]\n"
"buffer_load_dwordx4 a[128:131], v17, s[44:47], s80 offen offset:0 sc1\n"
"v_mfma_f32_16x16x32_bf16 a[20:23], a[80:83], v[224:227], a[20:23]\n"
"s_waitcnt vmcnt(15) lgkmcnt(0)\n"
"s_barrier\n"
"s_lshr_b32 s84, s76, 6\n"
"s_xor_b32 s83, s33, 1\n"
"s_cmp_lg_u32 s34, 0\n"
"s_cbranch_scc1 .Lfk_sig10\n"
"s_lshl_b32 s85, s84, 8\n"
"s_add_u32 s85, s85, 4096\n"
"v_mov_b32 v30, s85\n"
"v_mov_b32 v31, 1\n"
"s_mov_b64 exec, 1\n"
"global_atomic_add v30, v31, s[30:31]\n"
"s_lshl_b32 s85, s72, 8\n"
"s_add_u32 s85, s85, 12288\n"
"v_mov_b32 v30, s85\n"
"global_atomic_add v30, v31, s[30:31]\n"
"s_lshl_b32 s85, s83, 2\n"
"s_add_u32 s85, s85, 16384\n"
"v_mov_b32 v30, s85\n"
"global_atomic_add v30, v31, s[30:31]\n"
"s_mov_b64 exec, -1\n"
".Lfk_sig10:\n"
"v_mfma_f32_16x16x32_bf16 a[24:27], a[88:91], v[224:227], a[24:27]\n"
"ds_read_b64_tr_b16 a[64:65], v8 offset:32768\n"
"v_mfma_f32_16x16x32_bf16 a[24:27], a[92:95], v[240:243], a[24:27]\n"
"buffer_load_dwordx4 a[132:135], v17, s[44:47], s80 offen offset:1024 sc1\n"
"v_mfma_f32_16x16x32_bf16 a[56:59], a[92:95], v[244:247], a[56:59]\n"
"ds_read_b64_tr_b16 a[66:67], v8 offset:36864\n"
"v_mfma_f32_16x16x32_bf16 a[56:59], a[88:91], v[228:231], a[56:59]\n"
"v_mfma_f32_16x16x32_bf16 v[40:43], a[88:91], v[232:235], v[40:43]\n"
"ds_read_b64_tr_b16 a[68:69], v8 offset:40960\n"
"s_add_u32 m0, s77, 0x4000\n"
"v_mfma_f32_16x16x32_bf16 v[40:43], a[92:95], v[248:251], v[40:43]\n"
"s_bfe_u32 s79, s9, 0x60018\n"
"s_lshl_b32 s79, s79, 15\n"
"v_mfma_f32_16x16x32_bf16 v[56:59], a[92:95], v[252:255], v[56:59]\n"
"ds_read_b64_tr_b16 a[70:71], v8 offset:45056\n"
"v_mfma_f32_16x16x32_bf16 v[56:59], a[88:91], v[236:239], v[56:59]\n"
"v_mfma_f32_16x16x32_bf16 v[60:63], a[96:99], v[236:239], v[60:63]\n"
"ds_read_b64_tr_b16 a[72:73], v9 offset:32768\n"
"v_mfma_f32_16x16x32_bf16 v[60:63], a[100:103], v[252:255], v[60:63]\n"
"buffer_load_dwordx4 v4, s[48:51], s79 offen offset:0 lds\n"
"v_mfma_f32_16x16x32_bf16 v[44:47], a[100:103], v[248:251], v[44:47]\n"
"ds_read_b64_tr_b16 a[74:75], v9 offset:36864\n"
"v_mfma_f32_16x16x32_bf16 v[44:47], a[96:99], v[232:235], v[44:47]\n"
"v_mfma_f32_16x16x32_bf16 a[60:63], a[96:99], v[228:231], a[60:63]\n"
"ds_read_b64_tr_b16 a[76:77], v9 offset:40960\n"
"v_mfma_f32_16x16x32_bf16 a[60:63], a[100:103], v[244:247], a[60:63]\n"
"buffer_load_dwordx4 v5, s[48:51], s79 offen offset:1024 lds\n"
"v_mfma_f32_16x16x32_bf16 a[28:31], a[100:103], v[240:243], a[28:31]\n"
"ds_read_b64_tr_b16 a[78:79], v9 offset:45056\n"
"v_mfma_f32_16x16x32_bf16 a[28:31], a[96:99], v[224:227], a[28:31]\n"
"s_waitcnt lgkmcnt(4)\n"
"v_mfma_f32_16x16x32_bf16 a[0:3], a[64:67], a[136:139], a[0:3]\n"
"ds_read_b64_tr_b16 a[80:81], v10 offset:32768\n"
"v_mfma_f32_16x16x32_bf16 a[0:3], a[68:71], a[152:155], a[0:3]\n"
"buffer_load_dwordx4 v6, s[48:51], s79 offen offset:2048 lds\n"
"v_mfma_f32_16x16x32_bf16 a[32:35], a[68:71], a[156:159], a[32:35]\n"
"ds_read_b64_tr_b16 a[82:83], v10 offset:36864\n"
"v_mfma_f32_16x16x32_bf16 a[32:35], a[64:67], a[140:143], a[32:35]\n"
"v_mfma_f32_16x16x32_bf16 v[64:67], a[64:67], a[144:147], v[64:67]\n"
"ds_read_b64_tr_b16 a[84:85], v10 offset:40960\n"
"v_mfma_f32_16x16x32_bf16 v[64:67], a[68:71], a[160:163], v[64:67]\n"
"buffer_load_dwordx4 v7, s[48:51], s79 offen offset:3072 lds\n"
"v_mfma_f32_16x16x32_bf16 v[80:83], a[68:71], a[164:167], v[80:83]\n"
"ds_read_b64_tr_b16 a[86:87], v10 offset:45056\n"
"s_add_u32 m0, s77, 0x6000\n"
"v_mfma_f32_16x16x32_bf16 v[80:83], a[64:67], a[148:151], v[80:83]\n"
"s_bfe_u32 s79, s15, 0x60018\n"
"s_waitcnt lgkmcnt(4)\n"
"v_mfma_f32_16x16x32_bf16 v[84:87], a[72:75], a[148:151], v[84:87]\n"
"ds_read_b64_tr_b16 a[88:89], v11 offset:32768\n"
"s_lshl_b32 s79, s79, 15\n"
"v_mfma_f32_16x16x32_bf16 v[84:87], a[76:79], a[164:167], v[84:87]\n"
"buffer_load_dwordx4 v4, s[48:51], s79 offen offset:0 lds\n"
"v_mfma_f32_16x16x32_bf16 v[68:71], a[76:79], a[160:163], v[68:71]\n"
"ds_read_b64_tr_b16 a[90:91], v11 offset:36864\n"
"v_mfma_f32_16x16x32_bf16 v[68:71], a[72:75], a[144:147], v[68:71]\n"
"v_mfma_f32_16x16x32_bf16 a[36:39], a[72:75], a[140:143], a[36:39]\n"
"ds_read_b64_tr_b16 a[92:93], v11 offset:40960\n"
"v_mfma_f32_16x16x32_bf16 a[36:39], a[76:79], a[156:159], a[36:39]\n"
"buffer_load_dwordx4 v5, s[48:51], s79 offen offset:1024 lds\n"
"v_mfma_f32_16x16x32_bf16 a[4:7], a[76:79], a[152:155], a[4:7]\n"
"ds_read_b64_tr_b16 a[94:95], v11 offset:45056\n"
"v_mfma_f32_16x16x32_bf16 a[4:7], a[72:75], a[136:139], a[4:7]\n"
"ds_read_b64_tr_b16 a[96:97], v12 offset:32768\n"
"s_waitcnt lgkmcnt(5)\n"
"v_mfma_f32_16x16x32_bf16 a[8:11], a[80:83], a[136:139], a[8:11]\n"
"ds_read_b64_tr_b16 a[98:99], v12 offset:36864\n"
"v_mfma_f32_16x16x32_bf16 a[8:11], a[84:87], a[152:155], a[8:11]\n"
"buffer_load_dwordx4 v6, s[48:51], s79 offen offset:2048 lds\n"
"v_mfma_f32_16x16x32_bf16 a[40:43], a[84:87], a[156:159], a[40:43]\n"
"ds_read_b64_tr_b16 a[100:101], v12 offset:40960\n"
"v_mfma_f32_16x16x32_bf16 a[40:43], a[80:83], a[140:143], a[40:43]\n"
"ds_read_b64_tr_b16 a[102:103], v12 offset:45056\n"
"v_mfma_f32_16x16x32_bf16 v[72:75], a[80:83], a[144:147], v[72:75]\n"
"ds_read_b64_tr_b16 a[64:65], v13 offset:32768\n"
"v_mfma_f32_16x16x32_bf16 v[72:75], a[84:87], a[160:163], v[72:75]\n"
"buffer_load_dwordx4 v7, s[48:51], s79 offen offset:3072 lds\n"
"v_mfma_f32_16x16x32_bf16 v[88:91], a[84:87], a[164:167], v[88:91]\n"
"ds_read_b64_tr_b16 a[66:67], v13 offset:36864\n"
"v_mfma_f32_16x16x32_bf16 v[88:91], a[80:83], a[148:151], v[88:91]\n"
"ds_read_b64_tr_b16 a[68:69], v13 offset:40960\n"
"s_waitcnt lgkmcnt(7)\n"
"v_mfma_f32_16x16x32_bf16 v[92:95], a[88:91], a[148:151], v[92:95]\n"
"ds_read_b64_tr_b16 a[70:71], v13 offset:45056\n"
"s_bfe_u32 s80, s10, 0x50009\n"
"v_mfma_f32_16x16x32_bf16 v[92:95], a[92:95], a[164:167], v[92:95]\n"
"s_lshl_b32 s80, s80, 17\n"
"v_mfma_f32_16x16x32_bf16 v[76:79], a[92:95], a[160:163], v[76:79]\n"
"ds_read_b64_tr_b16 a[72:73], v14 offset:32768\n"
"v_mfma_f32_16x16x32_bf16 v[76:79], a[88:91], a[144:147], v[76:79]\n"
"ds_read_b64_tr_b16 a[74:75], v14 offset:36864\n"
"v_mfma_f32_16x16x32_bf16 a[44:47], a[88:91], a[140:143], a[44:47]\n"
"ds_read_b64_tr_b16 a[76:77], v14 offset:40960\n"
"v_mfma_f32_16x16x32_bf16 a[44:47], a[92:95], a[156:159], a[44:47]\n"
"buffer_load_dwordx4 v[96:99], v16, s[44:47], s80 offen offset:0 sc1\n"
"v_mfma_f32_16x16x32_bf16 a[12:15], a[92:95], a[152:155], a[12:15]\n"
"ds_read_b64_tr_b16 a[78:79], v14 offset:45056\n"
"v_mfma_f32_16x16x32_bf16 a[12:15], a[88:91], a[136:139], a[12:15]\n"
"ds_read_b64_tr_b16 a[80:81], v15 offset:32768\n"
"s_waitcnt lgkmcnt(9)\n"
"v_mfma_f32_16x16x32_bf16 a[16:19], a[96:99], a[136:139], a[16:19]\n"
"ds_read_b64_tr_b16 a[82:83], v15 offset:36864\n"
"v_mfma_f32_16x16x32_bf16 a[16:19], a[100:103], a[152:155], a[16:19]\n"
"buffer_load_dwordx4 v[100:103], v16, s[44:47], s80 offen offset:1024 sc1\n"
"v_mfma_f32_16x16x32_bf16 a[48:51], a[100:103], a[156:159], a[48:51]\n"
"ds_read_b64_tr_b16 a[84:85], v15 offset:40960\n"
"v_mfma_f32_16x16x32_bf16 a[48:51], a[96:99], a[140:143], a[48:51]\n"
"ds_read_b64_tr_b16 a[86:87], v15 offset:45056\n"
"v_mfma_f32_16x16x32_bf16 v[32:35], a[96:99], a[144:147], v[32:35]\n"
"buffer_load_dwordx4 v[104:107], v17, s[44:47], s80 offen offset:0 sc1\n"
"v_mfma_f32_16x16x32_bf16 v[32:35], a[100:103], a[160:163], v[32:35]\n"
"v_mfma_f32_16x16x32_bf16 v[48:51], a[100:103], a[164:167], v[48:51]\n"
"v_mfma_f32_16x16x32_bf16 v[48:51], a[96:99], a[148:151], v[48:51]\n"
"s_waitcnt lgkmcnt(8)\n"
"v_mfma_f32_16x16x32_bf16 v[52:55], a[64:67], a[148:151], v[52:55]\n"
"buffer_load_dwordx4 v[108:111], v17, s[44:47], s80 offen offset:1024 sc1\n"
"v_mfma_f32_16x16x32_bf16 v[52:55], a[68:71], a[164:167], v[52:55]\n"
"s_add_u32 s80, s80, 0x20000\n"
"v_mfma_f32_16x16x32_bf16 v[36:39], a[68:71], a[160:163], v[36:39]\n"
"v_mfma_f32_16x16x32_bf16 v[36:39], a[64:67], a[144:147], v[36:39]\n"
"buffer_load_dwordx4 v[112:115], v16, s[44:47], s80 offen offset:0 sc1\n"
"v_mfma_f32_16x16x32_bf16 a[52:55], a[64:67], a[140:143], a[52:55]\n"
"v_mfma_f32_16x16x32_bf16 a[52:55], a[68:71], a[156:159], a[52:55]\n"
"v_mfma_f32_16x16x32_bf16 a[20:23], a[68:71], a[152:155], a[20:23]\n"
"buffer_load_dwordx4 v[116:119], v16, s[44:47], s80 offen offset:1024 sc1\n"
"v_mfma_f32_16x16x32_bf16 a[20:23], a[64:67], a[136:139], a[20:23]\n"
"s_waitcnt vmcnt(22) lgkmcnt(0)\n"
"s_barrier\n"
"v_mfma_f32_16x16x32_bf16 a[24:27], a[72:75], a[136:139], a[24:27]\n"
"ds_read_b64_tr_b16 a[88:89], v8 offset:0\n"
"v_mfma_f32_16x16x32_bf16 a[24:27], a[76:79], a[152:155], a[24:27]\n"
"buffer_load_dwordx4 v[120:123], v17, s[44:47], s80 offen offset:0 sc1\n"
"v_mfma_f32_16x16x32_bf16 a[56:59], a[76:79], a[156:159], a[56:59]\n"
"ds_read_b64_tr_b16 a[90:91], v8 offset:4096\n"
"v_mfma_f32_16x16x32_bf16 a[56:59], a[72:75], a[140:143], a[56:59]\n"
"v_mfma_f32_16x16x32_bf16 v[40:43], a[72:75], a[144:147], v[40:43]\n"
"ds_read_b64_tr_b16 a[92:93], v8 offset:8192\n"
"v_mfma_f32_16x16x32_bf16 v[40:43], a[76:79], a[160:163], v[40:43]\n"
"buffer_load_dwordx4 v[124:127], v17, s[44:47], s80 offen offset:1024 sc1\n"
"v_mfma_f32_16x16x32_bf16 v[56:59], a[76:79], a[164:167], v[56:59]\n"
"ds_read_b64_tr_b16 a[94:95], v8 offset:12288\n"
"v_mfma_f32_16x16x32_bf16 v[56:59], a[72:75], a[148:151], v[56:59]\n"
"v_mfma_f32_16x16x32_bf16 v[60:63], a[80:83], a[148:151], v[60:63]\n"
"ds_read_b64_tr_b16 a[96:97], v9 offset:0\n"
"s_add_u32 m0, s77, 0x8000\n"
"v_mfma_f32_16x16x32_bf16 v[60:63], a[84:87], a[164:167], v[60:63]\n"
"s_bfe_u32 s79, s10, 0x60000\n"
"s_lshl_b32 s79, s79, 15\n"
"v_mfma_f32_16x16x32_bf16 v[44:47], a[84:87], a[160:163], v[44:47]\n"
"ds_read_b64_tr_b16 a[98:99], v9 offset:4096\n"
"v_mfma_f32_16x16x32_bf16 v[44:47], a[80:83], a[144:147], v[44:47]\n"
"buffer_load_dwordx4 v4, s[48:51], s79 offen offset:0 lds\n"
"v_mfma_f32_16x16x32_bf16 a[60:63], a[80:83], a[140:143], a[60:63]\n"
"ds_read_b64_tr_b16 a[100:101], v9 offset:8192\n"
"v_mfma_f32_16x16x32_bf16 a[60:63], a[84:87], a[156:159], a[60:63]\n"
"v_mfma_f32_16x16x32_bf16 a[28:31], a[84:87], a[152:155], a[28:31]\n"
"ds_read_b64_tr_b16 a[102:103], v9 offset:12288\n"
"v_mfma_f32_16x16x32_bf16 a[28:31], a[80:83], a[136:139], a[28:31]\n"
"s_waitcnt lgkmcnt(4)\n"
"v_mfma_f32_16x16x32_bf16 a[0:3], a[88:91], a[168:171], a[0:3]\n"
"ds_read_b64_tr_b16 a[64:65], v10 offset:0\n"
"s_lshl1_add_u32 s81, s74, s22\n"
"v_mfma_f32_16x16x32_bf16 a[0:3], a[92:95], a[184:187], a[0:3]\n"
"buffer_load_dwordx4 v5, s[48:51], s79 offen offset:1024 lds\n"
"v_mfma_f32_16x16x32_bf16 a[32:35], a[92:95], a[188:191], a[32:35]\n"
"ds_read_b64_tr_b16 a[66:67], v10 offset:4096\n"
"s_lshl1_add_u32 s81, s81, s21\n"
"v_mfma_f32_16x16x32_bf16 a[32:35], a[88:91], a[172:175], a[32:35]\n"
"v_mfma_f32_16x16x32_bf16 v[64:67], a[88:91], a[176:179], v[64:67]\n"
"ds_read_b64_tr_b16 a[68:69], v10 offset:8192\n"
"s_lshl_b32 s81, s81, 5\n"
"v_mfma_f32_16x16x32_bf16 v[64:67], a[92:95], a[192:195], v[64:67]\n"
"buffer_load_dwordx4 v6, s[48:51], s79 offen offset:2048 lds\n"
"v_mfma_f32_16x16x32_bf16 v[80:83], a[92:95], a[196:199], v[80:83]\n"
"ds_read_b64_tr_b16 a[70:71], v10 offset:12288\n"
"v_and_b32 v31, 15, v0\n"
"v_mfma_f32_16x16x32_bf16 v[80:83], a[88:91], a[180:183], v[80:83]\n"
"s_waitcnt lgkmcnt(4)\n"
"v_mfma_f32_16x16x32_bf16 v[84:87], a[96:99], a[180:183], v[84:87]\n"
"ds_read_b64_tr_b16 a[72:73], v11 offset:0\n"
"v_lshrrev_b32 v30, 4, v0\n"
"v_mfma_f32_16x16x32_bf16 v[84:87], a[100:103], a[196:199], v[84:87]\n"
"buffer_load_dwordx4 v7, s[48:51], s79 offen offset:3072 lds\n"
"v_mfma_f32_16x16x32_bf16 v[68:71], a[100:103], a[192:195], v[68:71]\n"
"ds_read_b64_tr_b16 a[74:75], v11 offset:4096\n"
"v_add_u32 v28, s81, v31\n"
"v_mfma_f32_16x16x32_bf16 v[68:71], a[96:99], a[176:179], v[68:71]\n"
"s_add_u32 m0, s77, 0xa000\n"
"s_bfe_u32 s79, s27, 0x60000\n"
"v_mfma_f32_16x16x32_bf16 a[36:39], a[96:99], a[172:175], a[36:39]\n"
"ds_read_b64_tr_b16 a[76:77], v11 offset:8192\n"
"v_lshlrev_b32 v26, 2, v28\n"
"v_mfma_f32_16x16x32_bf16 a[36:39], a[100:103], a[188:191], a[36:39]\n"
"s_lshl_b32 s79, s79, 15\n"
"v_mfma_f32_16x16x32_bf16 a[4:7], a[100:103], a[184:187], a[4:7]\n"
"ds_read_b64_tr_b16 a[78:79], v11 offset:12288\n"
"s_lshl_b32 s82, s75, 18\n"
"v_mfma_f32_16x16x32_bf16 a[4:7], a[96:99], a[168:171], a[4:7]\n"
"ds_read_b64_tr_b16 a[80:81], v12 offset:0\n"
"s_waitcnt lgkmcnt(5)\n"
"v_mfma_f32_16x16x32_bf16 a[8:11], a[64:67], a[168:171], a[8:11]\n"
"ds_read_b64_tr_b16 a[82:83], v12 offset:4096\n"
"s_add_u32 s82, s82, 0x100000\n"
"v_mfma_f32_16x16x32_bf16 a[8:11], a[68:71], a[184:187], a[8:11]\n"
"buffer_load_dwordx4 v4, s[48:51], s79 offen offset:0 lds\n"
"v_mfma_f32_16x16x32_bf16 a[40:43], a[68:71], a[188:191], a[40:43]\n"
"ds_read_b64_tr_b16 a[84:85], v12 offset:8192\n"
"v_lshl_add_u32 v28, v28, 3, s82\n"
"v_mfma_f32_16x16x32_bf16 a[40:43], a[64:67], a[172:175], a[40:43]\n"
"ds_read_b64_tr_b16 a[86:87], v12 offset:12288\n"
"v_mfma_f32_16x16x32_bf16 v[72:75], a[64:67], a[176:179], v[72:75]\n"
"ds_read_b64_tr_b16 a[88:89], v13 offset:0\n"
"v_lshl_add_u32 v28, v30, 14, v28\n"
"v_mfma_f32_16x16x32_bf16 v[72:75], a[68:71], a[192:195], v[72:75]\n"
"buffer_load_dwordx4 v5, s[48:51], s79 offen offset:1024 lds\n"
"v_mfma_f32_16x16x32_bf16 v[88:91], a[68:71], a[196:199], v[88:91]\n"
"ds_read_b64_tr_b16 a[90:91], v13 offset:4096\n"
"v_accvgpr_write_b32 a232, v26\n"
"v_mfma_f32_16x16x32_bf16 v[88:91], a[64:67], a[180:183], v[88:91]\n"
"ds_read_b64_tr_b16 a[92:93], v13 offset:8192\n"
"s_waitcnt lgkmcnt(7)\n"
"v_mfma_f32_16x16x32_bf16 v[92:95], a[72:75], a[180:183], v[92:95]\n"
"ds_read_b64_tr_b16 a[94:95], v13 offset:12288\n"
"s_lshl_b32 s78, s34, 12\n"
"v_mfma_f32_16x16x32_bf16 v[92:95], a[76:79], a[196:199], v[92:95]\n"
"buffer_load_dwordx4 v6, s[48:51], s79 offen offset:2048 lds\n"
"v_mfma_f32_16x16x32_bf16 v[76:79], a[76:79], a[192:195], v[76:79]\n"
"ds_read_b64_tr_b16 a[96:97], v14 offset:0\n"
"s_add_u32 s78, s78, 139264\n"
"v_mfma_f32_16x16x32_bf16 v[76:79], a[72:75], a[176:179], v[76:79]\n"
"ds_read_b64_tr_b16 a[98:99], v14 offset:4096\n"
"v_mfma_f32_16x16x32_bf16 a[44:47], a[72:75], a[172:175], a[44:47]\n"
"ds_read_b64_tr_b16 a[100:101], v14 offset:8192\n"
"v_lshrrev_b32 v26, 1, v31\n"
"v_mfma_f32_16x16x32_bf16 a[44:47], a[76:79], a[188:191], a[44:47]\n"
"buffer_load_dwordx4 v7, s[48:51], s79 offen offset:3072 lds\n"
"v_mfma_f32_16x16x32_bf16 a[12:15], a[76:79], a[184:187], a[12:15]\n"
"ds_read_b64_tr_b16 a[102:103], v14 offset:12288\n"
"v_lshrrev_b32 v27, 1, v30\n"
"v_mfma_f32_16x16x32_bf16 a[12:15], a[72:75], a[168:171], a[12:15]\n"
"ds_read_b64_tr_b16 a[64:65], v15 offset:0\n"
"s_waitcnt lgkmcnt(9)\n"
"v_mfma_f32_16x16x32_bf16 a[16:19], a[80:83], a[168:171], a[16:19]\n"
"ds_read_b64_tr_b16 a[66:67], v15 offset:4096\n"
"v_xor_b32 v26, v26, v27\n"
"v_mfma_f32_16x16x32_bf16 a[16:19], a[84:87], a[184:187], a[16:19]\n"
"s_bfe_u32 s80, s10, 0x50011\n"
"s_lshl_b32 s80, s80, 17\n"
"v_mfma_f32_16x16x32_bf16 a[48:51], a[84:87], a[188:191], a[48:51]\n"
"ds_read_b64_tr_b16 a[68:69], v15 offset:8192\n"
"v_lshlrev_b32 v26, 4, v26\n"
"v_mfma_f32_16x16x32_bf16 a[48:51], a[80:83], a[172:175], a[48:51]\n"
"ds_read_b64_tr_b16 a[70:71], v15 offset:12288\n"
"v_mfma_f32_16x16x32_bf16 v[32:35], a[80:83], a[176:179], v[32:35]\n"
"v_lshl_add_u32 v26, v31, 7, v26\n"
"v_mfma_f32_16x16x32_bf16 v[32:35], a[84:87], a[192:195], v[32:35]\n"
"buffer_load_dwordx4 v[128:131], v16, s[44:47], s80 offen offset:0 sc1\n"
"v_mfma_f32_16x16x32_bf16 v[48:51], a[84:87], a[196:199], v[48:51]\n"
"v_and_b32 v27, 1, v30\n"
"v_mfma_f32_16x16x32_bf16 v[48:51], a[80:83], a[180:183], v[48:51]\n"
"s_waitcnt lgkmcnt(8)\n"
"v_mfma_f32_16x16x32_bf16 v[52:55], a[88:91], a[180:183], v[52:55]\n"
"v_lshl_add_u32 v26, v27, 3, v26\n"
"v_mfma_f32_16x16x32_bf16 v[52:55], a[92:95], a[196:199], v[52:55]\n"
"buffer_load_dwordx4 v[132:135], v16, s[44:47], s80 offen offset:1024 sc1\n"
"v_mfma_f32_16x16x32_bf16 v[36:39], a[92:95], a[192:195], v[36:39]\n"
"v_add_u32 v2, s78, v26\n"
"v_mfma_f32_16x16x32_bf16 v[36:39], a[88:91], a[176:179], v[36:39]\n"
"v_mfma_f32_16x16x32_bf16 a[52:55], a[88:91], a[172:175], a[52:55]\n"
"v_xor_b32 v3, 32, v2\n"
"v_mfma_f32_16x16x32_bf16 a[52:55], a[92:95], a[188:191], a[52:55]\n"
".Lfk_hop6890:\n"
"s_cbranch_execz .Lfk_hop7433\n"
"buffer_load_dwordx4 v[136:139], v17, s[44:47], s80 offen offset:0 sc1\n"
"v_mfma_f32_16x16x32_bf16 a[20:23], a[92:95], a[184:187], a[20:23]\n"
"v_xor_b32 v24, 64, v2\n"
"v_mfma_f32_16x16x32_bf16 a[20:23], a[88:91], a[168:171], a[20:23]\n"
"s_waitcnt vmcnt(19) lgkmcnt(0)\n"
"s_barrier\n"
"v_mfma_f32_16x16x32_bf16 a[24:27], a[96:99], a[168:171], a[24:27]\n"
"ds_read_b64_tr_b16 a[72:73], v8 offset:16384\n"
"v_xor_b32 v25, 96, v2\n"
"v_mfma_f32_16x16x32_bf16 a[24:27], a[100:103], a[184:187], a[24:27]\n"
"buffer_load_dwordx4 v[140:143], v17, s[44:47], s80 offen offset:1024 sc1\n"
"v_mfma_f32_16x16x32_bf16 a[56:59], a[100:103], a[188:191], a[56:59]\n"
"ds_read_b64_tr_b16 a[74:75], v8 offset:20480\n"
"s_lshl_b32 s81, s81, 11\n"
"v_mfma_f32_16x16x32_bf16 a[56:59], a[96:99], a[172:175], a[56:59]\n"
"s_add_u32 s80, s80, 0x20000\n"
"v_mfma_f32_16x16x32_bf16 v[40:43], a[96:99], a[176:179], v[40:43]\n"
"ds_read_b64_tr_b16 a[76:77], v8 offset:24576\n"
"s_lshl_b32 s82, s75, 7\n"
"v_mfma_f32_16x16x32_bf16 v[40:43], a[100:103], a[192:195], v[40:43]\n"
"buffer_load_dwordx4 v[144:147], v16, s[44:47], s80 offen offset:0 sc1\n"
"v_mfma_f32_16x16x32_bf16 v[56:59], a[100:103], a[196:199], v[56:59]\n"
"ds_read_b64_tr_b16 a[78:79], v8 offset:28672\n"
"s_add_u32 s81, s81, s82\n"
"v_mfma_f32_16x16x32_bf16 v[56:59], a[96:99], a[180:183], v[56:59]\n"
"v_mfma_f32_16x16x32_bf16 v[60:63], a[64:67], a[180:183], v[60:63]\n"
"ds_read_b64_tr_b16 a[80:81], v9 offset:16384\n"
"v_lshrrev_b32 v26, 3, v0\n"
"v_mfma_f32_16x16x32_bf16 v[60:63], a[68:71], a[196:199], v[60:63]\n"
"buffer_load_dwordx4 v[148:151], v16, s[44:47], s80 offen offset:1024 sc1\n"
"v_mfma_f32_16x16x32_bf16 v[44:47], a[68:71], a[192:195], v[44:47]\n"
"ds_read_b64_tr_b16 a[82:83], v9 offset:20480\n"
"v_lshlrev_b32 v26, 11, v26\n"
"v_mfma_f32_16x16x32_bf16 v[44:47], a[64:67], a[176:179], v[44:47]\n"
"v_mfma_f32_16x16x32_bf16 a[60:63], a[64:67], a[172:175], a[60:63]\n"
"ds_read_b64_tr_b16 a[84:85], v9 offset:24576\n"
"v_add_u32 v26, s81, v26\n"
"v_mfma_f32_16x16x32_bf16 a[60:63], a[68:71], a[188:191], a[60:63]\n"
"buffer_load_dwordx4 v[152:155], v17, s[44:47], s80 offen offset:0 sc1\n"
"v_mfma_f32_16x16x32_bf16 a[28:31], a[68:71], a[184:187], a[28:31]\n"
"ds_read_b64_tr_b16 a[86:87], v9 offset:28672\n"
"v_and_b32 v27, 7, v0\n"
"v_mfma_f32_16x16x32_bf16 a[28:31], a[64:67], a[168:171], a[28:31]\n"
"s_waitcnt lgkmcnt(4)\n"
"v_mfma_f32_16x16x32_bf16 a[0:3], a[72:75], a[200:203], a[0:3]\n"
"ds_read_b64_tr_b16 a[88:89], v10 offset:16384\n"
"v_xor_b32 v27, v27, v30\n"
"v_mfma_f32_16x16x32_bf16 a[0:3], a[76:79], a[216:219], a[0:3]\n"
"buffer_load_dwordx4 v[156:159], v17, s[44:47], s80 offen offset:1024 sc1\n"
"v_mfma_f32_16x16x32_bf16 a[32:35], a[76:79], a[220:223], a[32:35]\n"
"ds_read_b64_tr_b16 a[90:91], v10 offset:20480\n"
"v_xor_b32 v31, 4, v27\n"
"v_mfma_f32_16x16x32_bf16 a[32:35], a[72:75], a[204:207], a[32:35]\n"
"v_mfma_f32_16x16x32_bf16 v[64:67], a[72:75], a[208:211], v[64:67]\n"
"ds_read_b64_tr_b16 a[92:93], v10 offset:24576\n"
"v_lshl_add_u32 v27, v27, 4, v26\n"
"v_mfma_f32_16x16x32_bf16 v[64:67], a[76:79], a[224:227], v[64:67]\n"
"s_add_u32 m0, s77, 0x0\n"
"s_bfe_u32 s79, s10, 0x60008\n"
"v_mfma_f32_16x16x32_bf16 v[80:83], a[76:79], a[228:231], v[80:83]\n"
"ds_read_b64_tr_b16 a[94:95], v10 offset:28672\n"
"v_lshl_add_u32 v31, v31, 4, v26\n"
"v_mfma_f32_16x16x32_bf16 v[80:83], a[72:75], a[212:215], v[80:83]\n"
"s_lshl_b32 s79, s79, 15\n"
"s_waitcnt lgkmcnt(4)\n"
"v_mfma_f32_16x16x32_bf16 v[84:87], a[80:83], a[212:215], v[84:87]\n"
"ds_read_b64_tr_b16 a[96:97], v11 offset:16384\n"
"v_add_u32 v20, 0x0, v27\n"
"v_mfma_f32_16x16x32_bf16 v[84:87], a[84:87], a[228:231], v[84:87]\n"
"buffer_load_dwordx4 v4, s[48:51], s79 offen offset:0 lds\n"
"v_mfma_f32_16x16x32_bf16 v[68:71], a[84:87], a[224:227], v[68:71]\n"
"ds_read_b64_tr_b16 a[98:99], v11 offset:20480\n"
"v_add_u32 v21, 0x3c00, v31\n"
"v_mfma_f32_16x16x32_bf16 v[68:71], a[80:83], a[208:211], v[68:71]\n"
"v_mfma_f32_16x16x32_bf16 a[36:39], a[80:83], a[204:207], a[36:39]\n"
"ds_read_b64_tr_b16 a[100:101], v11 offset:24576\n"
"v_add_u32 v22, 0x7800, v27\n"
"v_mfma_f32_16x16x32_bf16 a[36:39], a[84:87], a[220:223], a[36:39]\n"
"buffer_load_dwordx4 v5, s[48:51], s79 offen offset:1024 lds\n"
"v_mfma_f32_16x16x32_bf16 a[4:7], a[84:87], a[216:219], a[4:7]\n"
"ds_read_b64_tr_b16 a[102:103], v11 offset:28672\n"
"v_add_u32 v23, 0xb400, v31\n"
"v_mfma_f32_16x16x32_bf16 a[4:7], a[80:83], a[200:203], a[4:7]\n"
"ds_read_b64_tr_b16 a[64:65], v12 offset:16384\n"
"s_waitcnt lgkmcnt(5)\n"
"v_mfma_f32_16x16x32_bf16 a[8:11], a[88:91], a[200:203], a[8:11]\n"
"ds_read_b64_tr_b16 a[66:67], v12 offset:20480\n"
"s_mov_b32 s12, 0x10000\n"
"v_mfma_f32_16x16x32_bf16 a[8:11], a[92:95], a[216:219], a[8:11]\n"
"buffer_load_dwordx4 v6, s[48:51], s79 offen offset:2048 lds\n"
"v_mfma_f32_16x16x32_bf16 a[40:43], a[92:95], a[220:223], a[40:43]\n"
"ds_read_b64_tr_b16 a[68:69], v12 offset:24576\n"
"s_mov_b32 s13, 0x20000\n"
"v_mfma_f32_16x16x32_bf16 a[40:43], a[88:91], a[204:207], a[40:43]\n"
"ds_read_b64_tr_b16 a[70:71], v12 offset:28672\n"
"v_mfma_f32_16x16x32_bf16 v[72:75], a[88:91], a[208:211], v[72:75]\n"
"ds_read_b64_tr_b16 a[72:73], v13 offset:16384\n"
"s_mov_b32 s94, 0x30000\n"
"v_mfma_f32_16x16x32_bf16 v[72:75], a[92:95], a[224:227], v[72:75]\n"
"buffer_load_dwordx4 v7, s[48:51], s79 offen offset:3072 lds\n"
"v_mfma_f32_16x16x32_bf16 v[88:91], a[92:95], a[228:231], v[88:91]\n"
"ds_read_b64_tr_b16 a[74:75], v13 offset:20480\n"
"s_mov_b32 s95, 0x10000\n"
"v_mfma_f32_16x16x32_bf16 v[88:91], a[88:91], a[212:215], v[88:91]\n"
"ds_read_b64_tr_b16 a[76:77], v13 offset:24576\n"
"s_waitcnt lgkmcnt(7)\n"
"v_mfma_f32_16x16x32_bf16 v[92:95], a[96:99], a[212:215], v[92:95]\n"
"ds_read_b64_tr_b16 a[78:79], v13 offset:28672\n"
"s_lshl_b32 s81, s34, 13\n"
"v_mfma_f32_16x16x32_bf16 v[92:95], a[100:103], a[228:231], v[92:95]\n"
"s_add_u32 m0, s77, 0x2000\n"
"s_bfe_u32 s79, s27, 0x60008\n"
"v_mfma_f32_16x16x32_bf16 v[76:79], a[100:103], a[224:227], v[76:79]\n"
"ds_read_b64_tr_b16 a[80:81], v14 offset:16384\n"
"s_add_u32 s82, s81, 0x1a000\n"
"v_mfma_f32_16x16x32_bf16 v[76:79], a[96:99], a[208:211], v[76:79]\n"
"ds_read_b64_tr_b16 a[82:83], v14 offset:20480\n"
"s_lshl_b32 s79, s79, 15\n"
"v_mfma_f32_16x16x32_bf16 a[44:47], a[96:99], a[204:207], a[44:47]\n"
"ds_read_b64_tr_b16 a[84:85], v14 offset:24576\n"
"v_add_u32 v27, s82, v1\n"
"v_mfma_f32_16x16x32_bf16 a[44:47], a[100:103], a[220:223], a[44:47]\n"
"buffer_load_dwordx4 v4, s[48:51], s79 offen offset:0 lds\n"
"v_mfma_f32_16x16x32_bf16 a[12:15], a[100:103], a[216:219], a[12:15]\n"
"ds_read_b64_tr_b16 a[86:87], v14 offset:28672\n"
"s_xor_b32 s81, s81, 0x4000\n"
"v_mfma_f32_16x16x32_bf16 a[12:15], a[96:99], a[200:203], a[12:15]\n"
"ds_read_b64_tr_b16 a[88:89], v15 offset:16384\n"
"s_waitcnt lgkmcnt(9)\n"
"v_mfma_f32_16x16x32_bf16 a[16:19], a[64:67], a[200:203], a[16:19]\n"
"ds_read_b64_tr_b16 a[90:91], v15 offset:20480\n"
"s_add_u32 s82, s81, 0x1a000\n"
"v_mfma_f32_16x16x32_bf16 a[16:19], a[68:71], a[216:219], a[16:19]\n"
"buffer_load_dwordx4 v5, s[48:51], s79 offen offset:1024 lds\n"
"v_mfma_f32_16x16x32_bf16 a[48:51], a[68:71], a[220:223], a[48:51]\n"
"ds_read_b64_tr_b16 a[92:93], v15 offset:24576\n"
"v_add_u32 v26, s82, v1\n"
"v_mfma_f32_16x16x32_bf16 a[48:51], a[64:67], a[204:207], a[48:51]\n"
"ds_read_b64_tr_b16 a[94:95], v15 offset:28672\n"
"v_mfma_f32_16x16x32_bf16 v[32:35], a[64:67], a[208:211], v[32:35]\n"
"buffer_load_dwordx4 v6, s[48:51], s79 offen offset:2048 lds\n"
"v_mfma_f32_16x16x32_bf16 v[32:35], a[68:71], a[224:227], v[32:35]\n"
"v_mfma_f32_16x16x32_bf16 v[48:51], a[68:71], a[228:231], v[48:51]\n"
"v_mfma_f32_16x16x32_bf16 v[48:51], a[64:67], a[212:215], v[48:51]\n"
"s_waitcnt lgkmcnt(8)\n"
"v_mfma_f32_16x16x32_bf16 v[52:55], a[72:75], a[212:215], v[52:55]\n"
"buffer_load_dwordx4 v7, s[48:51], s79 offen offset:3072 lds\n"
"v_mfma_f32_16x16x32_bf16 v[52:55], a[76:79], a[228:231], v[52:55]\n"
"v_mfma_f32_16x16x32_bf16 v[36:39], a[76:79], a[224:227], v[36:39]\n"
"v_mfma_f32_16x16x32_bf16 v[36:39], a[72:75], a[208:211], v[36:39]\n"
"s_bfe_u32 s80, s10, 0x50019\n"
"s_lshl_b32 s80, s80, 17\n"
"v_mfma_f32_16x16x32_bf16 a[52:55], a[72:75], a[204:207], a[52:55]\n"
"buffer_load_dwordx4 v[160:163], v16, s[44:47], s80 offen offset:0 sc1\n"
"v_mfma_f32_16x16x32_bf16 a[52:55], a[76:79], a[220:223], a[52:55]\n"
"v_mfma_f32_16x16x32_bf16 a[20:23], a[76:79], a[216:219], a[20:23]\n"
"v_mfma_f32_16x16x32_bf16 a[20:23], a[72:75], a[200:203], a[20:23]\n"
"s_waitcnt vmcnt(17) lgkmcnt(0)\n"
"s_barrier\n"
"v_mfma_f32_16x16x32_bf16 a[24:27], a[80:83], a[200:203], a[24:27]\n"
"ds_read_b64_tr_b16 a[96:97], v8 offset:32768\n"
"v_mfma_f32_16x16x32_bf16 a[24:27], a[84:87], a[216:219], a[24:27]\n"
"buffer_load_dwordx4 v[164:167], v16, s[44:47], s80 offen offset:1024 sc1\n"
"v_mfma_f32_16x16x32_bf16 a[56:59], a[84:87], a[220:223], a[56:59]\n"
"ds_read_b64_tr_b16 a[98:99], v8 offset:36864\n"
"v_mfma_f32_16x16x32_bf16 a[56:59], a[80:83], a[204:207], a[56:59]\n"
"v_mfma_f32_16x16x32_bf16 v[40:43], a[80:83], a[208:211], v[40:43]\n"
"ds_read_b64_tr_b16 a[100:101], v8 offset:40960\n"
"v_mfma_f32_16x16x32_bf16 v[40:43], a[84:87], a[224:227], v[40:43]\n"
"buffer_load_dwordx4 v[168:171], v17, s[44:47], s80 offen offset:0 sc1\n"
"v_mfma_f32_16x16x32_bf16 v[56:59], a[84:87], a[228:231], v[56:59]\n"
"ds_read_b64_tr_b16 a[102:103], v8 offset:45056\n"
"v_mfma_f32_16x16x32_bf16 v[56:59], a[80:83], a[212:215], v[56:59]\n"
"v_mfma_f32_16x16x32_bf16 v[60:63], a[88:91], a[212:215], v[60:63]\n"
"ds_read_b64_tr_b16 a[64:65], v9 offset:32768\n"
"s_lshr_b32 s81, s74, 1\n"
"v_mfma_f32_16x16x32_bf16 v[60:63], a[92:95], a[228:231], v[60:63]\n"
"buffer_load_dwordx4 v[172:175], v17, s[44:47], s80 offen offset:1024 sc1\n"
"v_mfma_f32_16x16x32_bf16 v[44:47], a[92:95], a[224:227], v[44:47]\n"
"ds_read_b64_tr_b16 a[66:67], v9 offset:36864\n"
"s_lshl_b32 s81, s81, 8\n"
"v_mfma_f32_16x16x32_bf16 v[44:47], a[88:91], a[208:211], v[44:47]\n"
"s_add_u32 s81, s81, 12288\n"
"s_add_u32 s80, s80, 0x20000\n"
"v_mfma_f32_16x16x32_bf16 a[60:63], a[88:91], a[204:207], a[60:63]\n"
"ds_read_b64_tr_b16 a[68:69], v9 offset:40960\n"
"v_mov_b32 v30, s99\n"
"v_mfma_f32_16x16x32_bf16 a[60:63], a[92:95], a[220:223], a[60:63]\n"
"buffer_load_dwordx4 v[176:179], v16, s[44:47], s80 offen offset:0 sc1\n"
"v_mfma_f32_16x16x32_bf16 a[28:31], a[92:95], a[216:219], a[28:31]\n"
"ds_read_b64_tr_b16 a[70:71], v9 offset:45056\n"
"v_mov_b32 v31, 1\n"
"v_mfma_f32_16x16x32_bf16 a[28:31], a[88:91], a[200:203], a[28:31]\n"
"s_movk_i32 s82, 16\n"
"s_waitcnt lgkmcnt(4)\n"
"v_mfma_f32_16x16x32_bf16 a[0:3], a[96:99], a[104:107], a[0:3]\n"
"ds_read_b64_tr_b16 a[72:73], v10 offset:32768\n"
"s_movk_i32 s83, 32\n"
"v_mfma_f32_16x16x32_bf16 a[0:3], a[100:103], a[120:123], a[0:3]\n"
"buffer_load_dwordx4 v[180:183], v16, s[44:47], s80 offen offset:1024 sc1\n"
"v_mfma_f32_16x16x32_bf16 a[32:35], a[100:103], a[124:127], a[32:35]\n"
"ds_read_b64_tr_b16 a[74:75], v10 offset:36864\n"
"v_writelane_b32 v30, s97, 0\n"
"v_mfma_f32_16x16x32_bf16 a[32:35], a[96:99], a[108:111], a[32:35]\n"
"v_writelane_b32 v30, s81, 1\n"
"v_mfma_f32_16x16x32_bf16 v[64:67], a[96:99], a[112:115], v[64:67]\n"
"ds_read_b64_tr_b16 a[76:77], v10 offset:40960\n"
"v_writelane_b32 v31, s82, 0\n"
"v_mfma_f32_16x16x32_bf16 v[64:67], a[100:103], a[128:131], v[64:67]\n"
"buffer_load_dwordx4 v[184:187], v17, s[44:47], s80 offen offset:0 sc1\n"
"v_mfma_f32_16x16x32_bf16 v[80:83], a[100:103], a[132:135], v[80:83]\n"
"ds_read_b64_tr_b16 a[78:79], v10 offset:45056\n"
"v_writelane_b32 v31, s83, 1\n"
"v_mfma_f32_16x16x32_bf16 v[80:83], a[96:99], a[116:119], v[80:83]\n"
"v_accvgpr_write_b32 a234, v30\n"
"s_waitcnt lgkmcnt(4)\n"
"v_mfma_f32_16x16x32_bf16 v[84:87], a[64:67], a[116:119], v[84:87]\n"
"ds_read_b64_tr_b16 a[80:81], v11 offset:32768\n"
"v_mfma_f32_16x16x32_bf16 v[84:87], a[68:71], a[132:135], v[84:87]\n"
"buffer_load_dwordx4 v[188:191], v17, s[44:47], s80 offen offset:1024 sc1\n"
"v_mfma_f32_16x16x32_bf16 v[68:71], a[68:71], a[128:131], v[68:71]\n"
"ds_read_b64_tr_b16 a[82:83], v11 offset:36864\n"
"v_mfma_f32_16x16x32_bf16 v[68:71], a[64:67], a[112:115], v[68:71]\n"
"v_mfma_f32_16x16x32_bf16 a[36:39], a[64:67], a[108:111], a[36:39]\n"
"ds_read_b64_tr_b16 a[84:85], v11 offset:40960\n"
"s_add_u32 m0, s77, 0x4000\n"
"v_mfma_f32_16x16x32_bf16 a[36:39], a[68:71], a[124:127], a[36:39]\n"
"s_bfe_u32 s79, s10, 0x60010\n"
"s_lshl_b32 s79, s79, 15\n"
"v_mfma_f32_16x16x32_bf16 a[4:7], a[68:71], a[120:123], a[4:7]\n"
"ds_read_b64_tr_b16 a[86:87], v11 offset:45056\n"
"v_mfma_f32_16x16x32_bf16 a[4:7], a[64:67], a[104:107], a[4:7]\n"
"ds_read_b64_tr_b16 a[88:89], v12 offset:32768\n"
"s_waitcnt lgkmcnt(5)\n"
"v_mfma_f32_16x16x32_bf16 a[8:11], a[72:75], a[104:107], a[8:11]\n"
"ds_read_b64_tr_b16 a[90:91], v12 offset:36864\n"
"v_mfma_f32_16x16x32_bf16 a[8:11], a[76:79], a[120:123], a[8:11]\n"
"buffer_load_dwordx4 v4, s[48:51], s79 offen offset:0 lds\n"
"v_mfma_f32_16x16x32_bf16 a[40:43], a[76:79], a[124:127], a[40:43]\n"
"ds_read_b64_tr_b16 a[92:93], v12 offset:40960\n"
"v_mfma_f32_16x16x32_bf16 a[40:43], a[72:75], a[108:111], a[40:43]\n"
"ds_read_b64_tr_b16 a[94:95], v12 offset:45056\n"
"v_mfma_f32_16x16x32_bf16 v[72:75], a[72:75], a[112:115], v[72:75]\n"
"ds_read_b64_tr_b16 a[96:97], v13 offset:32768\n"
"v_mfma_f32_16x16x32_bf16 v[72:75], a[76:79], a[128:131], v[72:75]\n"
"buffer_load_dwordx4 v5, s[48:51], s79 offen offset:1024 lds\n"
"v_mfma_f32_16x16x32_bf16 v[88:91], a[76:79], a[132:135], v[88:91]\n"
"ds_read_b64_tr_b16 a[98:99], v13 offset:36864\n"
"v_mfma_f32_16x16x32_bf16 v[88:91], a[72:75], a[116:119], v[88:91]\n"
"ds_read_b64_tr_b16 a[100:101], v13 offset:40960\n"
"s_waitcnt lgkmcnt(7)\n"
"v_mfma_f32_16x16x32_bf16 v[92:95], a[80:83], a[116:119], v[92:95]\n"
"ds_read_b64_tr_b16 a[102:103], v13 offset:45056\n"
"v_mfma_f32_16x16x32_bf16 v[92:95], a[84:87], a[132:135], v[92:95]\n"
"buffer_load_dwordx4 v6, s[48:51], s79 offen offset:2048 lds\n"
"v_mfma_f32_16x16x32_bf16 v[76:79], a[84:87], a[128:131], v[76:79]\n"
"ds_read_b64_tr_b16 a[64:65], v14 offset:32768\n"
"v_mfma_f32_16x16x32_bf16 v[76:79], a[80:83], a[112:115], v[76:79]\n"
"ds_read_b64_tr_b16 a[66:67], v14 offset:36864\n"
"v_mfma_f32_16x16x32_bf16 a[44:47], a[80:83], a[108:111], a[44:47]\n"
"ds_read_b64_tr_b16 a[68:69], v14 offset:40960\n"
"v_mfma_f32_16x16x32_bf16 a[44:47], a[84:87], a[124:127], a[44:47]\n"
"buffer_load_dwordx4 v7, s[48:51], s79 offen offset:3072 lds\n"
"v_mfma_f32_16x16x32_bf16 a[12:15], a[84:87], a[120:123], a[12:15]\n"
"ds_read_b64_tr_b16 a[70:71], v14 offset:45056\n"
"s_add_u32 m0, s77, 0x6000\n"
"v_mfma_f32_16x16x32_bf16 a[12:15], a[80:83], a[104:107], a[12:15]\n"
"ds_read_b64_tr_b16 a[72:73], v15 offset:32768\n"
"s_waitcnt lgkmcnt(9)\n"
"v_mfma_f32_16x16x32_bf16 a[16:19], a[88:91], a[104:107], a[16:19]\n"
"ds_read_b64_tr_b16 a[74:75], v15 offset:36864\n"
"s_bfe_u32 s79, s27, 0x60010\n"
"v_mfma_f32_16x16x32_bf16 a[16:19], a[92:95], a[120:123], a[16:19]\n"
"s_lshl_b32 s79, s79, 15\n"
"v_mfma_f32_16x16x32_bf16 a[48:51], a[92:95], a[124:127], a[48:51]\n"
"ds_read_b64_tr_b16 a[76:77], v15 offset:40960\n"
"v_mfma_f32_16x16x32_bf16 a[48:51], a[88:91], a[108:111], a[48:51]\n"
"ds_read_b64_tr_b16 a[78:79], v15 offset:45056\n"
"v_mfma_f32_16x16x32_bf16 v[32:35], a[88:91], a[112:115], v[32:35]\n"
"buffer_load_dwordx4 v4, s[48:51], s79 offen offset:0 lds\n"
"v_mfma_f32_16x16x32_bf16 v[32:35], a[92:95], a[128:131], v[32:35]\n"
"v_mfma_f32_16x16x32_bf16 v[48:51], a[92:95], a[132:135], v[48:51]\n"
"v_mfma_f32_16x16x32_bf16 v[48:51], a[88:91], a[116:119], v[48:51]\n"
"s_waitcnt lgkmcnt(8)\n"
"v_mfma_f32_16x16x32_bf16 v[52:55], a[96:99], a[116:119], v[52:55]\n"
"buffer_load_dwordx4 v5, s[48:51], s79 offen offset:1024 lds\n"
"v_mfma_f32_16x16x32_bf16 v[52:55], a[100:103], a[132:135], v[52:55]\n"
"v_mfma_f32_16x16x32_bf16 v[36:39], a[100:103], a[128:131], v[36:39]\n"
"v_mfma_f32_16x16x32_bf16 v[36:39], a[96:99], a[112:115], v[36:39]\n"
"buffer_load_dwordx4 v6, s[48:51], s79 offen offset:2048 lds\n"
"v_mfma_f32_16x16x32_bf16 a[52:55], a[96:99], a[108:111], a[52:55]\n"
"v_mfma_f32_16x16x32_bf16 a[52:55], a[100:103], a[124:127], a[52:55]\n"
"v_mfma_f32_16x16x32_bf16 a[20:23], a[100:103], a[120:123], a[20:23]\n"
"buffer_load_dwordx4 v7, s[48:51], s79 offen offset:3072 lds\n"
"v_mfma_f32_16x16x32_bf16 a[20:23], a[96:99], a[104:107], a[20:23]\n"
"s_waitcnt vmcnt(16) lgkmcnt(0)\n"
"s_barrier\n"
"v_mfma_f32_16x16x32_bf16 a[24:27], a[64:67], a[104:107], a[24:27]\n"
"ds_read_b64_tr_b16 a[80:81], v8 offset:0\n"
"v_mfma_f32_16x16x32_bf16 a[24:27], a[68:71], a[120:123], a[24:27]\n"
"s_add_u32 m0, s77, 0x8000\n"
"s_bfe_u32 s79, s10, 0x60018\n"
"v_mfma_f32_16x16x32_bf16 a[56:59], a[68:71], a[124:127], a[56:59]\n"
"ds_read_b64_tr_b16 a[82:83], v8 offset:4096\n"
"s_lshl_b32 s79, s79, 15\n"
"v_mfma_f32_16x16x32_bf16 a[56:59], a[64:67], a[108:111], a[56:59]\n"
"buffer_load_dwordx4 v4, s[48:51], s79 offen offset:0 lds\n"
"v_mfma_f32_16x16x32_bf16 v[40:43], a[64:67], a[112:115], v[40:43]\n"
"ds_read_b64_tr_b16 a[84:85], v8 offset:8192\n"
"v_mfma_f32_16x16x32_bf16 v[40:43], a[68:71], a[128:131], v[40:43]\n"
"v_mfma_f32_16x16x32_bf16 v[56:59], a[68:71], a[132:135], v[56:59]\n"
"ds_read_b64_tr_b16 a[86:87], v8 offset:12288\n"
"v_mfma_f32_16x16x32_bf16 v[56:59], a[64:67], a[116:119], v[56:59]\n"
"v_mfma_f32_16x16x32_bf16 v[60:63], a[72:75], a[116:119], v[60:63]\n"
"ds_read_b64_tr_b16 a[88:89], v9 offset:0\n"
"v_mfma_f32_16x16x32_bf16 v[60:63], a[76:79], a[132:135], v[60:63]\n"
"buffer_load_dwordx4 v5, s[48:51], s79 offen offset:1024 lds\n"
"v_mfma_f32_16x16x32_bf16 v[44:47], a[76:79], a[128:131], v[44:47]\n"
"ds_read_b64_tr_b16 a[90:91], v9 offset:4096\n"
"v_mfma_f32_16x16x32_bf16 v[44:47], a[72:75], a[112:115], v[44:47]\n"
"v_mfma_f32_16x16x32_bf16 a[60:63], a[72:75], a[108:111], a[60:63]\n"
"ds_read_b64_tr_b16 a[92:93], v9 offset:8192\n"
"v_mfma_f32_16x16x32_bf16 a[60:63], a[76:79], a[124:127], a[60:63]\n"
"buffer_load_dwordx4 v6, s[48:51], s79 offen offset:2048 lds\n"
"v_mfma_f32_16x16x32_bf16 a[28:31], a[76:79], a[120:123], a[28:31]\n"
"ds_read_b64_tr_b16 a[94:95], v9 offset:12288\n"
"v_mfma_f32_16x16x32_bf16 a[28:31], a[72:75], a[104:107], a[28:31]\n"
"s_waitcnt lgkmcnt(4)\n"
"v_mfma_f32_16x16x32_bf16 a[0:3], a[80:83], v[96:99], a[0:3]\n"
"ds_read_b64_tr_b16 a[96:97], v10 offset:0\n"
"v_mfma_f32_16x16x32_bf16 a[0:3], a[84:87], v[112:115], a[0:3]\n"
"buffer_load_dwordx4 v7, s[48:51], s79 offen offset:3072 lds\n"
"v_mfma_f32_16x16x32_bf16 a[32:35], a[84:87], v[116:119], a[32:35]\n"
"ds_read_b64_tr_b16 a[98:99], v10 offset:4096\n"
"s_add_u32 m0, s77, 0xa000\n"
"v_mfma_f32_16x16x32_bf16 a[32:35], a[80:83], v[100:103], a[32:35]\n"
"s_bfe_u32 s79, s27, 0x60018\n"
"s_lshl_b32 s79, s79, 15\n"
"v_mfma_f32_16x16x32_bf16 v[64:67], a[80:83], v[104:107], v[64:67]\n"
"ds_read_b64_tr_b16 a[100:101], v10 offset:8192\n"
"v_mfma_f32_16x16x32_bf16 v[64:67], a[84:87], v[120:123], v[64:67]\n"
"buffer_load_dwordx4 v4, s[48:51], s79 offen offset:0 lds\n"
"v_mfma_f32_16x16x32_bf16 v[80:83], a[84:87], v[124:127], v[80:83]\n"
"ds_read_b64_tr_b16 a[102:103], v10 offset:12288\n"
"v_mfma_f32_16x16x32_bf16 v[80:83], a[80:83], v[108:111], v[80:83]\n"
"s_waitcnt lgkmcnt(4)\n"
"v_mfma_f32_16x16x32_bf16 v[84:87], a[88:91], v[108:111], v[84:87]\n"
"ds_read_b64_tr_b16 a[64:65], v11 offset:0\n"
"v_mfma_f32_16x16x32_bf16 v[84:87], a[92:95], v[124:127], v[84:87]\n"
"buffer_load_dwordx4 v5, s[48:51], s79 offen offset:1024 lds\n"
"v_mfma_f32_16x16x32_bf16 v[68:71], a[92:95], v[120:123], v[68:71]\n"
"ds_read_b64_tr_b16 a[66:67], v11 offset:4096\n"
"v_mfma_f32_16x16x32_bf16 v[68:71], a[88:91], v[104:107], v[68:71]\n"
"v_mfma_f32_16x16x32_bf16 a[36:39], a[88:91], v[100:103], a[36:39]\n"
"ds_read_b64_tr_b16 a[68:69], v11 offset:8192\n"
"v_mfma_f32_16x16x32_bf16 a[36:39], a[92:95], v[116:119], a[36:39]\n"
"buffer_load_dwordx4 v6, s[48:51], s79 offen offset:2048 lds\n"
"v_mfma_f32_16x16x32_bf16 a[4:7], a[92:95], v[112:115], a[4:7]\n"
"ds_read_b64_tr_b16 a[70:71], v11 offset:12288\n"
"v_mfma_f32_16x16x32_bf16 a[4:7], a[88:91], v[96:99], a[4:7]\n"
"ds_read_b64_tr_b16 a[72:73], v12 offset:0\n"
"s_waitcnt lgkmcnt(5)\n"
"v_mfma_f32_16x16x32_bf16 a[8:11], a[96:99], v[96:99], a[8:11]\n"
"ds_read_b64_tr_b16 a[74:75], v12 offset:4096\n"
"v_mfma_f32_16x16x32_bf16 a[8:11], a[100:103], v[112:115], a[8:11]\n"
"buffer_load_dwordx4 v7, s[48:51], s79 offen offset:3072 lds\n"
"v_mfma_f32_16x16x32_bf16 a[40:43], a[100:103], v[116:119], a[40:43]\n"
"ds_read_b64_tr_b16 a[76:77], v12 offset:8192\n"
"v_mfma_f32_16x16x32_bf16 a[40:43], a[96:99], v[100:103], a[40:43]\n"
"ds_read_b64_tr_b16 a[78:79], v12 offset:12288\n"
"v_mfma_f32_16x16x32_bf16 v[72:75], a[96:99], v[104:107], v[72:75]\n"
"ds_read_b64_tr_b16 a[80:81], v13 offset:0\n"
"v_mfma_f32_16x16x32_bf16 v[72:75], a[100:103], v[120:123], v[72:75]\n"
"buffer_load_dword v30, v30, s[64:67], 0 offen sc1\n"
"v_mfma_f32_16x16x32_bf16 v[88:91], a[100:103], v[124:127], v[88:91]\n"
"ds_read_b64_tr_b16 a[82:83], v13 offset:4096\n"
"v_mfma_f32_16x16x32_bf16 v[88:91], a[96:99], v[108:111], v[88:91]\n"
"ds_read_b64_tr_b16 a[84:85], v13 offset:8192\n"
"s_waitcnt lgkmcnt(7)\n"
"v_mfma_f32_16x16x32_bf16 v[92:95], a[64:67], v[108:111], v[92:95]\n"
"ds_read_b64_tr_b16 a[86:87], v13 offset:12288\n"
"s_xor_b32 s80, s87, 0\n"
"v_mfma_f32_16x16x32_bf16 v[92:95], a[68:71], v[124:127], v[92:95]\n"
"s_mul_i32 s80, s80, 0x1c0000\n"
"s_add_u32 s80, s80, 0xc0000\n"
"v_mfma_f32_16x16x32_bf16 v[76:79], a[68:71], v[120:123], v[76:79]\n"
"ds_read_b64_tr_b16 a[88:89], v14 offset:0\n"
"v_mfma_f32_16x16x32_bf16 v[76:79], a[64:67], v[104:107], v[76:79]\n"
"ds_read_b64_tr_b16 a[90:91], v14 offset:4096\n"
"v_mfma_f32_16x16x32_bf16 a[44:47], a[64:67], v[100:103], a[44:47]\n"
"ds_read_b64_tr_b16 a[92:93], v14 offset:8192\n"
"v_mfma_f32_16x16x32_bf16 a[44:47], a[68:71], v[116:119], a[44:47]\n"
"buffer_load_dwordx2 v[192:193], v18, s[44:47], s80 offen offset:0 sc1\n"
"v_mfma_f32_16x16x32_bf16 a[12:15], a[68:71], v[112:115], a[12:15]\n"
"ds_read_b64_tr_b16 a[94:95], v14 offset:12288\n"
"v_mfma_f32_16x16x32_bf16 a[12:15], a[64:67], v[96:99], a[12:15]\n"
"ds_read_b64_tr_b16 a[96:97], v15 offset:0\n"
"s_waitcnt lgkmcnt(9)\n"
"v_mfma_f32_16x16x32_bf16 a[16:19], a[72:75], v[96:99], a[16:19]\n"
"ds_read_b64_tr_b16 a[98:99], v15 offset:4096\n"
"v_mfma_f32_16x16x32_bf16 a[16:19], a[76:79], v[112:115], a[16:19]\n"
"buffer_load_dwordx2 v[194:195], v18, s[44:47], s80 offen offset:512 sc1\n"
"v_mfma_f32_16x16x32_bf16 a[48:51], a[76:79], v[116:119], a[48:51]\n"
"ds_read_b64_tr_b16 a[100:101], v15 offset:8192\n"
"v_mfma_f32_16x16x32_bf16 a[48:51], a[72:75], v[100:103], a[48:51]\n"
"ds_read_b64_tr_b16 a[102:103], v15 offset:12288\n"
"v_mfma_f32_16x16x32_bf16 v[32:35], a[72:75], v[104:107], v[32:35]\n"
"buffer_load_dwordx2 v[196:197], v18, s[44:47], s80 offen offset:1024 sc1\n"
"v_mfma_f32_16x16x32_bf16 v[32:35], a[76:79], v[120:123], v[32:35]\n"
"buffer_load_dwordx2 v[198:199], v18, s[44:47], s80 offen offset:1536 sc1\n"
"v_mfma_f32_16x16x32_bf16 v[48:51], a[76:79], v[124:127], v[48:51]\n"
"buffer_load_dwordx2 v[200:201], v19, s[44:47], s80 offen offset:0 sc1\n"
"v_mfma_f32_16x16x32_bf16 v[48:51], a[72:75], v[108:111], v[48:51]\n"
"s_waitcnt lgkmcnt(8)\n"
"v_mfma_f32_16x16x32_bf16 v[52:55], a[80:83], v[108:111], v[52:55]\n"
"buffer_load_dwordx2 v[202:203], v19, s[44:47], s80 offen offset:512 sc1\n"
"v_mfma_f32_16x16x32_bf16 v[52:55], a[84:87], v[124:127], v[52:55]\n"
"buffer_load_dwordx2 v[204:205], v19, s[44:47], s80 offen offset:1024 sc1\n"
"v_mfma_f32_16x16x32_bf16 v[36:39], a[84:87], v[120:123], v[36:39]\n"
"buffer_load_dwordx2 v[206:207], v19, s[44:47], s80 offen offset:1536 sc1\n"
"v_mfma_f32_16x16x32_bf16 v[36:39], a[80:83], v[104:107], v[36:39]\n"
"s_add_u32 s80, s80, 0xe0000\n"
"v_mfma_f32_16x16x32_bf16 a[52:55], a[80:83], v[100:103], a[52:55]\n"
"buffer_load_dwordx2 v[208:209], v18, s[44:47], s80 offen offset:0 sc1\n"
"v_mfma_f32_16x16x32_bf16 a[52:55], a[84:87], v[116:119], a[52:55]\n"
"buffer_load_dwordx2 v[210:211], v18, s[44:47], s80 offen offset:512 sc1\n"
"v_mfma_f32_16x16x32_bf16 a[20:23], a[84:87], v[112:115], a[20:23]\n"
"buffer_load_dwordx2 v[212:213], v18, s[44:47], s80 offen offset:1024 sc1\n"
"v_mfma_f32_16x16x32_bf16 a[20:23], a[80:83], v[96:99], a[20:23]\n"
"s_waitcnt vmcnt(20) lgkmcnt(0)\n"
"s_barrier\n"
"v_mfma_f32_16x16x32_bf16 a[24:27], a[88:91], v[96:99], a[24:27]\n"
"ds_read_b64_tr_b16 a[64:65], v8 offset:16384\n"
"v_mfma_f32_16x16x32_bf16 a[24:27], a[92:95], v[112:115], a[24:27]\n"
"buffer_load_dwordx2 v[214:215], v18, s[44:47], s80 offen offset:1536 sc1\n"
"v_mfma_f32_16x16x32_bf16 a[56:59], a[92:95], v[116:119], a[56:59]\n"
"ds_read_b64_tr_b16 a[66:67], v8 offset:20480\n"
"v_mfma_f32_16x16x32_bf16 a[56:59], a[88:91], v[100:103], a[56:59]\n"
"buffer_load_dwordx2 v[216:217], v19, s[44:47], s80 offen offset:0 sc1\n"
"v_mfma_f32_16x16x32_bf16 v[40:43], a[88:91], v[104:107], v[40:43]\n"
"ds_read_b64_tr_b16 a[68:69], v8 offset:24576\n"
"v_mfma_f32_16x16x32_bf16 v[40:43], a[92:95], v[120:123], v[40:43]\n"
"buffer_load_dwordx2 v[218:219], v19, s[44:47], s80 offen offset:512 sc1\n"
"v_mfma_f32_16x16x32_bf16 v[56:59], a[92:95], v[124:127], v[56:59]\n"
"ds_read_b64_tr_b16 a[70:71], v8 offset:28672\n"
"v_mfma_f32_16x16x32_bf16 v[56:59], a[88:91], v[108:111], v[56:59]\n"
"v_mfma_f32_16x16x32_bf16 v[60:63], a[96:99], v[108:111], v[60:63]\n"
"ds_read_b64_tr_b16 a[72:73], v9 offset:16384\n"
"v_mfma_f32_16x16x32_bf16 v[60:63], a[100:103], v[124:127], v[60:63]\n"
"buffer_load_dwordx2 v[220:221], v19, s[44:47], s80 offen offset:1024 sc1\n"
"v_mfma_f32_16x16x32_bf16 v[44:47], a[100:103], v[120:123], v[44:47]\n"
"ds_read_b64_tr_b16 a[74:75], v9 offset:20480\n"
"v_mfma_f32_16x16x32_bf16 v[44:47], a[96:99], v[104:107], v[44:47]\n"
"buffer_load_dwordx2 v[222:223], v19, s[44:47], s80 offen offset:1536 sc1\n"
"v_mfma_f32_16x16x32_bf16 a[60:63], a[96:99], v[100:103], a[60:63]\n"
"ds_read_b64_tr_b16 a[76:77], v9 offset:24576\n"
"v_mfma_f32_16x16x32_bf16 a[60:63], a[100:103], v[116:119], a[60:63]\n"
"v_mfma_f32_16x16x32_bf16 a[28:31], a[100:103], v[112:115], a[28:31]\n"
"ds_read_b64_tr_b16 a[78:79], v9 offset:28672\n"
"s_add_u32 m0, s77, 0x0\n"
"v_mfma_f32_16x16x32_bf16 a[28:31], a[96:99], v[96:99], a[28:31]\n"
"s_xor_b32 s79, s87, 0\n"
"s_waitcnt lgkmcnt(4)\n"
"v_mfma_f32_16x16x32_bf16 a[0:3], a[64:67], v[128:131], a[0:3]\n"
"ds_read_b64_tr_b16 a[80:81], v10 offset:16384\n"
"s_mul_i32 s79, s79, 0xe0000\n"
"v_mfma_f32_16x16x32_bf16 a[0:3], a[68:71], v[144:147], a[0:3]\n"
"s_add_u32 s79, s79, s17\n"
"v_mfma_f32_16x16x32_bf16 a[32:35], a[68:71], v[148:151], a[32:35]\n"
"ds_read_b64_tr_b16 a[82:83], v10 offset:20480\n"
"v_mfma_f32_16x16x32_bf16 a[32:35], a[64:67], v[132:135], a[32:35]\n"
"buffer_load_dwordx4 v4, s[48:51], s79 offen offset:0 lds\n"
"v_mfma_f32_16x16x32_bf16 v[64:67], a[64:67], v[136:139], v[64:67]\n"
"ds_read_b64_tr_b16 a[84:85], v10 offset:24576\n"
"v_mfma_f32_16x16x32_bf16 v[64:67], a[68:71], v[152:155], v[64:67]\n"
"v_mfma_f32_16x16x32_bf16 v[80:83], a[68:71], v[156:159], v[80:83]\n"
"ds_read_b64_tr_b16 a[86:87], v10 offset:28672\n"
"v_mfma_f32_16x16x32_bf16 v[80:83], a[64:67], v[140:143], v[80:83]\n"
"s_waitcnt lgkmcnt(4)\n"
"v_mfma_f32_16x16x32_bf16 v[84:87], a[72:75], v[140:143], v[84:87]\n"
"ds_read_b64_tr_b16 a[88:89], v11 offset:16384\n"
"v_mfma_f32_16x16x32_bf16 v[84:87], a[76:79], v[156:159], v[84:87]\n"
"buffer_load_dwordx4 v5, s[48:51], s79 offen offset:1024 lds\n"
"v_mfma_f32_16x16x32_bf16 v[68:71], a[76:79], v[152:155], v[68:71]\n"
"ds_read_b64_tr_b16 a[90:91], v11 offset:20480\n"
"v_mfma_f32_16x16x32_bf16 v[68:71], a[72:75], v[136:139], v[68:71]\n"
"v_mfma_f32_16x16x32_bf16 a[36:39], a[72:75], v[132:135], a[36:39]\n"
"ds_read_b64_tr_b16 a[92:93], v11 offset:24576\n"
"v_mfma_f32_16x16x32_bf16 a[36:39], a[76:79], v[148:151], a[36:39]\n"
"buffer_load_dwordx4 v6, s[48:51], s79 offen offset:2048 lds\n"
"v_mfma_f32_16x16x32_bf16 a[4:7], a[76:79], v[144:147], a[4:7]\n"
"ds_read_b64_tr_b16 a[94:95], v11 offset:28672\n"
"v_mfma_f32_16x16x32_bf16 a[4:7], a[72:75], v[128:131], a[4:7]\n"
"ds_read_b64_tr_b16 a[96:97], v12 offset:16384\n"
"s_waitcnt lgkmcnt(5)\n"
"v_mfma_f32_16x16x32_bf16 a[8:11], a[80:83], v[128:131], a[8:11]\n"
"ds_read_b64_tr_b16 a[98:99], v12 offset:20480\n"
"v_mfma_f32_16x16x32_bf16 a[8:11], a[84:87], v[144:147], a[8:11]\n"
"buffer_load_dwordx4 v7, s[48:51], s79 offen offset:3072 lds\n"
"v_mfma_f32_16x16x32_bf16 a[40:43], a[84:87], v[148:151], a[40:43]\n"
"ds_read_b64_tr_b16 a[100:101], v12 offset:24576\n"
"s_add_u32 m0, s77, 0x2000\n"
"v_mfma_f32_16x16x32_bf16 a[40:43], a[80:83], v[132:135], a[40:43]\n"
"ds_read_b64_tr_b16 a[102:103], v12 offset:28672\n"
"s_add_u32 s79, s79, 0x70000\n"
"v_mfma_f32_16x16x32_bf16 v[72:75], a[80:83], v[136:139], v[72:75]\n"
"ds_read_b64_tr_b16 a[64:65], v13 offset:16384\n"
"v_mfma_f32_16x16x32_bf16 v[72:75], a[84:87], v[152:155], v[72:75]\n"
"buffer_load_dwordx4 v4, s[48:51], s79 offen offset:0 lds\n"
"v_mfma_f32_16x16x32_bf16 v[88:91], a[84:87], v[156:159], v[88:91]\n"
"ds_read_b64_tr_b16 a[66:67], v13 offset:20480\n"
"v_mfma_f32_16x16x32_bf16 v[88:91], a[80:83], v[140:143], v[88:91]\n"
"ds_read_b64_tr_b16 a[68:69], v13 offset:24576\n"
"s_waitcnt lgkmcnt(7)\n"
"v_mfma_f32_16x16x32_bf16 v[92:95], a[88:91], v[140:143], v[92:95]\n"
"ds_read_b64_tr_b16 a[70:71], v13 offset:28672\n"
"v_mfma_f32_16x16x32_bf16 v[92:95], a[92:95], v[156:159], v[92:95]\n"
"buffer_load_dwordx4 v5, s[48:51], s79 offen offset:1024 lds\n"
"v_mfma_f32_16x16x32_bf16 v[76:79], a[92:95], v[152:155], v[76:79]\n"
"ds_read_b64_tr_b16 a[72:73], v14 offset:16384\n"
"v_mfma_f32_16x16x32_bf16 v[76:79], a[88:91], v[136:139], v[76:79]\n"
"ds_read_b64_tr_b16 a[74:75], v14 offset:20480\n"
"v_mfma_f32_16x16x32_bf16 a[44:47], a[88:91], v[132:135], a[44:47]\n"
"ds_read_b64_tr_b16 a[76:77], v14 offset:24576\n"
"v_mfma_f32_16x16x32_bf16 a[44:47], a[92:95], v[148:151], a[44:47]\n"
"buffer_load_dwordx4 v6, s[48:51], s79 offen offset:2048 lds\n"
"v_mfma_f32_16x16x32_bf16 a[12:15], a[92:95], v[144:147], a[12:15]\n"
"ds_read_b64_tr_b16 a[78:79], v14 offset:28672\n"
"v_mfma_f32_16x16x32_bf16 a[12:15], a[88:91], v[128:131], a[12:15]\n"
"ds_read_b64_tr_b16 a[80:81], v15 offset:16384\n"
"s_waitcnt lgkmcnt(9)\n"
"v_mfma_f32_16x16x32_bf16 a[16:19], a[96:99], v[128:131], a[16:19]\n"
"ds_read_b64_tr_b16 a[82:83], v15 offset:20480\n"
"v_mfma_f32_16x16x32_bf16 a[16:19], a[100:103], v[144:147], a[16:19]\n"
"buffer_load_dwordx4 v7, s[48:51], s79 offen offset:3072 lds\n"
"v_mfma_f32_16x16x32_bf16 a[48:51], a[100:103], v[148:151], a[48:51]\n"
"ds_read_b64_tr_b16 a[84:85], v15 offset:24576\n"
"v_mfma_f32_16x16x32_bf16 a[48:51], a[96:99], v[132:135], a[48:51]\n"
"ds_read_b64_tr_b16 a[86:87], v15 offset:28672\n"
"v_mfma_f32_16x16x32_bf16 v[32:35], a[96:99], v[136:139], v[32:35]\n"
"s_xor_b32 s80, s87, 1\n"
"s_mul_i32 s80, s80, 0x1c0000\n"
"v_mfma_f32_16x16x32_bf16 v[32:35], a[100:103], v[152:155], v[32:35]\n"
".Lfk_hop7433:\n"
"s_cbranch_execz .Lfk_hop7993\n"
"s_add_u32 s80, s80, 0xc0000\n"
"v_mfma_f32_16x16x32_bf16 v[48:51], a[100:103], v[156:159], v[48:51]\n"
"buffer_load_dwordx2 v[224:225], v18, s[44:47], s80 offen offset:0 sc1\n"
"v_mfma_f32_16x16x32_bf16 v[48:51], a[96:99], v[140:143], v[48:51]\n"
"s_waitcnt lgkmcnt(8)\n"
"v_mfma_f32_16x16x32_bf16 v[52:55], a[64:67], v[140:143], v[52:55]\n"
"buffer_load_dwordx2 v[226:227], v18, s[44:47], s80 offen offset:512 sc1\n"
"v_mfma_f32_16x16x32_bf16 v[52:55], a[68:71], v[156:159], v[52:55]\n"
"buffer_load_dwordx2 v[228:229], v18, s[44:47], s80 offen offset:1024 sc1\n"
"v_mfma_f32_16x16x32_bf16 v[36:39], a[68:71], v[152:155], v[36:39]\n"
"buffer_load_dwordx2 v[230:231], v18, s[44:47], s80 offen offset:1536 sc1\n"
"v_mfma_f32_16x16x32_bf16 v[36:39], a[64:67], v[136:139], v[36:39]\n"
"buffer_load_dwordx2 v[232:233], v19, s[44:47], s80 offen offset:0 sc1\n"
"v_mfma_f32_16x16x32_bf16 a[52:55], a[64:67], v[132:135], a[52:55]\n"
"buffer_load_dwordx2 v[234:235], v19, s[44:47], s80 offen offset:512 sc1\n"
"v_mfma_f32_16x16x32_bf16 a[52:55], a[68:71], v[148:151], a[52:55]\n"
"buffer_load_dwordx2 v[236:237], v19, s[44:47], s80 offen offset:1024 sc1\n"
"v_mfma_f32_16x16x32_bf16 a[20:23], a[68:71], v[144:147], a[20:23]\n"
"buffer_load_dwordx2 v[238:239], v19, s[44:47], s80 offen offset:1536 sc1\n"
"v_mfma_f32_16x16x32_bf16 a[20:23], a[64:67], v[128:131], a[20:23]\n"
"s_waitcnt vmcnt(33) lgkmcnt(0)\n"
"s_barrier\n"
"v_mfma_f32_16x16x32_bf16 a[24:27], a[72:75], v[128:131], a[24:27]\n"
"ds_read_b64_tr_b16 a[88:89], v8 offset:32768\n"
"s_add_u32 s80, s80, 0xe0000\n"
"v_mfma_f32_16x16x32_bf16 a[24:27], a[76:79], v[144:147], a[24:27]\n"
"buffer_load_dwordx2 v[240:241], v18, s[44:47], s80 offen offset:0 sc1\n"
"v_mfma_f32_16x16x32_bf16 a[56:59], a[76:79], v[148:151], a[56:59]\n"
"ds_read_b64_tr_b16 a[90:91], v8 offset:36864\n"
"v_mfma_f32_16x16x32_bf16 a[56:59], a[72:75], v[132:135], a[56:59]\n"
"buffer_load_dwordx2 v[242:243], v18, s[44:47], s80 offen offset:512 sc1\n"
"v_mfma_f32_16x16x32_bf16 v[40:43], a[72:75], v[136:139], v[40:43]\n"
"ds_read_b64_tr_b16 a[92:93], v8 offset:40960\n"
"v_mfma_f32_16x16x32_bf16 v[40:43], a[76:79], v[152:155], v[40:43]\n"
"buffer_load_dwordx2 v[244:245], v18, s[44:47], s80 offen offset:1024 sc1\n"
"v_mfma_f32_16x16x32_bf16 v[56:59], a[76:79], v[156:159], v[56:59]\n"
"ds_read_b64_tr_b16 a[94:95], v8 offset:45056\n"
"v_mfma_f32_16x16x32_bf16 v[56:59], a[72:75], v[140:143], v[56:59]\n"
"v_mfma_f32_16x16x32_bf16 v[60:63], a[80:83], v[140:143], v[60:63]\n"
"ds_read_b64_tr_b16 a[96:97], v9 offset:32768\n"
"v_mfma_f32_16x16x32_bf16 v[60:63], a[84:87], v[156:159], v[60:63]\n"
"buffer_load_dwordx2 v[246:247], v18, s[44:47], s80 offen offset:1536 sc1\n"
"v_mfma_f32_16x16x32_bf16 v[44:47], a[84:87], v[152:155], v[44:47]\n"
"ds_read_b64_tr_b16 a[98:99], v9 offset:36864\n"
"v_mfma_f32_16x16x32_bf16 v[44:47], a[80:83], v[136:139], v[44:47]\n"
"buffer_load_dwordx2 v[248:249], v19, s[44:47], s80 offen offset:0 sc1\n"
"v_mfma_f32_16x16x32_bf16 a[60:63], a[80:83], v[132:135], a[60:63]\n"
"ds_read_b64_tr_b16 a[100:101], v9 offset:40960\n"
"v_mfma_f32_16x16x32_bf16 a[60:63], a[84:87], v[148:151], a[60:63]\n"
"buffer_load_dwordx2 v[250:251], v19, s[44:47], s80 offen offset:512 sc1\n"
"v_mfma_f32_16x16x32_bf16 a[28:31], a[84:87], v[144:147], a[28:31]\n"
"ds_read_b64_tr_b16 a[102:103], v9 offset:45056\n"
"v_mfma_f32_16x16x32_bf16 a[28:31], a[80:83], v[128:131], a[28:31]\n"
"s_waitcnt lgkmcnt(4)\n"
"v_mfma_f32_16x16x32_bf16 a[0:3], a[88:91], v[160:163], a[0:3]\n"
"ds_read_b64_tr_b16 a[64:65], v10 offset:32768\n"
"v_mfma_f32_16x16x32_bf16 a[0:3], a[92:95], v[176:179], a[0:3]\n"
"buffer_load_dwordx2 v[252:253], v19, s[44:47], s80 offen offset:1024 sc1\n"
"v_mfma_f32_16x16x32_bf16 a[32:35], a[92:95], v[180:183], a[32:35]\n"
"ds_read_b64_tr_b16 a[66:67], v10 offset:36864\n"
"v_mfma_f32_16x16x32_bf16 a[32:35], a[88:91], v[164:167], a[32:35]\n"
"buffer_load_dwordx2 v[254:255], v19, s[44:47], s80 offen offset:1536 sc1\n"
"v_mfma_f32_16x16x32_bf16 v[64:67], a[88:91], v[168:171], v[64:67]\n"
"ds_read_b64_tr_b16 a[68:69], v10 offset:40960\n"
"v_mfma_f32_16x16x32_bf16 v[64:67], a[92:95], v[184:187], v[64:67]\n"
"v_mfma_f32_16x16x32_bf16 v[80:83], a[92:95], v[188:191], v[80:83]\n"
"ds_read_b64_tr_b16 a[70:71], v10 offset:45056\n"
"s_add_u32 m0, s77, 0x4000\n"
"v_mfma_f32_16x16x32_bf16 v[80:83], a[88:91], v[172:175], v[80:83]\n"
"s_xor_b32 s79, s87, 1\n"
"s_waitcnt lgkmcnt(4)\n"
"v_mfma_f32_16x16x32_bf16 v[84:87], a[96:99], v[172:175], v[84:87]\n"
"ds_read_b64_tr_b16 a[72:73], v11 offset:32768\n"
"s_mul_i32 s79, s79, 0xe0000\n"
"v_mfma_f32_16x16x32_bf16 v[84:87], a[100:103], v[188:191], v[84:87]\n"
"s_add_u32 s79, s79, s17\n"
"v_mfma_f32_16x16x32_bf16 v[68:71], a[100:103], v[184:187], v[68:71]\n"
"ds_read_b64_tr_b16 a[74:75], v11 offset:36864\n"
"v_mfma_f32_16x16x32_bf16 v[68:71], a[96:99], v[168:171], v[68:71]\n"
"buffer_load_dwordx4 v4, s[48:51], s79 offen offset:0 lds\n"
"v_mfma_f32_16x16x32_bf16 a[36:39], a[96:99], v[164:167], a[36:39]\n"
"ds_read_b64_tr_b16 a[76:77], v11 offset:40960\n"
"v_mfma_f32_16x16x32_bf16 a[36:39], a[100:103], v[180:183], a[36:39]\n"
"v_mfma_f32_16x16x32_bf16 a[4:7], a[100:103], v[176:179], a[4:7]\n"
"ds_read_b64_tr_b16 a[78:79], v11 offset:45056\n"
"v_mfma_f32_16x16x32_bf16 a[4:7], a[96:99], v[160:163], a[4:7]\n"
"ds_read_b64_tr_b16 a[80:81], v12 offset:32768\n"
"s_waitcnt lgkmcnt(5)\n"
"v_mfma_f32_16x16x32_bf16 a[8:11], a[64:67], v[160:163], a[8:11]\n"
"ds_read_b64_tr_b16 a[82:83], v12 offset:36864\n"
"v_mfma_f32_16x16x32_bf16 a[8:11], a[68:71], v[176:179], a[8:11]\n"
"buffer_load_dwordx4 v5, s[48:51], s79 offen offset:1024 lds\n"
"v_mfma_f32_16x16x32_bf16 a[40:43], a[68:71], v[180:183], a[40:43]\n"
"ds_read_b64_tr_b16 a[84:85], v12 offset:40960\n"
"v_mfma_f32_16x16x32_bf16 a[40:43], a[64:67], v[164:167], a[40:43]\n"
"ds_read_b64_tr_b16 a[86:87], v12 offset:45056\n"
"v_mfma_f32_16x16x32_bf16 v[72:75], a[64:67], v[168:171], v[72:75]\n"
"ds_read_b64_tr_b16 a[88:89], v13 offset:32768\n"
"v_mfma_f32_16x16x32_bf16 v[72:75], a[68:71], v[184:187], v[72:75]\n"
"buffer_load_dwordx4 v6, s[48:51], s79 offen offset:2048 lds\n"
"v_mfma_f32_16x16x32_bf16 v[88:91], a[68:71], v[188:191], v[88:91]\n"
"ds_read_b64_tr_b16 a[90:91], v13 offset:36864\n"
"v_mfma_f32_16x16x32_bf16 v[88:91], a[64:67], v[172:175], v[88:91]\n"
"ds_read_b64_tr_b16 a[92:93], v13 offset:40960\n"
"s_waitcnt lgkmcnt(7)\n"
"v_mfma_f32_16x16x32_bf16 v[92:95], a[72:75], v[172:175], v[92:95]\n"
"ds_read_b64_tr_b16 a[94:95], v13 offset:45056\n"
"v_mfma_f32_16x16x32_bf16 v[92:95], a[76:79], v[188:191], v[92:95]\n"
"buffer_load_dwordx4 v7, s[48:51], s79 offen offset:3072 lds\n"
"v_mfma_f32_16x16x32_bf16 v[76:79], a[76:79], v[184:187], v[76:79]\n"
"ds_read_b64_tr_b16 a[96:97], v14 offset:32768\n"
"s_add_u32 m0, s77, 0x6000\n"
"v_mfma_f32_16x16x32_bf16 v[76:79], a[72:75], v[168:171], v[76:79]\n"
"ds_read_b64_tr_b16 a[98:99], v14 offset:36864\n"
"s_add_u32 s79, s79, 0x70000\n"
"v_mfma_f32_16x16x32_bf16 a[44:47], a[72:75], v[164:167], a[44:47]\n"
"ds_read_b64_tr_b16 a[100:101], v14 offset:40960\n"
"v_mfma_f32_16x16x32_bf16 a[44:47], a[76:79], v[180:183], a[44:47]\n"
"buffer_load_dwordx4 v4, s[48:51], s79 offen offset:0 lds\n"
"v_mfma_f32_16x16x32_bf16 a[12:15], a[76:79], v[176:179], a[12:15]\n"
"ds_read_b64_tr_b16 a[102:103], v14 offset:45056\n"
"v_mfma_f32_16x16x32_bf16 a[12:15], a[72:75], v[160:163], a[12:15]\n"
"ds_read_b64_tr_b16 a[64:65], v15 offset:32768\n"
"s_waitcnt lgkmcnt(9)\n"
"v_mfma_f32_16x16x32_bf16 a[16:19], a[80:83], v[160:163], a[16:19]\n"
"ds_read_b64_tr_b16 a[66:67], v15 offset:36864\n"
"v_mfma_f32_16x16x32_bf16 a[16:19], a[84:87], v[176:179], a[16:19]\n"
"buffer_load_dwordx4 v5, s[48:51], s79 offen offset:1024 lds\n"
"v_mfma_f32_16x16x32_bf16 a[48:51], a[84:87], v[180:183], a[48:51]\n"
"ds_read_b64_tr_b16 a[68:69], v15 offset:40960\n"
"v_mfma_f32_16x16x32_bf16 a[48:51], a[80:83], v[164:167], a[48:51]\n"
"ds_read_b64_tr_b16 a[70:71], v15 offset:45056\n"
"v_mfma_f32_16x16x32_bf16 v[32:35], a[80:83], v[168:171], v[32:35]\n"
"buffer_load_dwordx4 v6, s[48:51], s79 offen offset:2048 lds\n"
"v_mfma_f32_16x16x32_bf16 v[32:35], a[84:87], v[184:187], v[32:35]\n"
"v_mfma_f32_16x16x32_bf16 v[48:51], a[84:87], v[188:191], v[48:51]\n"
"v_mfma_f32_16x16x32_bf16 v[48:51], a[80:83], v[172:175], v[48:51]\n"
"s_waitcnt lgkmcnt(8)\n"
"v_mfma_f32_16x16x32_bf16 v[52:55], a[88:91], v[172:175], v[52:55]\n"
"buffer_load_dwordx4 v7, s[48:51], s79 offen offset:3072 lds\n"
"v_mfma_f32_16x16x32_bf16 v[52:55], a[92:95], v[188:191], v[52:55]\n"
"v_mfma_f32_16x16x32_bf16 v[36:39], a[92:95], v[184:187], v[36:39]\n"
"v_mfma_f32_16x16x32_bf16 v[36:39], a[88:91], v[168:171], v[36:39]\n"
"s_waitcnt vmcnt(48)\n"
"v_cmp_lt_u32 vcc, v30, v31\n"
"s_nop 4\n"
"s_cbranch_vccz .Lfk_d311\n"
"s_mov_b32 s35, 0x10000\n"
".Lfk_p311:\n"
"s_sleep 1\n"
"v_accvgpr_read_b32 v30, a234\n"
"buffer_load_dword v30, v30, s[64:67], 0 offen sc1\n"
"s_waitcnt vmcnt(0)\n"
"v_cmp_lt_u32 vcc, v30, v31\n"
"s_nop 4\n"
"s_cbranch_vccz .Lfk_d311\n"
"s_sub_u32 s35, s35, 1\n"
"s_cmp_lg_u32 s35, 0\n"
"s_cbranch_scc1 .Lfk_p311\n"
".Lfk_d311:\n"
"v_mfma_f32_16x16x32_bf16 a[52:55], a[88:91], v[164:167], a[52:55]\n"
"v_accvgpr_read_b32 v31, a232\n"
"s_mov_b32 m0, s78\n"
"v_mfma_f32_16x16x32_bf16 a[52:55], a[92:95], v[180:183], a[52:55]\n"
"buffer_load_dword a232, v31, s[64:67], s95 offen sc1\n"
"v_mfma_f32_16x16x32_bf16 a[20:23], a[92:95], v[176:179], a[20:23]\n"
"buffer_load_dword a233, v31, s[64:67], s95 offen offset:64 sc1\n"
"v_mfma_f32_16x16x32_bf16 a[20:23], a[88:91], v[160:163], a[20:23]\n"
"s_waitcnt vmcnt(26) lgkmcnt(0)\n"
"s_barrier\n"
"v_mfma_f32_16x16x32_bf16 a[24:27], a[96:99], v[160:163], a[24:27]\n"
"ds_read_b64_tr_b16 a[72:73], v8 offset:0\n"
"v_mfma_f32_16x16x32_bf16 a[24:27], a[100:103], v[176:179], a[24:27]\n"
"buffer_load_dwordx4 v20, s[36:39], 0 offen offset:0 lds\n"
"v_mfma_f32_16x16x32_bf16 a[56:59], a[100:103], v[180:183], a[56:59]\n"
"ds_read_b64_tr_b16 a[74:75], v8 offset:4096\n"
"v_mfma_f32_16x16x32_bf16 a[56:59], a[96:99], v[164:167], a[56:59]\n"
"ds_read_b64_tr_b16 a[76:77], v8 offset:8192\n"
"v_mfma_f32_16x16x32_bf16 v[40:43], a[96:99], v[168:171], v[40:43]\n"
"ds_read_b64_tr_b16 a[78:79], v8 offset:12288\n"
"v_mfma_f32_16x16x32_bf16 v[40:43], a[100:103], v[184:187], v[40:43]\n"
"buffer_load_dwordx4 v21, s[36:39], 0 offen offset:1024 lds\n"
"v_mfma_f32_16x16x32_bf16 v[56:59], a[100:103], v[188:191], v[56:59]\n"
"ds_read_b64_tr_b16 a[80:81], v9 offset:0\n"
"v_mfma_f32_16x16x32_bf16 v[56:59], a[96:99], v[172:175], v[56:59]\n"
"ds_read_b64_tr_b16 a[82:83], v9 offset:4096\n"
"v_mfma_f32_16x16x32_bf16 v[60:63], a[64:67], v[172:175], v[60:63]\n"
"ds_read_b64_tr_b16 a[84:85], v9 offset:8192\n"
"v_mfma_f32_16x16x32_bf16 v[60:63], a[68:71], v[188:191], v[60:63]\n"
"buffer_load_dwordx4 v22, s[36:39], 0 offen offset:2048 lds\n"
"v_mfma_f32_16x16x32_bf16 v[44:47], a[68:71], v[184:187], v[44:47]\n"
"ds_read_b64_tr_b16 a[86:87], v9 offset:12288\n"
"v_mfma_f32_16x16x32_bf16 v[44:47], a[64:67], v[168:171], v[44:47]\n"
"ds_read_b64_tr_b16 a[88:89], v10 offset:0\n"
"v_mfma_f32_16x16x32_bf16 a[60:63], a[64:67], v[164:167], a[60:63]\n"
"ds_read_b64_tr_b16 a[90:91], v10 offset:4096\n"
"v_mfma_f32_16x16x32_bf16 a[60:63], a[68:71], v[180:183], a[60:63]\n"
"buffer_load_dwordx4 v23, s[36:39], 0 offen offset:3072 lds\n"
"v_mfma_f32_16x16x32_bf16 a[28:31], a[68:71], v[176:179], a[28:31]\n"
"ds_read_b64_tr_b16 a[92:93], v10 offset:8192\n"
"v_mfma_f32_16x16x32_bf16 a[28:31], a[64:67], v[160:163], a[28:31]\n"
"ds_read_b64_tr_b16 a[94:95], v10 offset:12288\n"
"s_waitcnt lgkmcnt(8)\n"
"v_mfma_f32_16x16x32_bf16 a[0:3], a[72:75], v[192:195], a[0:3]\n"
"ds_read_b64_tr_b16 a[96:97], v11 offset:0\n"
"v_mfma_f32_16x16x32_bf16 a[0:3], a[76:79], v[208:211], a[0:3]\n"
"buffer_load_dwordx2 a[236:237], v28, s[64:67], 0 offen offset:0 sc1\n"
"v_mfma_f32_16x16x32_bf16 a[32:35], a[76:79], v[212:215], a[32:35]\n"
"ds_read_b64_tr_b16 a[98:99], v11 offset:4096\n"
"v_mfma_f32_16x16x32_bf16 a[32:35], a[72:75], v[196:199], a[32:35]\n"
"ds_read_b64_tr_b16 a[100:101], v11 offset:8192\n"
"v_mfma_f32_16x16x32_bf16 v[64:67], a[72:75], v[200:203], v[64:67]\n"
"ds_read_b64_tr_b16 a[102:103], v11 offset:12288\n"
"v_mfma_f32_16x16x32_bf16 v[64:67], a[76:79], v[216:219], v[64:67]\n"
"buffer_load_dwordx2 a[238:239], v28, s[64:67], s12 offen offset:0 sc1\n"
"v_mfma_f32_16x16x32_bf16 v[80:83], a[76:79], v[220:223], v[80:83]\n"
"buffer_load_dwordx2 a[240:241], v28, s[64:67], s13 offen offset:0 sc1\n"
"v_mfma_f32_16x16x32_bf16 v[80:83], a[72:75], v[204:207], v[80:83]\n"
"s_waitcnt lgkmcnt(8)\n"
"v_mfma_f32_16x16x32_bf16 v[84:87], a[80:83], v[204:207], v[84:87]\n"
"buffer_load_dwordx2 a[242:243], v28, s[64:67], s94 offen offset:0 sc1\n"
"v_mfma_f32_16x16x32_bf16 v[84:87], a[84:87], v[220:223], v[84:87]\n"
"buffer_load_dwordx2 a[244:245], v28, s[64:67], 0 offen offset:128 sc1\n"
"v_mfma_f32_16x16x32_bf16 v[68:71], a[84:87], v[216:219], v[68:71]\n"
"buffer_load_dwordx2 a[246:247], v28, s[64:67], s12 offen offset:128 sc1\n"
"v_mfma_f32_16x16x32_bf16 v[68:71], a[80:83], v[200:203], v[68:71]\n"
"buffer_load_dwordx2 a[248:249], v28, s[64:67], s13 offen offset:128 sc1\n"
"v_mfma_f32_16x16x32_bf16 a[36:39], a[80:83], v[196:199], a[36:39]\n"
"buffer_load_dwordx2 a[250:251], v28, s[64:67], s94 offen offset:128 sc1\n"
"v_mfma_f32_16x16x32_bf16 a[36:39], a[84:87], v[212:215], a[36:39]\n"
"v_mfma_f32_16x16x32_bf16 a[4:7], a[84:87], v[208:211], a[4:7]\n"
"v_mfma_f32_16x16x32_bf16 a[4:7], a[80:83], v[192:195], a[4:7]\n"
"s_waitcnt vmcnt(14) lgkmcnt(0)\n"
"s_barrier\n"
"v_mfma_f32_16x16x32_bf16 a[8:11], a[88:91], v[192:195], a[8:11]\n"
"ds_read_b64_tr_b16 a[64:65], v8 offset:16384\n"
"v_mfma_f32_16x16x32_bf16 a[8:11], a[92:95], v[208:211], a[8:11]\n"
"v_mfma_f32_16x16x32_bf16 a[40:43], a[92:95], v[212:215], a[40:43]\n"
"ds_read_b64_tr_b16 a[66:67], v8 offset:20480\n"
"v_mfma_f32_16x16x32_bf16 a[40:43], a[88:91], v[196:199], a[40:43]\n"
"v_mfma_f32_16x16x32_bf16 v[72:75], a[88:91], v[200:203], v[72:75]\n"
"ds_read_b64_tr_b16 a[68:69], v8 offset:24576\n"
"v_mfma_f32_16x16x32_bf16 v[72:75], a[92:95], v[216:219], v[72:75]\n"
"v_mfma_f32_16x16x32_bf16 v[88:91], a[92:95], v[220:223], v[88:91]\n"
"ds_read_b64_tr_b16 a[70:71], v8 offset:28672\n"
"v_mfma_f32_16x16x32_bf16 v[88:91], a[88:91], v[204:207], v[88:91]\n"
"v_mfma_f32_16x16x32_bf16 v[92:95], a[96:99], v[204:207], v[92:95]\n"
"ds_read_b64_tr_b16 a[72:73], v9 offset:16384\n"
"v_mfma_f32_16x16x32_bf16 v[92:95], a[100:103], v[220:223], v[92:95]\n"
"v_mfma_f32_16x16x32_bf16 v[76:79], a[100:103], v[216:219], v[76:79]\n"
"ds_read_b64_tr_b16 a[74:75], v9 offset:20480\n"
"v_mfma_f32_16x16x32_bf16 v[76:79], a[96:99], v[200:203], v[76:79]\n"
"v_mfma_f32_16x16x32_bf16 a[44:47], a[96:99], v[196:199], a[44:47]\n"
"ds_read_b64_tr_b16 a[76:77], v9 offset:24576\n"
"v_mfma_f32_16x16x32_bf16 a[44:47], a[100:103], v[212:215], a[44:47]\n"
"v_mfma_f32_16x16x32_bf16 a[12:15], a[100:103], v[208:211], a[12:15]\n"
"ds_read_b64_tr_b16 a[78:79], v9 offset:28672\n"
"v_mfma_f32_16x16x32_bf16 a[12:15], a[96:99], v[192:195], a[12:15]\n"
"s_waitcnt lgkmcnt(4)\n"
"v_mfma_f32_16x16x32_bf16 a[0:3], a[64:67], v[224:227], a[0:3]\n"
"ds_read_b64_tr_b16 a[80:81], v10 offset:16384\n"
"v_mfma_f32_16x16x32_bf16 a[0:3], a[68:71], v[240:243], a[0:3]\n"
"v_mfma_f32_16x16x32_bf16 a[32:35], a[68:71], v[244:247], a[32:35]\n"
"ds_read_b64_tr_b16 a[82:83], v10 offset:20480\n"
"v_mfma_f32_16x16x32_bf16 a[32:35], a[64:67], v[228:231], a[32:35]\n"
"v_mfma_f32_16x16x32_bf16 v[64:67], a[64:67], v[232:235], v[64:67]\n"
"ds_read_b64_tr_b16 a[84:85], v10 offset:24576\n"
"v_mfma_f32_16x16x32_bf16 v[64:67], a[68:71], v[248:251], v[64:67]\n"
"v_mfma_f32_16x16x32_bf16 v[80:83], a[68:71], v[252:255], v[80:83]\n"
"ds_read_b64_tr_b16 a[86:87], v10 offset:28672\n"
"v_mfma_f32_16x16x32_bf16 v[80:83], a[64:67], v[236:239], v[80:83]\n"
"s_waitcnt lgkmcnt(4)\n"
"v_mfma_f32_16x16x32_bf16 v[84:87], a[72:75], v[236:239], v[84:87]\n"
"ds_read_b64_tr_b16 a[88:89], v11 offset:16384\n"
"v_mfma_f32_16x16x32_bf16 v[84:87], a[76:79], v[252:255], v[84:87]\n"
"v_mfma_f32_16x16x32_bf16 v[68:71], a[76:79], v[248:251], v[68:71]\n"
"ds_read_b64_tr_b16 a[90:91], v11 offset:20480\n"
"v_mfma_f32_16x16x32_bf16 v[68:71], a[72:75], v[232:235], v[68:71]\n"
"ds_write_b64 v26, a[0:1] offset:0\n"
"v_mfma_f32_16x16x32_bf16 a[36:39], a[72:75], v[228:231], a[36:39]\n"
"ds_read_b64_tr_b16 a[92:93], v11 offset:24576\n"
"v_mfma_f32_16x16x32_bf16 a[36:39], a[76:79], v[244:247], a[36:39]\n"
"ds_write_b64 v26, a[2:3] offset:8\n"
"v_mfma_f32_16x16x32_bf16 a[4:7], a[76:79], v[240:243], a[4:7]\n"
"ds_read_b64_tr_b16 a[94:95], v11 offset:28672\n"
"v_mfma_f32_16x16x32_bf16 a[4:7], a[72:75], v[224:227], a[4:7]\n"
"ds_write_b64 v26, a[32:33] offset:4096\n"
"s_waitcnt lgkmcnt(7)\n"
"v_mfma_f32_16x16x32_bf16 a[8:11], a[80:83], v[224:227], a[8:11]\n"
"ds_read_b64_tr_b16 a[96:97], v12 offset:0\n"
"v_mfma_f32_16x16x32_bf16 a[8:11], a[84:87], v[240:243], a[8:11]\n"
"ds_write_b64 v26, a[34:35] offset:4104\n"
"v_mfma_f32_16x16x32_bf16 a[40:43], a[84:87], v[244:247], a[40:43]\n"
"ds_read_b64_tr_b16 a[98:99], v12 offset:4096\n"
"v_mfma_f32_16x16x32_bf16 a[40:43], a[80:83], v[228:231], a[40:43]\n"
"v_mfma_f32_16x16x32_bf16 v[72:75], a[80:83], v[232:235], v[72:75]\n"
"ds_read_b64_tr_b16 a[100:101], v12 offset:8192\n"
"v_mfma_f32_16x16x32_bf16 v[72:75], a[84:87], v[248:251], v[72:75]\n"
"v_mfma_f32_16x16x32_bf16 v[88:91], a[84:87], v[252:255], v[88:91]\n"
"ds_read_b64_tr_b16 a[102:103], v12 offset:12288\n"
"v_mfma_f32_16x16x32_bf16 v[88:91], a[80:83], v[236:239], v[88:91]\n"
"ds_write_b64 v26, a[36:37] offset:5120\n"
"s_waitcnt lgkmcnt(7)\n"
"v_mfma_f32_16x16x32_bf16 v[92:95], a[88:91], v[236:239], v[92:95]\n"
"ds_read_b64_tr_b16 a[64:65], v13 offset:0\n"
"v_mfma_f32_16x16x32_bf16 v[92:95], a[92:95], v[252:255], v[92:95]\n"
"ds_write_b64 v26, a[4:5] offset:1024\n"
"v_mfma_f32_16x16x32_bf16 v[76:79], a[92:95], v[248:251], v[76:79]\n"
"ds_read_b64_tr_b16 a[66:67], v13 offset:4096\n"
"v_mfma_f32_16x16x32_bf16 v[76:79], a[88:91], v[232:235], v[76:79]\n"
"ds_write_b64 v26, a[6:7] offset:1032\n"
"v_mfma_f32_16x16x32_bf16 a[44:47], a[88:91], v[228:231], a[44:47]\n"
"ds_read_b64_tr_b16 a[68:69], v13 offset:8192\n"
"v_mfma_f32_16x16x32_bf16 a[44:47], a[92:95], v[244:247], a[44:47]\n"
"ds_write_b64 v26, a[8:9] offset:2048\n"
"v_mfma_f32_16x16x32_bf16 a[12:15], a[92:95], v[240:243], a[12:15]\n"
"ds_read_b64_tr_b16 a[70:71], v13 offset:12288\n"
"v_mfma_f32_16x16x32_bf16 a[12:15], a[88:91], v[224:227], a[12:15]\n"
"ds_write_b64 v26, a[10:11] offset:2056\n"
"s_waitcnt lgkmcnt(9)\n"
"v_mfma_f32_16x16x32_bf16 a[16:19], a[96:99], v[192:195], a[16:19]\n"
"ds_read_b64_tr_b16 a[72:73], v14 offset:0\n"
"v_mfma_f32_16x16x32_bf16 a[16:19], a[100:103], v[208:211], a[16:19]\n"
"ds_write_b64 v26, a[38:39] offset:5128\n"
"v_mfma_f32_16x16x32_bf16 a[48:51], a[100:103], v[212:215], a[48:51]\n"
"ds_read_b64_tr_b16 a[74:75], v14 offset:4096\n"
"v_mfma_f32_16x16x32_bf16 a[48:51], a[96:99], v[196:199], a[48:51]\n"
"ds_write_b64 v26, a[40:41] offset:6144\n"
"v_mfma_f32_16x16x32_bf16 v[32:35], a[96:99], v[200:203], v[32:35]\n"
"ds_read_b64_tr_b16 a[76:77], v14 offset:8192\n"
"v_mfma_f32_16x16x32_bf16 v[32:35], a[100:103], v[216:219], v[32:35]\n"
"ds_write_b64 v26, a[42:43] offset:6152\n"
"v_mfma_f32_16x16x32_bf16 v[48:51], a[100:103], v[220:223], v[48:51]\n"
"ds_read_b64_tr_b16 a[78:79], v14 offset:12288\n"
"v_mfma_f32_16x16x32_bf16 v[48:51], a[96:99], v[204:207], v[48:51]\n"
"ds_write_b64 v26, a[44:45] offset:7168\n"
"s_waitcnt lgkmcnt(9)\n"
"v_mfma_f32_16x16x32_bf16 v[52:55], a[64:67], v[204:207], v[52:55]\n"
"ds_read_b64_tr_b16 a[80:81], v15 offset:0\n"
"v_mfma_f32_16x16x32_bf16 v[52:55], a[68:71], v[220:223], v[52:55]\n"
"ds_write_b64 v26, a[12:13] offset:3072\n"
"v_mfma_f32_16x16x32_bf16 v[36:39], a[68:71], v[216:219], v[36:39]\n"
"ds_read_b64_tr_b16 a[82:83], v15 offset:4096\n"
"v_mfma_f32_16x16x32_bf16 v[36:39], a[64:67], v[200:203], v[36:39]\n"
"ds_write_b64 v26, a[14:15] offset:3080\n"
"v_mfma_f32_16x16x32_bf16 a[52:55], a[64:67], v[196:199], a[52:55]\n"
"ds_read_b64_tr_b16 a[84:85], v15 offset:8192\n"
"v_mfma_f32_16x16x32_bf16 a[52:55], a[68:71], v[212:215], a[52:55]\n"
"ds_write_b64 v26, a[46:47] offset:7176\n"
"v_mfma_f32_16x16x32_bf16 a[20:23], a[68:71], v[208:211], a[20:23]\n"
"ds_read_b64_tr_b16 a[86:87], v15 offset:12288\n"
"v_mfma_f32_16x16x32_bf16 a[20:23], a[64:67], v[192:195], a[20:23]\n"
"s_waitcnt lgkmcnt(8)\n"
"v_mfma_f32_16x16x32_bf16 a[24:27], a[72:75], v[192:195], a[24:27]\n"
"ds_read_b64_tr_b16 a[88:89], v12 offset:16384\n"
"v_mfma_f32_16x16x32_bf16 a[24:27], a[76:79], v[208:211], a[24:27]\n"
"s_waitcnt lgkmcnt(2)\n"
"s_barrier\n"
"v_mfma_f32_16x16x32_bf16 a[56:59], a[76:79], v[212:215], a[56:59]\n"
"ds_read_b64_tr_b16 a[90:91], v12 offset:20480\n"
"v_mfma_f32_16x16x32_bf16 a[56:59], a[72:75], v[196:199], a[56:59]\n"
"ds_read_b128 v[128:131], v27 offset:0\n"
"v_mfma_f32_16x16x32_bf16 v[40:43], a[72:75], v[200:203], v[40:43]\n"
"ds_read_b64_tr_b16 a[92:93], v12 offset:24576\n"
"v_mfma_f32_16x16x32_bf16 v[40:43], a[76:79], v[216:219], v[40:43]\n"
"ds_read_b128 v[132:135], v27 offset:1024\n"
"v_mfma_f32_16x16x32_bf16 v[56:59], a[76:79], v[220:223], v[56:59]\n"
"ds_read_b64_tr_b16 a[94:95], v12 offset:28672\n"
"v_mfma_f32_16x16x32_bf16 v[56:59], a[72:75], v[204:207], v[56:59]\n"
"ds_read_b128 v[136:139], v27 offset:2048\n"
"s_waitcnt lgkmcnt(7)\n"
"v_mfma_f32_16x16x32_bf16 v[60:63], a[80:83], v[204:207], v[60:63]\n"
"ds_read_b64_tr_b16 a[96:97], v13 offset:16384\n"
"v_mfma_f32_16x16x32_bf16 v[60:63], a[84:87], v[220:223], v[60:63]\n"
"ds_read_b128 v[140:143], v27 offset:3072\n"
"v_mfma_f32_16x16x32_bf16 v[44:47], a[84:87], v[216:219], v[44:47]\n"
"ds_read_b64_tr_b16 a[98:99], v13 offset:20480\n"
"v_mfma_f32_16x16x32_bf16 v[44:47], a[80:83], v[200:203], v[44:47]\n"
"ds_read_b128 v[144:147], v27 offset:4096\n"
"v_mfma_f32_16x16x32_bf16 a[60:63], a[80:83], v[196:199], a[60:63]\n"
"ds_read_b64_tr_b16 a[100:101], v13 offset:24576\n"
"v_mfma_f32_16x16x32_bf16 a[60:63], a[84:87], v[212:215], a[60:63]\n"
"ds_read_b128 v[148:151], v27 offset:5120\n"
"s_waitcnt lgkmcnt(8)\n"
"v_mfma_f32_16x16x32_bf16 a[28:31], a[84:87], v[208:211], a[28:31]\n"
"ds_read_b64_tr_b16 a[102:103], v13 offset:28672\n"
"v_add_f32 v64, v64, v128\n"
"v_mfma_f32_16x16x32_bf16 a[28:31], a[80:83], v[192:195], a[28:31]\n"
"ds_read_b128 v[152:155], v27 offset:6144\n"
"s_waitcnt lgkmcnt(9)\n"
"v_mfma_f32_16x16x32_bf16 a[16:19], a[88:91], v[224:227], a[16:19]\n"
"ds_read_b64_tr_b16 a[64:65], v14 offset:16384\n"
"v_add_f32 v65, v65, v129\n"
"v_mfma_f32_16x16x32_bf16 a[16:19], a[92:95], v[240:243], a[16:19]\n"
"ds_read_b128 v[156:159], v27 offset:7168\n"
"v_add_f32 v66, v66, v130\n"
"v_mfma_f32_16x16x32_bf16 a[48:51], a[92:95], v[244:247], a[48:51]\n"
"ds_read_b64_tr_b16 a[66:67], v14 offset:20480\n"
"v_add_f32 v67, v67, v131\n"
"v_mfma_f32_16x16x32_bf16 a[48:51], a[88:91], v[228:231], a[48:51]\n"
"v_cvt_pk_bf16_f32 v64, v64, v65\n"
"v_cvt_pk_bf16_f32 v65, v66, v67\n"
"v_mfma_f32_16x16x32_bf16 v[32:35], a[88:91], v[232:235], v[32:35]\n"
"ds_read_b64_tr_b16 a[68:69], v14 offset:24576\n"
"v_add_f32 v68, v68, v132\n"
"v_mfma_f32_16x16x32_bf16 v[32:35], a[92:95], v[248:251], v[32:35]\n"
"v_add_f32 v69, v69, v133\n"
"v_add_f32 v70, v70, v134\n"
"v_mfma_f32_16x16x32_bf16 v[48:51], a[92:95], v[252:255], v[48:51]\n"
"ds_read_b64_tr_b16 a[70:71], v14 offset:28672\n"
"v_add_f32 v71, v71, v135\n"
"v_mfma_f32_16x16x32_bf16 v[48:51], a[88:91], v[236:239], v[48:51]\n"
"v_cvt_pk_bf16_f32 v66, v68, v69\n"
"s_waitcnt lgkmcnt(6)\n"
"v_mfma_f32_16x16x32_bf16 v[52:55], a[96:99], v[236:239], v[52:55]\n"
"ds_read_b64_tr_b16 a[72:73], v15 offset:16384\n"
"v_cvt_pk_bf16_f32 v67, v70, v71\n"
"v_mfma_f32_16x16x32_bf16 v[52:55], a[100:103], v[252:255], v[52:55]\n"
"buffer_store_dwordx4 v[64:67], v1, s[56:59], s23 offen offset:0 sc1\n"
"v_mfma_f32_16x16x32_bf16 v[36:39], a[100:103], v[248:251], v[36:39]\n"
"ds_read_b64_tr_b16 a[74:75], v15 offset:20480\n"
"v_mfma_f32_16x16x32_bf16 v[36:39], a[96:99], v[232:235], v[36:39]\n"
"v_add_f32 v72, v72, v136\n"
"v_add_f32 v73, v73, v137\n"
"v_mfma_f32_16x16x32_bf16 a[52:55], a[96:99], v[228:231], a[52:55]\n"
"ds_read_b64_tr_b16 a[76:77], v15 offset:24576\n"
"v_add_f32 v74, v74, v138\n"
"v_mfma_f32_16x16x32_bf16 a[52:55], a[100:103], v[244:247], a[52:55]\n"
"v_add_f32 v75, v75, v139\n"
"v_cvt_pk_bf16_f32 v72, v72, v73\n"
"v_mfma_f32_16x16x32_bf16 a[20:23], a[100:103], v[240:243], a[20:23]\n"
"ds_read_b64_tr_b16 a[78:79], v15 offset:28672\n"
"v_cvt_pk_bf16_f32 v73, v74, v75\n"
"v_mfma_f32_16x16x32_bf16 a[20:23], a[96:99], v[224:227], a[20:23]\n"
"v_add_f32 v76, v76, v140\n"
"s_waitcnt lgkmcnt(4)\n"
"v_mfma_f32_16x16x32_bf16 a[24:27], a[64:67], v[224:227], a[24:27]\n"
"v_add_f32 v77, v77, v141\n"
"v_add_f32 v78, v78, v142\n"
"v_mfma_f32_16x16x32_bf16 a[24:27], a[68:71], v[240:243], a[24:27]\n"
"v_add_f32 v79, v79, v143\n"
"v_cvt_pk_bf16_f32 v74, v76, v77\n"
"v_mfma_f32_16x16x32_bf16 a[56:59], a[68:71], v[244:247], a[56:59]\n"
"ds_write_b64 v27, a[16:17] offset:0\n"
"v_cvt_pk_bf16_f32 v75, v78, v79\n"
"v_mfma_f32_16x16x32_bf16 a[56:59], a[64:67], v[228:231], a[56:59]\n"
"buffer_store_dwordx4 v[72:75], v1, s[56:59], s23 offen offset:1024 sc1\n"
"v_mfma_f32_16x16x32_bf16 v[40:43], a[64:67], v[232:235], v[40:43]\n"
"v_add_f32 v80, v80, v144\n"
"v_mfma_f32_16x16x32_bf16 v[40:43], a[68:71], v[248:251], v[40:43]\n"
"v_add_f32 v81, v81, v145\n"
"v_add_f32 v82, v82, v146\n"
"v_mfma_f32_16x16x32_bf16 v[56:59], a[68:71], v[252:255], v[56:59]\n"
"v_add_f32 v83, v83, v147\n"
"v_cvt_pk_bf16_f32 v80, v80, v81\n"
"v_mfma_f32_16x16x32_bf16 v[56:59], a[64:67], v[236:239], v[56:59]\n"
"v_cvt_pk_bf16_f32 v81, v82, v83\n"
"s_waitcnt lgkmcnt(1)\n"
"v_mfma_f32_16x16x32_bf16 v[60:63], a[72:75], v[236:239], v[60:63]\n"
"v_add_f32 v84, v84, v148\n"
"v_add_f32 v85, v85, v149\n"
"v_mfma_f32_16x16x32_bf16 v[60:63], a[76:79], v[252:255], v[60:63]\n"
"v_add_f32 v86, v86, v150\n"
"v_add_f32 v87, v87, v151\n"
"v_mfma_f32_16x16x32_bf16 v[44:47], a[76:79], v[248:251], v[44:47]\n"
"v_cvt_pk_bf16_f32 v82, v84, v85\n"
"v_cvt_pk_bf16_f32 v83, v86, v87\n"
"v_mfma_f32_16x16x32_bf16 v[44:47], a[72:75], v[232:235], v[44:47]\n"
"buffer_store_dwordx4 v[80:83], v1, s[56:59], s23 offen offset:2048 sc1\n"
"v_mfma_f32_16x16x32_bf16 a[60:63], a[72:75], v[228:231], a[60:63]\n"
"v_add_f32 v88, v88, v152\n"
"v_mfma_f32_16x16x32_bf16 a[60:63], a[76:79], v[244:247], a[60:63]\n"
"v_add_f32 v89, v89, v153\n"
"v_add_f32 v90, v90, v154\n"
"v_mfma_f32_16x16x32_bf16 a[28:31], a[76:79], v[240:243], a[28:31]\n"
"v_add_f32 v91, v91, v155\n"
"v_cvt_pk_bf16_f32 v88, v88, v89\n"
"v_mfma_f32_16x16x32_bf16 a[28:31], a[72:75], v[224:227], a[28:31]\n"
"v_cvt_pk_bf16_f32 v89, v90, v91\n"
"v_add_f32 v92, v92, v156\n"
"v_add_f32 v93, v93, v157\n"
"v_add_f32 v94, v94, v158\n"
"v_add_f32 v95, v95, v159\n"
"v_cvt_pk_bf16_f32 v90, v92, v93\n"
"v_cvt_pk_bf16_f32 v91, v94, v95\n"
"buffer_store_dwordx4 v[88:91], v1, s[56:59], s23 offen offset:3072 sc1\n"
"ds_write_b64 v27, a[18:19] offset:8\n"
"ds_write_b64 v27, a[48:49] offset:4096\n"
"ds_write_b64 v27, a[50:51] offset:4104\n"
"ds_write_b64 v27, a[20:21] offset:1024\n"
"ds_write_b64 v27, a[22:23] offset:1032\n"
"ds_write_b64 v27, a[52:53] offset:5120\n"
"ds_write_b64 v27, a[54:55] offset:5128\n"
"ds_write_b64 v27, a[24:25] offset:2048\n"
"ds_write_b64 v27, a[26:27] offset:2056\n"
"ds_write_b64 v27, a[56:57] offset:6144\n"
"ds_write_b64 v27, a[58:59] offset:6152\n"
"ds_write_b128 v27, a[28:31] offset:3072\n"
"ds_write_b128 v27, a[60:63] offset:7168\n"
"buffer_load_dwordx4 v[192:195], v1, s[56:59], s18 offen offset:0 sc1\n"
"buffer_load_dwordx4 v[196:199], v1, s[56:59], s18 offen offset:1024 sc1\n"
"buffer_load_dwordx4 v[200:203], v1, s[56:59], s18 offen offset:2048 sc1\n"
"buffer_load_dwordx4 v[204:207], v1, s[56:59], s18 offen offset:3072 sc1\n"
"s_waitcnt lgkmcnt(0)\n"
"s_barrier\n"
"ds_read_b128 v[160:163], v26 offset:0\n"
"ds_read_b128 v[164:167], v26 offset:1024\n"
"ds_read_b128 v[168:171], v26 offset:2048\n"
"ds_read_b128 v[172:175], v26 offset:3072\n"
"ds_read_b128 v[176:179], v26 offset:4096\n"
"ds_read_b128 v[180:183], v26 offset:5120\n"
"ds_read_b128 v[184:187], v26 offset:6144\n"
"ds_read_b128 v[188:191], v26 offset:7168\n"
"s_waitcnt vmcnt(8)\n"
"ds_read_b64 v[112:113], v2 offset:0\n"
"ds_read_b64 v[114:115], v3 offset:0\n"
"ds_read_b64 v[116:117], v24 offset:0\n"
"ds_read_b64 v[118:119], v25 offset:0\n"
"ds_read_b64 v[120:121], v2 offset:2048\n"
"ds_read_b64 v[122:123], v3 offset:2048\n"
"ds_read_b64 v[124:125], v24 offset:2048\n"
"ds_read_b64 v[126:127], v25 offset:2048\n"
"v_accvgpr_read_b32 v18, a232\n"
"v_accvgpr_read_b32 v19, a233\n"
"v_sub_f32 v16, 1.0, v18\n"
"v_sub_f32 v17, 1.0, v19\n"
"s_waitcnt lgkmcnt(7)\n"
"v_lshlrev_b32 v224, 16, v112\n"
"v_and_b32 v225, 0xffff0000, v112\n"
"v_accvgpr_read_b32 v5, a236\n"
"v_lshlrev_b32 v4, 16, v5\n"
"v_and_b32 v5, 0xffff0000, v5\n"
"v_lshlrev_b32 v226, 16, v113\n"
"v_and_b32 v227, 0xffff0000, v113\n"
"v_accvgpr_read_b32 v7, a237\n"
"v_lshlrev_b32 v6, 16, v7\n"
"v_and_b32 v7, 0xffff0000, v7\n"
"v_pk_mul_f32 v[224:225], v[224:225], v[4:5]\n"
"v_pk_mul_f32 v[226:227], v[226:227], v[6:7]\n"
"v_pk_mul_f32 v[224:225], v[224:225], v[18:19] op_sel:[0,0] op_sel_hi:[1,0]\n"
"v_pk_mul_f32 v[226:227], v[226:227], v[18:19] op_sel:[0,0] op_sel_hi:[1,0]\n"
"v_pk_add_f32 v[32:33], v[32:33], v[160:161]\n"
"v_pk_add_f32 v[34:35], v[34:35], v[162:163]\n"
"v_pk_fma_f32 v[32:33], v[32:33], v[16:17], v[224:225] op_sel:[0,0,0] op_sel_hi:[1,0,1]\n"
"v_pk_fma_f32 v[34:35], v[34:35], v[16:17], v[226:227] op_sel:[0,0,0] op_sel_hi:[1,0,1]\n"
"s_waitcnt lgkmcnt(6)\n"
"v_lshlrev_b32 v228, 16, v114\n"
"v_and_b32 v229, 0xffff0000, v114\n"
"v_accvgpr_read_b32 v5, a238\n"
"v_lshlrev_b32 v4, 16, v5\n"
"v_and_b32 v5, 0xffff0000, v5\n"
"v_lshlrev_b32 v230, 16, v115\n"
"v_and_b32 v231, 0xffff0000, v115\n"
"v_accvgpr_read_b32 v7, a239\n"
"v_lshlrev_b32 v6, 16, v7\n"
"v_and_b32 v7, 0xffff0000, v7\n"
"v_pk_mul_f32 v[228:229], v[228:229], v[4:5]\n"
"v_pk_mul_f32 v[230:231], v[230:231], v[6:7]\n"
"v_pk_mul_f32 v[228:229], v[228:229], v[18:19] op_sel:[0,0] op_sel_hi:[1,0]\n"
"v_pk_mul_f32 v[230:231], v[230:231], v[18:19] op_sel:[0,0] op_sel_hi:[1,0]\n"
".Lfk_hop7993:\n"
"s_cbranch_execz .Lfk_hopret\n"
"v_pk_add_f32 v[36:37], v[36:37], v[164:165]\n"
"v_pk_add_f32 v[38:39], v[38:39], v[166:167]\n"
"v_pk_fma_f32 v[36:37], v[36:37], v[16:17], v[228:229] op_sel:[0,0,0] op_sel_hi:[1,0,1]\n"
"v_pk_fma_f32 v[38:39], v[38:39], v[16:17], v[230:231] op_sel:[0,0,0] op_sel_hi:[1,0,1]\n"
"s_waitcnt lgkmcnt(5)\n"
"v_lshlrev_b32 v232, 16, v116\n"
"v_and_b32 v233, 0xffff0000, v116\n"
"v_accvgpr_read_b32 v5, a240\n"
"v_lshlrev_b32 v4, 16, v5\n"
"v_and_b32 v5, 0xffff0000, v5\n"
"v_lshlrev_b32 v234, 16, v117\n"
"v_and_b32 v235, 0xffff0000, v117\n"
"v_accvgpr_read_b32 v7, a241\n"
"v_lshlrev_b32 v6, 16, v7\n"
"v_and_b32 v7, 0xffff0000, v7\n"
"v_pk_mul_f32 v[232:233], v[232:233], v[4:5]\n"
"v_pk_mul_f32 v[234:235], v[234:235], v[6:7]\n"
"v_pk_mul_f32 v[232:233], v[232:233], v[18:19] op_sel:[0,0] op_sel_hi:[1,0]\n"
"v_pk_mul_f32 v[234:235], v[234:235], v[18:19] op_sel:[0,0] op_sel_hi:[1,0]\n"
"v_pk_add_f32 v[40:41], v[40:41], v[168:169]\n"
"v_pk_add_f32 v[42:43], v[42:43], v[170:171]\n"
"v_pk_fma_f32 v[40:41], v[40:41], v[16:17], v[232:233] op_sel:[0,0,0] op_sel_hi:[1,0,1]\n"
"v_pk_fma_f32 v[42:43], v[42:43], v[16:17], v[234:235] op_sel:[0,0,0] op_sel_hi:[1,0,1]\n"
"s_waitcnt lgkmcnt(4)\n"
"v_lshlrev_b32 v236, 16, v118\n"
"v_and_b32 v237, 0xffff0000, v118\n"
"v_accvgpr_read_b32 v5, a242\n"
"v_lshlrev_b32 v4, 16, v5\n"
"v_and_b32 v5, 0xffff0000, v5\n"
"v_lshlrev_b32 v238, 16, v119\n"
"v_and_b32 v239, 0xffff0000, v119\n"
"v_accvgpr_read_b32 v7, a243\n"
"v_lshlrev_b32 v6, 16, v7\n"
"v_and_b32 v7, 0xffff0000, v7\n"
"v_pk_mul_f32 v[236:237], v[236:237], v[4:5]\n"
"v_pk_mul_f32 v[238:239], v[238:239], v[6:7]\n"
"v_pk_mul_f32 v[236:237], v[236:237], v[18:19] op_sel:[0,0] op_sel_hi:[1,0]\n"
"v_pk_mul_f32 v[238:239], v[238:239], v[18:19] op_sel:[0,0] op_sel_hi:[1,0]\n"
"v_pk_add_f32 v[44:45], v[44:45], v[172:173]\n"
"v_pk_add_f32 v[46:47], v[46:47], v[174:175]\n"
"v_pk_fma_f32 v[44:45], v[44:45], v[16:17], v[236:237] op_sel:[0,0,0] op_sel_hi:[1,0,1]\n"
"v_pk_fma_f32 v[46:47], v[46:47], v[16:17], v[238:239] op_sel:[0,0,0] op_sel_hi:[1,0,1]\n"
"s_waitcnt lgkmcnt(3)\n"
"v_lshlrev_b32 v240, 16, v120\n"
"v_and_b32 v241, 0xffff0000, v120\n"
"v_accvgpr_read_b32 v5, a244\n"
"v_lshlrev_b32 v4, 16, v5\n"
"v_and_b32 v5, 0xffff0000, v5\n"
"v_lshlrev_b32 v242, 16, v121\n"
"v_and_b32 v243, 0xffff0000, v121\n"
"v_accvgpr_read_b32 v7, a245\n"
"v_lshlrev_b32 v6, 16, v7\n"
"v_and_b32 v7, 0xffff0000, v7\n"
"v_pk_mul_f32 v[240:241], v[240:241], v[4:5]\n"
"v_pk_mul_f32 v[242:243], v[242:243], v[6:7]\n"
"v_pk_mul_f32 v[240:241], v[240:241], v[18:19] op_sel:[0,1] op_sel_hi:[1,1]\n"
"v_pk_mul_f32 v[242:243], v[242:243], v[18:19] op_sel:[0,1] op_sel_hi:[1,1]\n"
"v_pk_add_f32 v[48:49], v[48:49], v[176:177]\n"
"v_pk_add_f32 v[50:51], v[50:51], v[178:179]\n"
"v_pk_fma_f32 v[48:49], v[48:49], v[16:17], v[240:241] op_sel:[0,1,0] op_sel_hi:[1,1,1]\n"
"v_pk_fma_f32 v[50:51], v[50:51], v[16:17], v[242:243] op_sel:[0,1,0] op_sel_hi:[1,1,1]\n"
"s_waitcnt lgkmcnt(2)\n"
"v_lshlrev_b32 v244, 16, v122\n"
"v_and_b32 v245, 0xffff0000, v122\n"
"v_accvgpr_read_b32 v5, a246\n"
"v_lshlrev_b32 v4, 16, v5\n"
"v_and_b32 v5, 0xffff0000, v5\n"
"v_lshlrev_b32 v246, 16, v123\n"
"v_and_b32 v247, 0xffff0000, v123\n"
"v_accvgpr_read_b32 v7, a247\n"
"v_lshlrev_b32 v6, 16, v7\n"
"v_and_b32 v7, 0xffff0000, v7\n"
"v_pk_mul_f32 v[244:245], v[244:245], v[4:5]\n"
"v_pk_mul_f32 v[246:247], v[246:247], v[6:7]\n"
"v_pk_mul_f32 v[244:245], v[244:245], v[18:19] op_sel:[0,1] op_sel_hi:[1,1]\n"
"v_pk_mul_f32 v[246:247], v[246:247], v[18:19] op_sel:[0,1] op_sel_hi:[1,1]\n"
"v_pk_add_f32 v[52:53], v[52:53], v[180:181]\n"
"v_pk_add_f32 v[54:55], v[54:55], v[182:183]\n"
"v_pk_fma_f32 v[52:53], v[52:53], v[16:17], v[244:245] op_sel:[0,1,0] op_sel_hi:[1,1,1]\n"
"v_pk_fma_f32 v[54:55], v[54:55], v[16:17], v[246:247] op_sel:[0,1,0] op_sel_hi:[1,1,1]\n"
"s_waitcnt lgkmcnt(1)\n"
"v_lshlrev_b32 v248, 16, v124\n"
"v_and_b32 v249, 0xffff0000, v124\n"
"v_accvgpr_read_b32 v5, a248\n"
"v_lshlrev_b32 v4, 16, v5\n"
"v_and_b32 v5, 0xffff0000, v5\n"
"v_lshlrev_b32 v250, 16, v125\n"
"v_and_b32 v251, 0xffff0000, v125\n"
"v_accvgpr_read_b32 v7, a249\n"
"v_lshlrev_b32 v6, 16, v7\n"
"v_and_b32 v7, 0xffff0000, v7\n"
"v_pk_mul_f32 v[248:249], v[248:249], v[4:5]\n"
"v_pk_mul_f32 v[250:251], v[250:251], v[6:7]\n"
"v_pk_mul_f32 v[248:249], v[248:249], v[18:19] op_sel:[0,1] op_sel_hi:[1,1]\n"
"v_pk_mul_f32 v[250:251], v[250:251], v[18:19] op_sel:[0,1] op_sel_hi:[1,1]\n"
"v_pk_add_f32 v[56:57], v[56:57], v[184:185]\n"
"v_pk_add_f32 v[58:59], v[58:59], v[186:187]\n"
"v_pk_fma_f32 v[56:57], v[56:57], v[16:17], v[248:249] op_sel:[0,1,0] op_sel_hi:[1,1,1]\n"
"v_pk_fma_f32 v[58:59], v[58:59], v[16:17], v[250:251] op_sel:[0,1,0] op_sel_hi:[1,1,1]\n"
"s_waitcnt lgkmcnt(0)\n"
"v_lshlrev_b32 v252, 16, v126\n"
"v_and_b32 v253, 0xffff0000, v126\n"
"v_accvgpr_read_b32 v5, a250\n"
"v_lshlrev_b32 v4, 16, v5\n"
"v_and_b32 v5, 0xffff0000, v5\n"
"v_lshlrev_b32 v254, 16, v127\n"
"v_and_b32 v255, 0xffff0000, v127\n"
"v_accvgpr_read_b32 v7, a251\n"
"v_lshlrev_b32 v6, 16, v7\n"
"v_and_b32 v7, 0xffff0000, v7\n"
"v_pk_mul_f32 v[252:253], v[252:253], v[4:5]\n"
"v_pk_mul_f32 v[254:255], v[254:255], v[6:7]\n"
"v_pk_mul_f32 v[252:253], v[252:253], v[18:19] op_sel:[0,1] op_sel_hi:[1,1]\n"
"v_pk_mul_f32 v[254:255], v[254:255], v[18:19] op_sel:[0,1] op_sel_hi:[1,1]\n"
"v_pk_add_f32 v[60:61], v[60:61], v[188:189]\n"
"v_pk_add_f32 v[62:63], v[62:63], v[190:191]\n"
"v_pk_fma_f32 v[60:61], v[60:61], v[16:17], v[252:253] op_sel:[0,1,0] op_sel_hi:[1,1,1]\n"
"v_pk_fma_f32 v[62:63], v[62:63], v[16:17], v[254:255] op_sel:[0,1,0] op_sel_hi:[1,1,1]\n"
"s_waitcnt vmcnt(0)\n"
"v_max_u32 v4, v192, v193\n"
"v_max3_u32 v4, v4, v194, v195\n"
"v_max3_u32 v4, v4, v196, v197\n"
"v_max3_u32 v4, v4, v198, v199\n"
"v_max3_u32 v4, v4, v200, v201\n"
"v_max3_u32 v4, v4, v202, v203\n"
"v_max3_u32 v4, v4, v204, v205\n"
"v_max3_u32 v4, v4, v206, v207\n"
"v_cmp_eq_u32 vcc, -1, v4\n"
"s_nop 4\n"
"s_cbranch_vccz .Lfk_xd12\n"
"s_mov_b32 s35, 0x8000\n"
".Lfk_xs12:\n"
"buffer_load_dwordx4 v[192:195], v1, s[56:59], s18 offen offset:0 sc1\n"
"buffer_load_dwordx4 v[196:199], v1, s[56:59], s18 offen offset:1024 sc1\n"
"buffer_load_dwordx4 v[200:203], v1, s[56:59], s18 offen offset:2048 sc1\n"
"buffer_load_dwordx4 v[204:207], v1, s[56:59], s18 offen offset:3072 sc1\n"
"s_waitcnt vmcnt(0)\n"
"v_max_u32 v4, v192, v193\n"
"v_max3_u32 v4, v4, v194, v195\n"
"v_max3_u32 v4, v4, v196, v197\n"
"v_max3_u32 v4, v4, v198, v199\n"
"v_max3_u32 v4, v4, v200, v201\n"
"v_max3_u32 v4, v4, v202, v203\n"
"v_max3_u32 v4, v4, v204, v205\n"
"v_max3_u32 v4, v4, v206, v207\n"
"v_cmp_eq_u32 vcc, -1, v4\n"
"s_nop 4\n"
"s_cbranch_vccz .Lfk_xd12\n"
"s_sub_u32 s35, s35, 1\n"
"s_cmp_lg_u32 s35, 0\n"
"s_cbranch_scc1 .Lfk_xs12\n"
".Lfk_xd12:\n"
"v_add_u32 v8, s78, v1\n"
"v_lshlrev_b32 v4, 16, v192\n"
"v_and_b32 v5, 0xffff0000, v192\n"
"v_lshlrev_b32 v6, 16, v193\n"
"v_and_b32 v7, 0xffff0000, v193\n"
"v_pk_fma_f32 v[32:33], v[4:5], v[16:17], v[32:33] op_sel:[0,0,0] op_sel_hi:[1,0,1]\n"
"v_pk_fma_f32 v[34:35], v[6:7], v[16:17], v[34:35] op_sel:[0,0,0] op_sel_hi:[1,0,1]\n"
"v_cvt_pk_bf16_f32 v32, v32, v33\n"
"v_cvt_pk_bf16_f32 v33, v34, v35\n"
"ds_write_b64 v2, v[32:33] offset:0\n"
"v_lshlrev_b32 v4, 16, v194\n"
"v_and_b32 v5, 0xffff0000, v194\n"
"v_lshlrev_b32 v6, 16, v195\n"
"v_and_b32 v7, 0xffff0000, v195\n"
"v_pk_fma_f32 v[36:37], v[4:5], v[16:17], v[36:37] op_sel:[0,0,0] op_sel_hi:[1,0,1]\n"
"v_pk_fma_f32 v[38:39], v[6:7], v[16:17], v[38:39] op_sel:[0,0,0] op_sel_hi:[1,0,1]\n"
"v_cvt_pk_bf16_f32 v36, v36, v37\n"
"v_cvt_pk_bf16_f32 v37, v38, v39\n"
"ds_write_b64 v3, v[36:37] offset:0\n"
"v_lshlrev_b32 v4, 16, v196\n"
"v_and_b32 v5, 0xffff0000, v196\n"
"v_lshlrev_b32 v6, 16, v197\n"
"v_and_b32 v7, 0xffff0000, v197\n"
"v_pk_fma_f32 v[40:41], v[4:5], v[16:17], v[40:41] op_sel:[0,0,0] op_sel_hi:[1,0,1]\n"
"v_pk_fma_f32 v[42:43], v[6:7], v[16:17], v[42:43] op_sel:[0,0,0] op_sel_hi:[1,0,1]\n"
"v_cvt_pk_bf16_f32 v40, v40, v41\n"
"v_cvt_pk_bf16_f32 v41, v42, v43\n"
"ds_write_b64 v24, v[40:41] offset:0\n"
"v_lshlrev_b32 v4, 16, v198\n"
"v_and_b32 v5, 0xffff0000, v198\n"
"v_lshlrev_b32 v6, 16, v199\n"
"v_and_b32 v7, 0xffff0000, v199\n"
"v_pk_fma_f32 v[44:45], v[4:5], v[16:17], v[44:45] op_sel:[0,0,0] op_sel_hi:[1,0,1]\n"
"v_pk_fma_f32 v[46:47], v[6:7], v[16:17], v[46:47] op_sel:[0,0,0] op_sel_hi:[1,0,1]\n"
"v_cvt_pk_bf16_f32 v44, v44, v45\n"
"v_cvt_pk_bf16_f32 v45, v46, v47\n"
"ds_write_b64 v25, v[44:45] offset:0\n"
"ds_read_b128 v[160:163], v8 offset:0\n"
"ds_read_b128 v[164:167], v8 offset:1024\n"
"v_lshlrev_b32 v4, 16, v200\n"
"v_and_b32 v5, 0xffff0000, v200\n"
"v_lshlrev_b32 v6, 16, v201\n"
"v_and_b32 v7, 0xffff0000, v201\n"
"v_pk_fma_f32 v[48:49], v[4:5], v[16:17], v[48:49] op_sel:[0,1,0] op_sel_hi:[1,1,1]\n"
"v_pk_fma_f32 v[50:51], v[6:7], v[16:17], v[50:51] op_sel:[0,1,0] op_sel_hi:[1,1,1]\n"
"v_cvt_pk_bf16_f32 v48, v48, v49\n"
"v_cvt_pk_bf16_f32 v49, v50, v51\n"
"ds_write_b64 v2, v[48:49] offset:2048\n"
"v_lshlrev_b32 v4, 16, v202\n"
"v_and_b32 v5, 0xffff0000, v202\n"
"v_lshlrev_b32 v6, 16, v203\n"
"v_and_b32 v7, 0xffff0000, v203\n"
"v_pk_fma_f32 v[52:53], v[4:5], v[16:17], v[52:53] op_sel:[0,1,0] op_sel_hi:[1,1,1]\n"
"v_pk_fma_f32 v[54:55], v[6:7], v[16:17], v[54:55] op_sel:[0,1,0] op_sel_hi:[1,1,1]\n"
"v_cvt_pk_bf16_f32 v52, v52, v53\n"
"v_cvt_pk_bf16_f32 v53, v54, v55\n"
"ds_write_b64 v3, v[52:53] offset:2048\n"
"s_waitcnt lgkmcnt(3)\n"
"buffer_store_dwordx4 v[160:163], v20, s[68:71], 0 offen offset:0 nt\n"
"s_waitcnt lgkmcnt(2)\n"
"buffer_store_dwordx4 v[164:167], v21, s[68:71], 0 offen offset:1024 nt\n"
"v_lshlrev_b32 v4, 16, v204\n"
"v_and_b32 v5, 0xffff0000, v204\n"
"v_lshlrev_b32 v6, 16, v205\n"
"v_and_b32 v7, 0xffff0000, v205\n"
"v_pk_fma_f32 v[56:57], v[4:5], v[16:17], v[56:57] op_sel:[0,1,0] op_sel_hi:[1,1,1]\n"
"v_pk_fma_f32 v[58:59], v[6:7], v[16:17], v[58:59] op_sel:[0,1,0] op_sel_hi:[1,1,1]\n"
"v_cvt_pk_bf16_f32 v56, v56, v57\n"
"v_cvt_pk_bf16_f32 v57, v58, v59\n"
"ds_write_b64 v24, v[56:57] offset:2048\n"
"v_lshlrev_b32 v4, 16, v206\n"
"v_and_b32 v5, 0xffff0000, v206\n"
"v_lshlrev_b32 v6, 16, v207\n"
"v_and_b32 v7, 0xffff0000, v207\n"
"v_pk_fma_f32 v[60:61], v[4:5], v[16:17], v[60:61] op_sel:[0,1,0] op_sel_hi:[1,1,1]\n"
"v_pk_fma_f32 v[62:63], v[6:7], v[16:17], v[62:63] op_sel:[0,1,0] op_sel_hi:[1,1,1]\n"
"v_cvt_pk_bf16_f32 v60, v60, v61\n"
"v_cvt_pk_bf16_f32 v61, v62, v63\n"
"ds_write_b64 v25, v[60:61] offset:2048\n"
"ds_read_b128 v[168:171], v8 offset:2048\n"
"ds_read_b128 v[172:175], v8 offset:3072\n"
"s_waitcnt lgkmcnt(1)\n"
"buffer_store_dwordx4 v[168:171], v22, s[68:71], 0 offen offset:2048 nt\n"
"s_waitcnt lgkmcnt(0)\n"
"buffer_store_dwordx4 v[172:175], v23, s[68:71], 0 offen offset:3072 nt\n"
  :: "s"(__builtin_amdgcn_kernarg_segment_ptr()), "s"(blockIdx.x), "s"(wid), "s"((unsigned)(size_t)smem)
  : "v0","v1","v2","v3","v4","v5","v6","v7","v8","v9","v10","v11","v12","v13","v14","v15","v16","v17","v18","v19","v20","v21","v22","v23","v24","v25","v26","v27","v28","v29","v30","v31","v32","v33","v34","v35","v36","v37","v38","v39","v40","v41","v42","v43","v44","v45","v46","v47","v48","v49","v50","v51","v52","v53","v54","v55","v56","v57","v58","v59","v60","v61","v62","v63","v64","v65","v66","v67","v68","v69","v70","v71","v72","v73","v74","v75","v76","v77","v78","v79","v80","v81","v82","v83","v84","v85","v86","v87","v88","v89","v90","v91","v92","v93","v94","v95","v96","v97","v98","v99","v100","v101","v102","v103","v104","v105","v106","v107","v108","v109","v110","v111","v112","v113","v114","v115","v116","v117","v118","v119","v120","v121","v122","v123","v124","v125","v126","v127","v128","v129","v130","v131","v132","v133","v134","v135","v136","v137","v138","v139","v140","v141","v142","v143","v144","v145","v146","v147","v148","v149","v150","v151","v152","v153","v154","v155","v156","v157","v158","v159","v160","v161","v162","v163","v164","v165","v166","v167","v168","v169","v170","v171","v172","v173","v174","v175","v176","v177","v178","v179","v180","v181","v182","v183","v184","v185","v186","v187","v188","v189","v190","v191","v192","v193","v194","v195","v196","v197","v198","v199","v200","v201","v202","v203","v204","v205","v206","v207","v208","v209","v210","v211","v212","v213","v214","v215","v216","v217","v218","v219","v220","v221","v222","v223","v224","v225","v226","v227","v228","v229","v230","v231","v232","v233","v234","v235","v236","v237","v238","v239","v240","v241","v242","v243","v244","v245","v246","v247","v248","v249","v250","v251","v252","v253","v254","v255","a0","a1","a2","a3","a4","a5","a6","a7","a8","a9","a10","a11","a12","a13","a14","a15","a16","a17","a18","a19","a20","a21","a22","a23","a24","a25","a26","a27","a28","a29","a30","a31","a32","a33","a34","a35","a36","a37","a38","a39","a40","a41","a42","a43","a44","a45","a46","a47","a48","a49","a50","a51","a52","a53","a54","a55","a56","a57","a58","a59","a60","a61","a62","a63","a64","a65","a66","a67","a68","a69","a70","a71","a72","a73","a74","a75","a76","a77","a78","a79","a80","a81","a82","a83","a84","a85","a86","a87","a88","a89","a90","a91","a92","a93","a94","a95","a96","a97","a98","a99","a100","a101","a102","a103","a104","a105","a106","a107","a108","a109","a110","a111","a112","a113","a114","a115","a116","a117","a118","a119","a120","a121","a122","a123","a124","a125","a126","a127","a128","a129","a130","a131","a132","a133","a134","a135","a136","a137","a138","a139","a140","a141","a142","a143","a144","a145","a146","a147","a148","a149","a150","a151","a152","a153","a154","a155","a156","a157","a158","a159","a160","a161","a162","a163","a164","a165","a166","a167","a168","a169","a170","a171","a172","a173","a174","a175","a176","a177","a178","a179","a180","a181","a182","a183","a184","a185","a186","a187","a188","a189","a190","a191","a192","a193","a194","a195","a196","a197","a198","a199","a200","a201","a202","a203","a204","a205","a206","a207","a208","a209","a210","a211","a212","a213","a214","a215","a216","a217","a218","a219","a220","a221","a222","a223","a224","a225","a226","a227","a228","a229","a230","a231","a232","a233","a234","a235","a236","a237","a238","a239","a240","a241","a242","a243","a244","a245","a246","a247","a248","a249","a250","a251","a252","a253","a254","a255","s8","s9","s10","s11","s12","s13","s14","s15","s16","s17","s18","s19","s20","s21","s22","s23","s24","s25","s26","s27","s28","s29","s30","s31","s33","s34","s35","s36","s37","s38","s39","s40","s41","s42","s43","s44","s45","s46","s47","s48","s49","s50","s51","s52","s53","s54","s55","s56","s57","s58","s59","s60","s61","s62","s63","s64","s65","s66","s67","s68","s69","s70","s71","s72","s73","s74","s75","s76","s77","s78","s79","s80","s81","s82","s83","s84","s85","s86","s87","s88","s89","s90","s91","s92","s93","s94","s95","s96","s97","s98","s99","vcc","scc","memory");
}
extern "C" void kernel_launch(void* const* d_in, const int* in_sizes, int n_in, void* d_out, int out_size, void* d_ws, size_t ws_size, hipStream_t stream) {
  Params p;
  p.in[0] = d_in[0]; p.in[1] = d_in[1]; p.in[2] = d_in[2]; p.in[3] = d_in[3]; p.in[4] = d_in[4]; p.in[5] = d_in[5];
  p.in[6] = d_in[7]; p.in[7] = d_in[8]; p.in[8] = d_in[9]; p.in[9] = d_in[10]; p.out = d_out; p.ws = d_ws;
  fused_kernel<<<256, 256, 0, stream>>>(p);
}
```
